```python
import torch

def _softmax(x, axis=-1):
    return torch.softmax(x, dim=axis)

def _gelu(x):
    return torch.nn.functional.gelu(x, approximate='tanh')

def _silu(x):
    return torch.nn.functional.silu(x)

def _sigmoid(x):
    return torch.sigmoid(x)

def _layernorm(x, axis=-1, eps=1e-05):
    m = x.mean(dim=axis, keepdim=True)
    v = x.var(dim=axis, keepdim=True, unbiased=False)
    return (x - m) / torch.sqrt(v + eps)

def _scatter_add(tgt, idx, src, axis=0):
    return tgt.clone().index_add_(axis, idx, src)

def _segment_sum(src, seg, n):
    out = torch.zeros((n,) + src.shape[1:], dtype=src.dtype)
    return out.index_add_(0, seg, src)

def _pad(x, pw):
    flat = tuple((v for pair in reversed(pw) for v in pair))
    return torch.nn.functional.pad(x, flat)
kernel_name = 'adaLN_cond__memory_aug_recurrent_mem_tr__swiglu_ffn__prompt'

def setup_inputs():

    _w = lambda a: (lambda t: t.to(torch.bfloat16) if t.is_floating_point() else t)(torch.as_tensor(a))

    def _A_setup():
        B, S, Dm = (1, 2048, 512)
        H, Dh = (32, 128)
        M, seg = (8, 32)
        seg = min(seg, S)
        S = S // seg * seg
        return {'x': _w((lambda *s: torch.randn(*s, dtype=torch.bfloat16) * 0.05)(B, S, Dm)), 'mem0': _w((lambda *s: torch.randn(*s, dtype=torch.bfloat16) * 0.4)(B, M, Dm)), 'Wq': _w((lambda *s: torch.randn(*s, dtype=torch.bfloat16) * 3.2)(Dm, H, Dh) * Dm ** (-0.5)), 'Wk': _w((lambda *s: torch.randn(*s, dtype=torch.bfloat16) * 3.2)(Dm, H, Dh) * Dm ** (-0.5)), 'Wv': _w((lambda *s: torch.randn(*s, dtype=torch.bfloat16) * 0.4)(Dm, H, Dh) * Dm ** (-0.5)), 'Wo': _w((lambda *s: torch.randn(*s, dtype=torch.bfloat16) * 0.4)(H, Dh, Dm) * (H * Dh) ** (-0.5)), 'seg_len': seg}
    _A_in = _A_setup()

    def _B_setup():
        B, S, D, F = (1, 2048, 512, 1792)
        return {'x': _w((lambda *s: torch.randn(*s, dtype=torch.bfloat16) * 0.05)(B, S, D)), 'w_gate': _w((lambda *s: torch.randn(*s, dtype=torch.bfloat16) * 0.05)(D, F)), 'w_up': _w((lambda *s: torch.randn(*s, dtype=torch.bfloat16) * 0.05)(D, F)), 'w_down': _w((lambda *s: torch.randn(*s, dtype=torch.bfloat16) * 0.05)(F, D))}
    _B_in = _B_setup()
    return {'A_x': _A_in['x'], 'A_mem0': _A_in['mem0'], 'A_Wq': _A_in['Wq'], 'A_Wk': _A_in['Wk'], 'A_Wv': _A_in['Wv'], 'A_Wo': _A_in['Wo'], 'A_seg_len': _A_in['seg_len'], 'B_x': _B_in['x'], 'B_w_gate': _B_in['w_gate'], 'B_w_up': _B_in['w_up'], 'B_w_down': _B_in['w_down'], 'c': _w((lambda *s: torch.randn(*s, dtype=torch.bfloat16) * 0.1)(1, 512) * 8.0), 'w_mod': _w((lambda *s: torch.randn(*s, dtype=torch.bfloat16) * 0.1)(512, 6 * 512) * 512 ** (-0.5) * 8.0)}

def reference(A_x, A_mem0, A_Wq, A_Wk, A_Wv, A_Wo, A_seg_len, B_x, B_w_gate, B_w_up, B_w_down, c, w_mod):

    def _A_ref(x, mem0, Wq, Wk, Wv, Wo, seg_len):
        B, S, Dm = x.shape
        H, Dh = (Wq.shape[1], Wq.shape[2])
        M = mem0.shape[1]
        scale = Dh ** (-0.5)
        n_seg = S // seg_len
        L = M + seg_len
        idx = torch.arange(L)
        mask = (idx[:, None] < M) | (idx[:, None] >= idx[None, :])
        mem = mem0
        outs = []
        for s in range(n_seg):
            xs = x[:, s * seg_len:(s + 1) * seg_len, :].clone()
            h = torch.concatenate([mem, xs], axis=1)
            q = torch.einsum('btd,dhe->bhte', h, Wq)
            k = torch.einsum('btd,dhe->bhte', h, Wk)
            v = torch.einsum('btd,dhe->bhte', h, Wv)
            scores = torch.einsum('bhqe,bhke->bhqk', q, k) * scale
            scores = torch.where(mask[None, None], scores, -1000000000.0)
            p = _softmax(scores, axis=-1)
            o = torch.einsum('bhqk,bhke->bhqe', p, v)
            o = torch.einsum('bhte,hed->btd', o, Wo)
            mem = o[:, :M, :].clone()
            outs.append(o[:, M:, :])
        return torch.concatenate(outs, axis=1)

    def _B_ref(x, w_gate, w_up, w_down):
        g = _silu(x @ w_gate)
        u = x @ w_up
        return g * u @ w_down
    x = A_x
    mod = (_silu(c) @ w_mod).reshape(c.shape[0], 6, 1, -1)
    sa, ca, ga, sb, cb, gb = (mod[:, 0].clone(), mod[:, 1].clone(), mod[:, 2].clone(), mod[:, 3].clone(), mod[:, 4].clone(), mod[:, 5].clone())
    h = _layernorm(x, axis=-1) * (1 + ca) + sa
    x = x + ga * _A_ref(h, A_mem0, A_Wq, A_Wk, A_Wv, A_Wo, A_seg_len)
    h = _layernorm(x, axis=-1) * (1 + cb) + sb
    x = x + gb * _B_ref(h, B_w_gate, B_w_up, B_w_down)
    return x
```

```cpp
#include <hip/hip_runtime.h>
#include <hip/hip_bf16.h>
#include <stdint.h>

typedef unsigned short u16;
__device__ __forceinline__ float bf2f(u16 x) { return __uint_as_float(((unsigned)x) << 16); }
__device__ __forceinline__ u16 f2bf(float f) { __hip_bfloat16 b = __float2bfloat16(f); return *(u16*)&b; }

constexpr int S = 2048, D = 512, H = 32, DH = 128, M = 8, SEG = 32, NSEG = 64, F = 1792, HD = 4096;

__global__ void k_mod(const u16* c, const u16* wmod, float* mod) {
    int n = blockIdx.x * blockDim.x + threadIdx.x;
    float acc = 0.f;
    for (int k = 0; k < D; ++k) {
        float cv = bf2f(c[k]);
        float s = cv / (1.f + __expf(-cv));
        s = bf2f(f2bf(s));
        acc += s * bf2f(wmod[k * 3072 + n]);
    }
    mod[n] = bf2f(f2bf(acc));
}

template <bool XF32>
__global__ void k_ln(const void* xin, const float* sh, const float* sc, u16* h) {
    int row = blockIdx.x; int t = threadIdx.x;
    float v[8]; float sum = 0.f;
    for (int i = 0; i < 8; ++i) {
        int c = t * 8 + i;
        v[i] = XF32 ? ((const float*)xin)[row * D + c] : bf2f(((const u16*)xin)[row * D + c]);
        sum += v[i];
    }
    for (int o = 32; o >= 1; o >>= 1) sum += __shfl_xor(sum, o);
    float mean = sum / D; float var = 0.f;
    for (int i = 0; i < 8; ++i) { float d = v[i] - mean; var += d * d; }
    for (int o = 32; o >= 1; o >>= 1) var += __shfl_xor(var, o);
    float rstd = rsqrtf(var / D + 1e-5f);
    for (int i = 0; i < 8; ++i) { int c = t * 8 + i; h[row * D + c] = f2bf((v[i] - mean) * rstd * (1.f + sc[c]) + sh[c]); }
}

__global__ void k_gemm(const u16* A, const u16* B, float* C, int Mr, int N, int K) {
    __shared__ float As[16][65], Bs[16][65];
    int bm = blockIdx.y * 64, bn = blockIdx.x * 64; int tx = threadIdx.x & 15, ty = threadIdx.x >> 4;
    float acc[4][4] = {};
    for (int k0 = 0; k0 < K; k0 += 16) {
        for (int i = threadIdx.x; i < 1024; i += 256) {
            int r = i >> 4, k = i & 15; As[k][r] = (bm + r < Mr) ? bf2f(A[(size_t)(bm + r) * K + k0 + k]) : 0.f;
            int kk = i >> 6, n = i & 63; Bs[kk][n] = bf2f(B[(size_t)(k0 + kk) * N + bn + n]);
        }
        __syncthreads();
        for (int k = 0; k < 16; ++k) {
            float a[4], b[4];
            for (int i = 0; i < 4; ++i) { a[i] = As[k][ty * 4 + i]; b[i] = Bs[k][tx * 4 + i]; }
            for (int i = 0; i < 4; ++i) for (int j = 0; j < 4; ++j) acc[i][j] += a[i] * b[j];
        }
        __syncthreads();
    }
    for (int i = 0; i < 4; ++i) for (int j = 0; j < 4; ++j) if (bm + ty * 4 + i < Mr) C[(size_t)(bm + ty * 4 + i) * N + bn + tx * 4 + j] = acc[i][j];
}
__global__ void k_cvt(const float* in, u16* out, int n) { int i = blockIdx.x * blockDim.x + threadIdx.x; if (i < n) out[i] = f2bf(in[i]); }

__global__ void k_attn(const u16* Qx, const u16* Kx, const u16* Vx, const u16* Qm, const u16* Km, const u16* Vm,
                       u16* Ox, u16* Om, int seg0, int do_mem, int do_x) {
    int h = blockIdx.x, s = seg0 + blockIdx.y, t = threadIdx.x;
    if (t >= 40) return;
    if (t < 8 && !do_mem) return;
    if (t >= 8 && !do_x) return;
    const u16* q = t < 8 ? Qm + (size_t)(s * 8 + t) * HD + h * DH : Qx + (size_t)(s * 32 + t - 8) * HD + h * DH;
    float sc[40]; float mx = -1e30f;
    for (int j = 0; j < 40; ++j) {
        const u16* k = j < 8 ? Km + (size_t)(s * 8 + j) * HD + h * DH : Kx + (size_t)(s * 32 + j - 8) * HD + h * DH;
        float a = 0.f; for (int e = 0; e < DH; ++e) a += bf2f(q[e]) * bf2f(k[e]);
        a *= 0.08838834764831845f;
        bool ok = (t < 8) || (t >= j);
        sc[j] = ok ? a : -1e9f; mx = fmaxf(mx, sc[j]);
    }
    float sum = 0.f; for (int j = 0; j < 40; ++j) { sc[j] = __expf(sc[j] - mx); sum += sc[j]; }
    u16* o = t < 8 ? Om + (size_t)t * HD + h * DH : Ox + (size_t)(s * 32 + t - 8) * HD + h * DH;
    for (int e = 0; e < DH; ++e) {
        float a = 0.f;
        for (int j = 0; j < 40; ++j) {
            const u16* v = j < 8 ? Vm + (size_t)(s * 8 + j) * HD + h * DH : Vx + (size_t)(s * 32 + j - 8) * HD + h * DH;
            a += bf2f(f2bf(sc[j] / sum)) * bf2f(v[e]);
        }
        o[e] = f2bf(a);
    }
}
__global__ void k_small(const u16* A, const u16* B, u16* out, int N, int K) {
    int n = blockIdx.x * blockDim.x + threadIdx.x; float acc[8] = {};
    for (int k = 0; k < K; ++k) { float b = bf2f(B[(size_t)k * N + n]); for (int i = 0; i < 8; ++i) acc[i] += bf2f(A[i * K + k]) * b; }
    for (int i = 0; i < 8; ++i) out[i * N + n] = f2bf(acc[i]);
}
__global__ void k_res1(const u16* x, const float* a, const float* g, float* x1) { int i = blockIdx.x * blockDim.x + threadIdx.x; int c = i % D; x1[i] = bf2f(f2bf(bf2f(x[i]) + bf2f(f2bf(g[c] * bf2f(f2bf(a[i]))))));}
__global__ void k_glu(const float* g, const float* u, u16* o, int n) { int i = blockIdx.x * blockDim.x + threadIdx.x; float gv = g[i]; o[i] = f2bf(gv / (1.f + __expf(-gv)) * u[i]); }
__global__ void k_res2(const float* x1, const float* a, const float* g, u16* out) { int i = blockIdx.x * blockDim.x + threadIdx.x; int c = i % D; out[i] = f2bf(x1[i] + g[c] * a[i]); }

extern "C" void kernel_launch(void* const* d_in, const int* in_sizes, int n_in, void* d_out, int out_size, void* d_ws, size_t ws_size, hipStream_t stream) {
    const u16* x = (const u16*)d_in[0]; const u16* mem0 = (const u16*)d_in[1];
    const u16* Wq = (const u16*)d_in[2]; const u16* Wk = (const u16*)d_in[3]; const u16* Wv = (const u16*)d_in[4]; const u16* Wo = (const u16*)d_in[5];
    const u16* wg = (const u16*)d_in[8]; const u16* wu = (const u16*)d_in[9]; const u16* wd = (const u16*)d_in[10];
    const u16* c = (const u16*)d_in[11]; const u16* wmod = (const u16*)d_in[12];
    char* ws = (char*)d_ws; size_t off = 0;
    auto alloc = [&](size_t b) { void* p = ws + off; off += (b + 255) / 256 * 256; return p; };
    float* mod = (float*)alloc(3072 * 4);
    u16* h1 = (u16*)alloc(S * D * 2);
    float* tmp = (float*)alloc((size_t)S * HD * 4);
    float* tmp2 = (float*)alloc((size_t)S * F * 4);
    u16* Qx = (u16*)alloc((size_t)S * HD * 2); u16* Kx = (u16*)alloc((size_t)S * HD * 2); u16* Vx = (u16*)alloc((size_t)S * HD * 2); u16* Ox = (u16*)alloc((size_t)S * HD * 2);
    u16* Qm = (u16*)alloc((size_t)NSEG * 8 * HD * 2); u16* Km = (u16*)alloc((size_t)NSEG * 8 * HD * 2); u16* Vm = (u16*)alloc((size_t)NSEG * 8 * HD * 2);
    u16* Om = (u16*)alloc(8 * HD * 2); u16* mem = (u16*)alloc(8 * D * 2);
    float* x1 = (float*)alloc(S * D * 4); u16* h2 = (u16*)alloc(S * D * 2); u16* gl = (u16*)alloc((size_t)S * F * 2);
    k_mod<<<12, 256, 0, stream>>>(c, wmod, mod);
    k_ln<false><<<S, 64, 0, stream>>>(x, mod + 0, mod + 512, h1);
    const u16* Ws[3] = {Wq, Wk, Wv}; u16* Os[3] = {Qx, Kx, Vx};
    for (int i = 0; i < 3; ++i) { k_gemm<<<dim3(HD / 64, S / 64), 256, 0, stream>>>(h1, Ws[i], tmp, S, HD, D); k_cvt<<<S * HD / 256, 256, 0, stream>>>(tmp, Os[i], S * HD); }
    for (int s = 0; s < NSEG; ++s) {
        const u16* m = s == 0 ? mem0 : mem;
        k_small<<<HD / 64, 64, 0, stream>>>(m, Wq, Qm + (size_t)s * 8 * HD, HD, D);
        k_small<<<HD / 64, 64, 0, stream>>>(m, Wk, Km + (size_t)s * 8 * HD, HD, D);
        k_small<<<HD / 64, 64, 0, stream>>>(m, Wv, Vm + (size_t)s * 8 * HD, HD, D);
        k_attn<<<dim3(H, 1), 64, 0, stream>>>(Qx, Kx, Vx, Qm, Km, Vm, Ox, Om, s, 1, 0);
        k_small<<<D / 64, 64, 0, stream>>>(Om, Wo, mem, D, HD);
    }
    k_attn<<<dim3(H, NSEG), 64, 0, stream>>>(Qx, Kx, Vx, Qm, Km, Vm, Ox, Om, 0, 0, 1);
    k_gemm<<<dim3(D / 64, S / 64), 256, 0, stream>>>(Ox, Wo, tmp, S, D, HD);
    k_res1<<<S * D / 256, 256, 0, stream>>>(x, tmp, mod + 1024, x1);
    k_ln<true><<<S, 64, 0, stream>>>(x1, mod + 1536, mod + 2048, h2);
    k_gemm<<<dim3(F / 64, S / 64), 256, 0, stream>>>(h2, wg, tmp, S, F, D);
    k_gemm<<<dim3(F / 64, S / 64), 256, 0, stream>>>(h2, wu, tmp2, S, F, D);
    k_glu<<<S * F / 256, 256, 0, stream>>>(tmp, tmp2, gl, S * F);
    k_gemm<<<dim3(D / 64, S / 64), 256, 0, stream>>>(gl, wd, tmp, S, D, F);
    k_res2<<<S * D / 256, 256, 0, stream>>>(x1, tmp, mod + 2560, (u16*)d_out);
}
```

```cpp
#include <hip/hip_runtime.h>
#include <hip/hip_bf16.h>
#include <stdint.h>

typedef unsigned short u16;
__device__ __forceinline__ float bf2f(u16 x) { return __uint_as_float(((unsigned)x) << 16); }
__device__ __forceinline__ u16 f2bf(float f) { __hip_bfloat16 b = __float2bfloat16(f); return *(u16*)&b; }

__device__ __forceinline__ float rb(float f) { return bf2f(f2bf(f)); }
constexpr int S = 2048, D = 512, H = 32, DH = 128, M = 8, SEG = 32, NSEG = 64, F = 1792, HD = 4096;

__global__ void k_mod(const u16* c, const u16* wmod, float* mod) {
    int n = blockIdx.x * blockDim.x + threadIdx.x;
    float acc = 0.f;
    for (int k = 0; k < D; ++k) {
        float cv = bf2f(c[k]);
        float s = cv / (1.f + __expf(-cv));
        s = bf2f(f2bf(s));
        acc += s * bf2f(wmod[k * 3072 + n]);
    }
    mod[n] = bf2f(f2bf(acc));
}

template <bool XF32>
__global__ void k_ln(const void* xin, const float* sh, const float* sc, u16* h) {
    int row = blockIdx.x; int t = threadIdx.x;
    float v[8]; float sum = 0.f;
    for (int i = 0; i < 8; ++i) {
        int c = t * 8 + i;
        v[i] = XF32 ? ((const float*)xin)[row * D + c] : bf2f(((const u16*)xin)[row * D + c]);
        sum += v[i];
    }
    for (int o = 32; o >= 1; o >>= 1) sum += __shfl_xor(sum, o);
    float mean = sum / D; float var = 0.f;
    for (int i = 0; i < 8; ++i) { float d = v[i] - mean; var += d * d; }
    for (int o = 32; o >= 1; o >>= 1) var += __shfl_xor(var, o);
    float mb = rb(mean); float vb = rb(var / D); float sd = rb(sqrtf(rb(vb + 1e-5f)));
    for (int i = 0; i < 8; ++i) { int c = t * 8 + i; float n = rb(rb(v[i] - mb) / sd); h[row * D + c] = f2bf(rb(n * rb(1.f + sc[c])) + sh[c]); }
}

__global__ void k_gemm(const u16* A, const u16* B, float* C, int Mr, int N, int K) {
    __shared__ float As[16][65], Bs[16][65];
    int bm = blockIdx.y * 64, bn = blockIdx.x * 64; int tx = threadIdx.x & 15, ty = threadIdx.x >> 4;
    float acc[4][4] = {};
    for (int k0 = 0; k0 < K; k0 += 16) {
        for (int i = threadIdx.x; i < 1024; i += 256) {
            int r = i >> 4, k = i & 15; As[k][r] = (bm + r < Mr) ? bf2f(A[(size_t)(bm + r) * K + k0 + k]) : 0.f;
            int kk = i >> 6, n = i & 63; Bs[kk][n] = bf2f(B[(size_t)(k0 + kk) * N + bn + n]);
        }
        __syncthreads();
        for (int k = 0; k < 16; ++k) {
            float a[4], b[4];
            for (int i = 0; i < 4; ++i) { a[i] = As[k][ty * 4 + i]; b[i] = Bs[k][tx * 4 + i]; }
            for (int i = 0; i < 4; ++i) for (int j = 0; j < 4; ++j) acc[i][j] += a[i] * b[j];
        }
        __syncthreads();
    }
    for (int i = 0; i < 4; ++i) for (int j = 0; j < 4; ++j) if (bm + ty * 4 + i < Mr) C[(size_t)(bm + ty * 4 + i) * N + bn + tx * 4 + j] = acc[i][j];
}
__global__ void k_cvt(const float* in, u16* out, int n) { int i = blockIdx.x * blockDim.x + threadIdx.x; if (i < n) out[i] = f2bf(in[i]); }

__global__ void k_attn(const u16* Qx, const u16* Kx, const u16* Vx, const u16* Qm, const u16* Km, const u16* Vm,
                       u16* Ox, u16* Om, int seg0, int do_mem, int do_x) {
    int h = blockIdx.x, s = seg0 + blockIdx.y, t = threadIdx.x;
    if (t >= 40) return;
    if (t < 8 && !do_mem) return;
    if (t >= 8 && !do_x) return;
    const u16* q = t < 8 ? Qm + (size_t)(s * 8 + t) * HD + h * DH : Qx + (size_t)(s * 32 + t - 8) * HD + h * DH;
    float sc[40]; float mx = -1e30f;
    for (int j = 0; j < 40; ++j) {
        const u16* k = j < 8 ? Km + (size_t)(s * 8 + j) * HD + h * DH : Kx + (size_t)(s * 32 + j - 8) * HD + h * DH;
        float a = 0.f; for (int e = 0; e < DH; ++e) a += bf2f(q[e]) * bf2f(k[e]);
        a = rb(rb(a) * 0.08838834764831845f);
        bool ok = (t < 8) || (t >= j);
        sc[j] = ok ? a : -1e9f; mx = fmaxf(mx, sc[j]);
    }
    float sum = 0.f; for (int j = 0; j < 40; ++j) { sc[j] = __expf(sc[j] - mx); sum += sc[j]; }
    u16* o = t < 8 ? Om + (size_t)t * HD + h * DH : Ox + (size_t)(s * 32 + t - 8) * HD + h * DH;
    for (int e = 0; e < DH; ++e) {
        float a = 0.f;
        for (int j = 0; j < 40; ++j) {
            const u16* v = j < 8 ? Vm + (size_t)(s * 8 + j) * HD + h * DH : Vx + (size_t)(s * 32 + j - 8) * HD + h * DH;
            a += bf2f(f2bf(sc[j] / sum)) * bf2f(v[e]);
        }
        o[e] = f2bf(a);
    }
}
__global__ void k_small(const u16* A, const u16* B, u16* out, int N, int K) {
    int n = blockIdx.x * blockDim.x + threadIdx.x; float acc[8] = {};
    for (int k = 0; k < K; ++k) { float b = bf2f(B[(size_t)k * N + n]); for (int i = 0; i < 8; ++i) acc[i] += bf2f(A[i * K + k]) * b; }
    for (int i = 0; i < 8; ++i) out[i * N + n] = f2bf(acc[i]);
}
__global__ void k_res1(const u16* x, const float* a, const float* g, float* x1) { int i = blockIdx.x * blockDim.x + threadIdx.x; int c = i % D; x1[i] = bf2f(f2bf(bf2f(x[i]) + bf2f(f2bf(g[c] * bf2f(f2bf(a[i]))))));}
__global__ void k_glu(const float* g, const float* u, u16* o, int n) { int i = blockIdx.x * blockDim.x + threadIdx.x; float gv = rb(g[i]); o[i] = f2bf(rb(gv / (1.f + __expf(-gv))) * rb(u[i])); }
__global__ void k_res2(const float* x1, const float* a, const float* g, u16* out) { int i = blockIdx.x * blockDim.x + threadIdx.x; int c = i % D; out[i] = f2bf(x1[i] + rb(g[c] * rb(a[i]))); }

extern "C" void kernel_launch(void* const* d_in, const int* in_sizes, int n_in, void* d_out, int out_size, void* d_ws, size_t ws_size, hipStream_t stream) {
    const u16* x = (const u16*)d_in[0]; const u16* mem0 = (const u16*)d_in[1];
    const u16* Wq = (const u16*)d_in[2]; const u16* Wk = (const u16*)d_in[3]; const u16* Wv = (const u16*)d_in[4]; const u16* Wo = (const u16*)d_in[5];
    const u16* wg = (const u16*)d_in[8]; const u16* wu = (const u16*)d_in[9]; const u16* wd = (const u16*)d_in[10];
    const u16* c = (const u16*)d_in[11]; const u16* wmod = (const u16*)d_in[12];
    char* ws = (char*)d_ws; size_t off = 0;
    auto alloc = [&](size_t b) { void* p = ws + off; off += (b + 255) / 256 * 256; return p; };
    float* mod = (float*)alloc(3072 * 4);
    u16* h1 = (u16*)alloc(S * D * 2);
    float* tmp = (float*)alloc((size_t)S * HD * 4);
    float* tmp2 = (float*)alloc((size_t)S * F * 4);
    u16* Qx = (u16*)alloc((size_t)S * HD * 2); u16* Kx = (u16*)alloc((size_t)S * HD * 2); u16* Vx = (u16*)alloc((size_t)S * HD * 2); u16* Ox = (u16*)alloc((size_t)S * HD * 2);
    u16* Qm = (u16*)alloc((size_t)NSEG * 8 * HD * 2); u16* Km = (u16*)alloc((size_t)NSEG * 8 * HD * 2); u16* Vm = (u16*)alloc((size_t)NSEG * 8 * HD * 2);
    u16* Om = (u16*)alloc(8 * HD * 2); u16* mem = (u16*)alloc(8 * D * 2);
    float* x1 = (float*)alloc(S * D * 4); u16* h2 = (u16*)alloc(S * D * 2); u16* gl = (u16*)alloc((size_t)S * F * 2);
    k_mod<<<12, 256, 0, stream>>>(c, wmod, mod);
    k_ln<false><<<S, 64, 0, stream>>>(x, mod + 0, mod + 512, h1);
    const u16* Ws[3] = {Wq, Wk, Wv}; u16* Os[3] = {Qx, Kx, Vx};
    for (int i = 0; i < 3; ++i) { k_gemm<<<dim3(HD / 64, S / 64), 256, 0, stream>>>(h1, Ws[i], tmp, S, HD, D); k_cvt<<<S * HD / 256, 256, 0, stream>>>(tmp, Os[i], S * HD); }
    for (int s = 0; s < NSEG; ++s) {
        const u16* m = s == 0 ? mem0 : mem;
        k_small<<<HD / 64, 64, 0, stream>>>(m, Wq, Qm + (size_t)s * 8 * HD, HD, D);
        k_small<<<HD / 64, 64, 0, stream>>>(m, Wk, Km + (size_t)s * 8 * HD, HD, D);
        k_small<<<HD / 64, 64, 0, stream>>>(m, Wv, Vm + (size_t)s * 8 * HD, HD, D);
        k_attn<<<dim3(H, 1), 64, 0, stream>>>(Qx, Kx, Vx, Qm, Km, Vm, Ox, Om, s, 1, 0);
        k_small<<<D / 64, 64, 0, stream>>>(Om, Wo, mem, D, HD);
    }
    k_attn<<<dim3(H, NSEG), 64, 0, stream>>>(Qx, Kx, Vx, Qm, Km, Vm, Ox, Om, 0, 0, 1);
    k_gemm<<<dim3(D / 64, S / 64), 256, 0, stream>>>(Ox, Wo, tmp, S, D, HD);
    k_res1<<<S * D / 256, 256, 0, stream>>>(x, tmp, mod + 1024, x1);
    k_ln<true><<<S, 64, 0, stream>>>(x1, mod + 1536, mod + 2048, h2);
    k_gemm<<<dim3(F / 64, S / 64), 256, 0, stream>>>(h2, wg, tmp, S, F, D);
    k_gemm<<<dim3(F / 64, S / 64), 256, 0, stream>>>(h2, wu, tmp2, S, F, D);
    k_glu<<<S * F / 256, 256, 0, stream>>>(tmp, tmp2, gl, S * F);
    k_gemm<<<dim3(D / 64, S / 64), 256, 0, stream>>>(gl, wd, tmp, S, D, F);
    k_res2<<<S * D / 256, 256, 0, stream>>>(x1, tmp, mod + 2560, (u16*)d_out);
}
```

```cpp
#include <hip/hip_runtime.h>
#include <hip/hip_bf16.h>
#include <stdint.h>

typedef unsigned short u16;
typedef __bf16 bf16x2 __attribute__((ext_vector_type(2)));
typedef __bf16 bf16x8 __attribute__((ext_vector_type(8)));
typedef short s16x4 __attribute__((ext_vector_type(4)));
typedef float f32x4 __attribute__((ext_vector_type(4)));
typedef float f32x2 __attribute__((ext_vector_type(2)));
typedef unsigned u32x2 __attribute__((ext_vector_type(2)));
typedef unsigned u32x4 __attribute__((ext_vector_type(4)));
typedef unsigned u32x8 __attribute__((ext_vector_type(8)));
#define LAS __attribute__((address_space(3)))
typedef LAS char* lptr;
#define DI __device__ __forceinline__

#define TX 185
#define TY 84
#define TV1 22
#define TV2 13
#define TPEN 120
#define NPRE 4
#define LEAD 8

constexpr int S = 2048, D = 512, NSEG = 64, F = 1792, HD = 4096;
constexpr unsigned SENT = 0xFFFFFFFFu;
constexpr int NRING = 3;
constexpr unsigned WS_MOD = 0;
constexpr unsigned WS_FLAG = 16384;
constexpr unsigned FL_LN = 0, FL_QKV = 128, FL_KM = 320, FL_B = 384, FL_L = 448, FL_C = 464, FL_A = 520, FL_N = 536;
constexpr unsigned WS_SENT = 131072;
constexpr unsigned WS_PART = WS_SENT;
constexpr unsigned WS_MEMF = 122u << 20;
constexpr unsigned MSLOT = 8320, MCOPY = 64 * MSLOT;
constexpr unsigned WS_SENT_END = WS_PART + NRING * 524288;
constexpr unsigned WS_DBG = 3u << 20;
constexpr unsigned WS_H1 = 4u << 20, WS_H2 = 6u << 20, WS_X1 = 8u << 20, WS_G = 10u << 20, WS_O = 18u << 20, WS_QKV = 34u << 20, WS_PB = 82u << 20, WS_KVM = 114u << 20, WS_END = 132u << 20;

DI float bflo(unsigned x) { return __uint_as_float(x << 16); }
DI float bfhi(unsigned x) { return __uint_as_float(x & 0xffff0000u); }
DI unsigned pk(float lo, float hi) { bf16x2 v = {(__bf16)lo, (__bf16)hi}; return __builtin_bit_cast(unsigned, v); }
DI float rb(float x) { return (float)(__bf16)x; }
DI void bar() { asm volatile("s_waitcnt lgkmcnt(0)\n\ts_barrier" ::: "memory"); }
DI void drain() { asm volatile("s_waitcnt vmcnt(0)" ::: "memory"); }
DI u32x2 trread(lptr p) { return __builtin_bit_cast(u32x2, __builtin_amdgcn_ds_read_tr16_b64_v4i16((LAS s16x4*)p)); }
DI u32x4 tr2(lptr p, int off) { const u32x2 a = trread(p), b = trread(p + off); return (u32x4){a.x, a.y, b.x, b.y}; }
DI u32x4 ldsr128(lptr p) { return *(LAS u32x4*)p; }
DI void ldsw128(lptr p, u32x4 v) { *(LAS u32x4*)p = v; }
DI void ldsw64(lptr p, u32x2 v) { *(LAS u32x2*)p = v; }
DI void ldsw32(lptr p, unsigned v) { *(LAS unsigned*)p = v; }
DI f32x4 mfma32(u32x4 a, u32x4 b, f32x4 c) { return __builtin_amdgcn_mfma_f32_16x16x32_bf16(__builtin_bit_cast(bf16x8, a), __builtin_bit_cast(bf16x8, b), c, 0, 0, 0); }
DI float swadd32(float a, float b) {
    auto r = __builtin_amdgcn_permlane32_swap(__float_as_uint(a), __float_as_uint(b), false, false);
    return __uint_as_float(r[0]) + __uint_as_float(r[1]);
}
DI float vmax(float a, float b) { float r; asm("v_max_f32 %0, %1, %2" : "=v"(r) : "v"(a), "v"(b)); return r; }
DI float vmax3(float a, float b, float c) { float r; asm("v_max3_f32 %0, %1, %2, %3" : "=v"(r) : "v"(a), "v"(b), "v"(c)); return r; }
DI float xmax(float x) {
    auto s = __builtin_amdgcn_permlane16_swap(__float_as_uint(x), __float_as_uint(x), false, false);
    x = vmax(__uint_as_float(s[0]), __uint_as_float(s[1]));
    auto t = __builtin_amdgcn_permlane32_swap(__float_as_uint(x), __float_as_uint(x), false, false);
    return vmax(__uint_as_float(t[0]), __uint_as_float(t[1]));
}
DI float xsum(float x) {
    auto s = __builtin_amdgcn_permlane16_swap(__float_as_uint(x), __float_as_uint(x), false, false);
    x = __uint_as_float(s[0]) + __uint_as_float(s[1]);
    auto t = __builtin_amdgcn_permlane32_swap(__float_as_uint(x), __float_as_uint(x), false, false);
    return __uint_as_float(t[0]) + __uint_as_float(t[1]);
}
template <int CTRL> DI float dpp(float x) { return __builtin_bit_cast(float, __builtin_amdgcn_mov_dpp(__builtin_bit_cast(int, x), CTRL, 0xf, 0xf, true)); }
template <int CTRL> DI int dppi(int x) { return __builtin_amdgcn_mov_dpp(x, CTRL, 0xf, 0xf, true); }
DI unsigned mx4(u32x4 v, unsigned m) { return max(max(max(v.x, v.y), max(v.z, v.w)), m); }
typedef __amdgpu_buffer_rsrc_t rsrc_t;
DI rsrc_t mkrs(const void* p, unsigned bytes) { return __builtin_amdgcn_make_buffer_rsrc((void*)p, 0, bytes, 0x00020000); }
DI u32x4 ld16(rsrc_t r, unsigned off, unsigned soff = 0) { return __builtin_amdgcn_raw_buffer_load_b128(r, off, soff, 16); }
DI u32x2 ld8(rsrc_t r, unsigned off) { return __builtin_amdgcn_raw_buffer_load_b64(r, off, 0, 16); }
DI unsigned ld4(rsrc_t r, unsigned off, unsigned soff = 0) { return __builtin_amdgcn_raw_buffer_load_b32(r, off, soff, 16); }
DI void st16(rsrc_t r, unsigned off, u32x4 v, unsigned soff = 0) { __builtin_amdgcn_raw_buffer_store_b128(v, r, off, soff, 16); }
DI void st8(rsrc_t r, unsigned off, u32x2 v) { __builtin_amdgcn_raw_buffer_store_b64(v, r, off, 0, 16); }
DI void st4(rsrc_t r, unsigned off, unsigned v, unsigned soff = 0) { __builtin_amdgcn_raw_buffer_store_b32(v, r, off, soff, 16); }
DI u32x4 ldw(rsrc_t r, unsigned voff, unsigned soff) { return __builtin_amdgcn_raw_buffer_load_b128(r, voff, soff, 0); }
DI unsigned rt() { return (unsigned)__builtin_amdgcn_s_memrealtime(); }
DI void wait_rt(unsigned t) { while ((int)(rt() - t) < 0) { } asm volatile("" ::: "memory"); }

template <bool V> struct bool_c { static constexpr bool value = V; };
struct Params {
    const u16 *x, *mem0, *Wq, *Wk, *Wv, *Wo, *wg, *wu, *wd, *c, *wmod;
    u16* out; char* ws;
};

__global__ void __launch_bounds__(256) k_init(Params p) {
    __shared__ float red[4][64];
    const int tid = threadIdx.x, w = tid >> 6, lane = tid & 63;
    if (blockIdx.x < 48) {
        const int r8 = lane >> 3, cc = lane & 7, n0 = blockIdx.x * 64 + cc * 8;
        u32x4 wv[16]; float s[16];
#pragma unroll
        for (int it = 0; it < 16; ++it) wv[it] = *(const u32x4*)(p.wmod + (size_t)(w * 128 + it * 8 + r8) * 3072 + n0);
#pragma unroll
        for (int it = 0; it < 16; ++it) { const float cv = bflo(p.c[w * 128 + it * 8 + r8]); s[it] = rb(cv / (1.f + __expf(-cv))); }
        float acc[8] = {};
#pragma unroll
        for (int it = 0; it < 16; ++it) {
            acc[0] += s[it] * bflo(wv[it].x); acc[1] += s[it] * bfhi(wv[it].x); acc[2] += s[it] * bflo(wv[it].y); acc[3] += s[it] * bfhi(wv[it].y);
            acc[4] += s[it] * bflo(wv[it].z); acc[5] += s[it] * bfhi(wv[it].z); acc[6] += s[it] * bflo(wv[it].w); acc[7] += s[it] * bfhi(wv[it].w);
        }
#pragma unroll
        for (int i = 0; i < 8; ++i) { acc[i] += dpp<0x128>(acc[i]); acc[i] = xsum(acc[i]); }
        if (lane < 8) for (int i = 0; i < 8; ++i) red[w][cc * 8 + i] = acc[i];
        __syncthreads();
        if (tid < 64) ((float*)(p.ws + WS_MOD))[blockIdx.x * 64 + tid] = rb((red[0][tid] + red[1][tid]) + (red[2][tid] + red[3][tid]));
    } else {
        const unsigned t = (blockIdx.x - 48) * 256 + tid, nt = (gridDim.x - 48) * 256;
        const u32x4 z = {SENT, SENT, SENT, SENT}, zero = {0, 0, 0, 0};
        for (unsigned i = t; i < FL_N * 8; i += nt) ((u32x4*)(p.ws + WS_FLAG))[i] = zero;
        for (unsigned i = t; i < (WS_SENT_END - WS_SENT) / 16; i += nt) ((u32x4*)(p.ws + WS_SENT))[i] = z;
        for (unsigned i = t; i < 8 * MCOPY / 16; i += nt) {
            u32x4 v = z;
            const unsigned m = i % (MCOPY / 16);
            if (m < 512) {
                const unsigned* mp = (const unsigned*)(p.mem0 + (m & 7) * D + 16 * (m >> 4) + 2 * ((m >> 3) & 1));
                v = (u32x4){min(mp[0], SENT - 1), min(mp[2], SENT - 1), min(mp[4], SENT - 1), min(mp[6], SENT - 1)};
            } else if (m < 520) v = zero;
            ((u32x4*)(p.ws + WS_MEMF))[i] = v;
        }
    }
}

DI float lnmod(float v, float mb, float sd, float t, float sh) { return rb(rb(rb(v - mb) / sd) * t) + sh; }

DI void wait_flags(rsrc_t rw, unsigned first, int cnt, int stride, unsigned need, int lane, unsigned& cap) {
    const unsigned off = WS_FLAG + (first + (lane < cnt ? lane : 0) * stride) * 128;
    for (unsigned spins = 0;; ++spins) {
        asm volatile("" ::: "memory");
        if (__all(ld4(rw, off) >= need)) break;
        if (spins > cap) { cap = 0; break; }
        __builtin_amdgcn_s_sleep(2);
    }
    asm volatile("" ::: "memory");
}

constexpr int PSTR = 272;
DI void panel(lptr lds, const u16* srcA, const u16* srcB, int ld, int tid, int colA, int colB, u32x4 (&wa)[16], u32x4 (&wb)[16]) {
    const int lane = tid & 63, g = lane >> 4, q = (lane & 15) >> 2;
    const rsrc_t ra = mkrs(srcA, (511 * ld + 64) * 2), rb_ = mkrs(srcB, (511 * ld + 64) * 2);
    const unsigned voff = ((tid >> 3) * ld + (tid & 7) * 8) * 2;
    const lptr wp = lds + (tid >> 3) * PSTR + (tid & 7) * 16;
#pragma unroll
    for (int half = 0; half < 2; ++half) {
        u32x4 v[16];
#pragma unroll
        for (int i = 0; i < 8; ++i) { v[i] = ldw(ra, voff, (half * 256 + i * 32) * ld * 2); v[8 + i] = ldw(rb_, voff, (half * 256 + i * 32) * ld * 2); }
        bar();
#pragma unroll
        for (int i = 0; i < 8; ++i) { ldsw128(wp + i * 32 * PSTR, v[i]); ldsw128(wp + i * 32 * PSTR + 128, v[8 + i]); }
        bar();
        lptr pa = lds + (8 * g + q) * PSTR + colA * 2, pb = lds + (8 * g + q) * PSTR + colB * 2;
#pragma unroll
        for (int ks = 0; ks < 8; ++ks) { wa[half * 8 + ks] = tr2(pa + ks * 32 * PSTR, 4 * PSTR); wb[half * 8 + ks] = tr2(pb + ks * 32 * PSTR, 4 * PSTR); }
    }
    bar();
}
constexpr int L_A = 73728;
#define MFWA(acc, wgt, x) asm volatile("v_mfma_f32_16x16x32_bf16 %0, %1, %2, %0" : "+v"(acc) : "a"(wgt), "v"(x))
#define MFWB(acc, wgt, x) asm volatile("v_mfma_f32_16x16x32_bf16 %0, %2, %1, %0" : "+v"(acc) : "a"(wgt), "v"(x))
template <bool SWAP> DI void tile_mma(lptr lds, int lane, const u32x4 (&wa)[16], const u32x4 (&wb)[16], f32x4& ca, f32x4& cb) {
    ca = (f32x4){0, 0, 0, 0}; cb = ca;
    u32x4 a[16];
#pragma unroll
    for (int ks = 0; ks < 16; ++ks) a[ks] = ldsr128(lds + L_A + ks * 1024 + lane * 16);
    asm volatile("s_nop 1" : "+v"(ca), "+v"(cb));
    __builtin_amdgcn_sched_barrier(0);
#pragma unroll
    for (int ks = 0; ks < 16; ++ks) { if (SWAP) { MFWB(ca, wa[ks], a[ks]); MFWB(cb, wb[ks], a[ks]); } else { MFWA(ca, wa[ks], a[ks]); MFWA(cb, wb[ks], a[ks]); } }
    asm volatile("s_nop 7" : "+v"(ca), "+v"(cb));
}

constexpr int CSTR = 656, OSTR = 1040;
constexpr int L_MEMF = 0, L_QT = 8192, L_KT = 12288, L_VT = 16384, L_RED = 20480, L_TS = 21504, L_SEL = 23552, L_ONES = 24576, L_QKV = 25600;
constexpr float SC_L2E = 0.08838834764831845f * 1.4426950408889634f;
constexpr unsigned OOB = 0x80000000u;
#define SMFA(acc, a, w) asm volatile("v_smfmac_f32_16x16x64_bf16 %0, %1, %2, %3" : "+v"(acc) : "v"(a), "a"(w), "v"(sidx))
#define SMFV(acc, a, w) asm volatile("v_smfmac_f32_16x16x64_bf16 %0, %1, %2, %3" : "+v"(acc) : "v"(a), "v"(w), "v"(sidx))
#define MF0(acc, a, b) asm volatile("v_mfma_f32_16x16x32_bf16 %0, %1, %2, 0" : "=&v"(acc) : "v"(a), "v"(b))
#define MF(acc, a, b) asm volatile("v_mfma_f32_16x16x32_bf16 %0, %1, %2, %0" : "+v"(acc) : "v"(a), "v"(b))
#define NOP2() asm volatile("s_nop 1")
#define SB() __builtin_amdgcn_sched_barrier(0)
DI f32x4 zero4() { f32x2 a, b; asm volatile("v_mov_b64 %0, 0\n\tv_mov_b64 %1, 0" : "=v"(a), "=v"(b)); return (f32x4){a.x, a.y, b.x, b.y}; }
DI u32x4 sent4() { u32x2 a, b; asm volatile("v_mov_b64 %0, -1\n\tv_mov_b64 %1, -1" : "=v"(a), "=v"(b)); return (u32x4){a.x, a.y, b.x, b.y}; }
DI float vadd(float a, float b) { float r; asm volatile("v_add_f32 %0, %1, %2" : "=v"(r) : "v"(a), "v"(b)); return r; }
DI unsigned vcvt(float a, float b) { unsigned r; asm volatile("v_cvt_pk_bf16_f32 %0, %1, %2" : "=v"(r) : "v"(a), "v"(b)); return r; }
template <int O0, int O1> DI void dsw2(lptr p, unsigned a, unsigned b) { asm volatile("ds_write2_b32 %0, %1, %2 offset0:%3 offset1:%4" :: "v"(p), "v"(a), "v"(b), "i"(O0), "i"(O1) : "memory"); }
#define ST(k)
#define ST2(k)
#define TS(k)
DI int wave_max_rel(unsigned v, unsigned base) {
    int d = (int)(v - base);
    d = max(d, dppi<0xB1>(d)); d = max(d, dppi<0x4E>(d)); d = max(d, dppi<0x124>(d)); d = max(d, dppi<0x128>(d));
    auto s = __builtin_amdgcn_permlane16_swap((unsigned)d, (unsigned)d, false, false); d = max((int)s[0], (int)s[1]);
    auto t = __builtin_amdgcn_permlane32_swap((unsigned)d, (unsigned)d, false, false); d = max((int)t[0], (int)t[1]);
    return __builtin_amdgcn_readfirstlane(d);
}

DI void chain_role(const Params& p, lptr lds, const int b, const int tid) {
    const int w = __builtin_amdgcn_readfirstlane(tid >> 6), lane = tid & 63, n = lane & 15, g = lane >> 4;
    const int h = (b & 7) + 8 * (b >> 4), hf = (b >> 3) & 1;
    u32x8 wq[2][8], wk[2][8], wv[8], wo[8];
    {
        const int q = n >> 2, pp = lane & 3;
        constexpr int WSTR = 208;
        u32x4 v[2][12];
        const rsrc_t rq = mkrs(p.Wq + h * 128 + 32 * w, (511 * HD + 32) * 2), rk = mkrs(p.Wk + h * 128 + 32 * w, (511 * HD + 32) * 2), rv = mkrs(p.Wv + h * 128 + hf * 64 + 32 * (w >> 1), (511 * HD + 32) * 2);
        const unsigned vo = ((lane >> 2) * HD + (lane & 3) * 8) * 2;
        const lptr img = lds + w * 64 * WSTR, wp = img + (lane >> 2) * WSTR + (lane & 3) * 16;
        auto issue = [&](int j, u32x4 (&d)[12]) {
            const unsigned so = ((2 * w + j) & 7) * 64 * HD * 2;
#pragma unroll
            for (int i = 0; i < 4; ++i) { d[i] = ldw(rq, vo, so + i * 16 * HD * 2); d[4 + i] = ldw(rk, vo, so + i * 16 * HD * 2); d[8 + i] = ldw(rv, vo, so + i * 16 * HD * 2); } };
        issue(0, v[0]); issue(1, v[1]);
#pragma unroll
        for (int ks = 0; ks < 8; ++ks) {
#pragma unroll
            for (int i = 0; i < 4; ++i) { ldsw128(wp + i * 16 * WSTR, v[ks & 1][i]); ldsw128(wp + i * 16 * WSTR + 64, v[ks & 1][4 + i]); ldsw128(wp + i * 16 * WSTR + 128, v[ks & 1][8 + i]); }
            if (ks + 2 < 8) issue(ks + 2, v[ks & 1]);
            asm volatile("s_waitcnt lgkmcnt(0)" ::: "memory");
            lptr base = img + (8 * g + q) * WSTR;
            auto frag = [&](int colbyte) { u32x8 f;
#pragma unroll
                for (int m = 0; m < 4; ++m) { const u32x2 t = trread(base + (32 * (m >> 1) + 4 * (m & 1)) * WSTR + colbyte); f[2 * m] = t.x; f[2 * m + 1] = t.y; }
                return f; };
            wq[0][ks] = frag(8 * pp); wq[1][ks] = frag(32 + 8 * pp);
            wk[0][ks] = frag(64 + 8 * pp); wk[1][ks] = frag(96 + 8 * pp);
            wv[ks] = frag(128 + 16 * pp + 8 * (w & 1));
            asm volatile("s_waitcnt lgkmcnt(0)" : "+a"(wq[0][ks]), "+a"(wq[1][ks]), "+a"(wk[0][ks]), "+a"(wk[1][ks]), "+v"(wv[ks]) :: "memory");
        }
        {
            u32x4 u[16];
            const rsrc_t ro = mkrs(p.Wo + (size_t)(h * 128 + hf * 64) * D, 65536);
#pragma unroll
            for (int i = 0; i < 16; ++i) u[i] = ldw(ro, tid * 16, i * 4096);
            bar();
#pragma unroll
            for (int i = 0; i < 16; ++i) ldsw128(lds + ((tid >> 6) + 4 * i) * OSTR + (tid & 63) * 16, u[i]);
            bar();
            lptr base = lds + (32 * (q >> 1) + 4 * g + (q & 1)) * OSTR;
#pragma unroll
            for (int t = 0; t < 8; ++t) {
                const int col = 128 * w + 64 * (t >> 2) + 16 * pp + 4 * (t & 3);
#pragma unroll
                for (int m = 0; m < 4; ++m) { const u32x2 tv = trread(base + (16 * (m >> 1) + 2 * (m & 1)) * OSTR + col * 2); wo[t][2 * m] = tv.x; wo[t][2 * m + 1] = tv.y; }
            }
            bar();
        }
    }
    const rsrc_t rw = mkrs(p.ws, WS_END);
    const int hi = (n >> 1) & 1, rowi = 2 * (n >> 2) + (n & 1);
    const int sidx = hi ? 0xEEEE : 0x4444;
    unsigned cap = 1u << 17;
    const unsigned l16 = lane * 16;
    const unsigned mcp = WS_MEMF + (__builtin_amdgcn_s_getreg(20 | (3 << 11)) & 7) * MCOPY;
    const unsigned mvo = (2 * g + hi) * 128 + rowi * 16;
    const unsigned t0o = tid == 0 ? 0u : OOB;
    auto qkv_issue = [&](int s, u32x4 (&v)[3]) {
        const unsigned base = WS_QKV + (unsigned)(s * 32 + h) * 24576u;
        v[0] = ld16(rw, l16, base + 8192 + w * 2048); v[1] = ld16(rw, l16, base + 8192 + w * 2048 + 1024);
        v[2] = ld16(rw, l16, base + 16384 + hf * 4096 + w * 1024);
    };
    auto qkv_store = [&](int s, u32x4 (&v)[3]) {
        lptr d = lds + L_QKV + (s & 1) * 12288 + lane * 16;
        ldsw128(d + w * 2048, v[0]); ldsw128(d + w * 2048 + 1024, v[1]);
        ldsw64(d + 8192 + (w >> 1) * 2048 + (w & 1) * 8, (u32x2){v[2].x, v[2].y}); ldsw64(d + 8192 + (w >> 1) * 2048 + 1024 + (w & 1) * 8, (u32x2){v[2].z, v[2].w});
    };
    const unsigned qflag = WS_FLAG + (FL_QKV + (h & 7) + ((((lane >> 1) & 1) * 32 + 32 + h) >> 3) * 16 + (lane & 1) * 8) * 128;
    auto qkv_wait = [&](unsigned need) { for (unsigned spins = 0;; ++spins) { asm volatile("" ::: "memory"); if (__all(ld4(rw, qflag) >= need)) break; if (spins > cap) { cap = 0; break; } __builtin_amdgcn_s_sleep(1); } asm volatile("" ::: "memory"); };
    { qkv_wait(1); u32x4 v[3]; qkv_issue(0, v); qkv_store(0, v); }
    const lptr qtw = lds + L_QT + w * 1024 + (n >> 3) * 256 + (((n & 7) * 32) ^ ((n >> 3) * 128)) + g * 8;
    const lptr tr0 = lds + L_QT + g * 256 + (n >> 2) * 32 + (lane & 3) * 8 + (g & 1) * 128, tr1 = tr0 + ((g & 1) ? -128 : 128);
    auto trf = [&](int off) { const u32x2 a = trread(tr0 + off), c = trread(tr1 + off); return (u32x4){a.x, a.y, c.x, c.y}; };
    const lptr mw = lds + L_MEMF + tid * 16 + w * 1024, tsw = lds + L_TS + tid * 4, vtw = lds + L_VT + tid * 4, vtr = lds + L_VT + lane * 4;
    const lptr rdw = lds + L_RED + (w * 64 + 16 * (n & 3) + 4 * (g & 1) + ((n >> 2) & 1)) * 4, rdr = lds + L_RED + (lane & 31) * 8;
    const unsigned tso = w == 3 ? l16 : OOB, sco = w == 3 ? (lane & 1) * 4 : OOB, mso = (lane >> 5) * MCOPY + (lane & 31) * 4, kso = (hf | hi) ? OOB : (rowi * 4 + g) * 16, pvo = (n >> 1) * 8192 + ((n & 1) * 4 + g) * 16;
    if (w == 1) ldsw128(lds + L_ONES + lane * 16, (u32x4){0x3f803f80u, 0x3f803f80u, 0x3f803f80u, 0x3f803f80u});
    if (w == 0) { const unsigned one = 0x3f80u << (16 * (n & 1)), k2 = (n >> 1) & 3; ldsw128(lds + L_SEL + lane * 16, (u32x4){k2 == 0 ? one : 0u, k2 == 1 ? one : 0u, k2 == 2 ? one : 0u, k2 == 3 ? one : 0u}); }
    const unsigned long long himask = 0xCCCCCCCCCCCCCCCCull;
    bar();
    unsigned T2 = rt(), T1 = 0, ring = 0; bool qrdy = false;
    auto step = [&](const int s, auto last_) {
        constexpr bool LAST = decltype(last_)::value;
        f32x4 aq0, aq1, ak0, ak1, av;
        const unsigned pprev = WS_PART + (ring == 0 ? NRING - 1 : ring - 1) * 524288 + b * 8192 + w * 2048;
        const unsigned mo = mcp + s * MSLOT + w * 2048, to = mcp + s * MSLOT + 8192;
        u32x4 A[8], kx[2][4], qf[4], kf[4]; unsigned tsv = 0;
        wait_rt(T2);
        ST(0);
        A[0] = ld16(rw, mvo, mo); A[1] = ld16(rw, mvo, mo + 1024); tsv = ld4(rw, sco, to);
        SB();
        st4(rw, t0o, s, WS_FLAG + (FL_KM + b) * 128);
        SB();
        lptr qb = lds + L_QKV + (s & 1) * 12288 + lane * 16;
        lptr ar0 = lds + L_MEMF + lane * 16 + ((2 * w + 2) & 7) * 1024, ar1 = lds + L_MEMF + lane * 16 + ((2 * w + 4) & 7) * 1024, ar2 = lds + L_MEMF + lane * 16 + ((2 * w + 6) & 7) * 1024;
        asm volatile("" : "+v"(qb), "+v"(ar0), "+v"(ar1), "+v"(ar2));
        if constexpr (!LAST) {
#pragma unroll
            for (int ks = 0; ks < NPRE; ++ks) { kx[0][ks] = ldsr128(qb + ks * 1024); kx[1][ks] = ldsr128(qb + 4096 + ks * 1024); }
        }
        aq0 = zero4(); aq1 = zero4(); ak0 = zero4(); ak1 = zero4(); av = zero4();
        if constexpr (!LAST) {
#pragma unroll
            for (int ks = 0; ks < NPRE; ++ks) asm volatile("" : "+v"(kx[0][ks]), "+v"(kx[1][ks]));
        }
        SB();
        ldsw128(mw, A[0]); ldsw128(mw + 1024, A[1]); ldsw32(tsw, tsv);
        SB();
        if (!__all(mx4(A[0], mx4(A[1], tsv)) != SENT)) {
            for (unsigned spins = 0;; ++spins) {
                asm volatile("" ::: "memory");
                A[0] = ld16(rw, mvo, mo); A[1] = ld16(rw, mvo, mo + 1024); tsv = ld4(rw, sco, to);
                if (__all(mx4(A[0], mx4(A[1], tsv)) != SENT)) break;
                if (spins > cap) { cap = 0; break; }
            }
            ldsw128(mw, A[0]); ldsw128(mw + 1024, A[1]); ldsw32(tsw, tsv);
        }
        ST(1);
        TS(0);
        NOP2();
        if constexpr (!LAST) { SMFA(aq0, A[0], wq[0][0]); SMFA(aq1, A[0], wq[1][0]); }
        SMFA(ak0, A[0], wk[0][0]);
        bar();
        TS(1);
        A[2] = ldsr128(ar0); A[3] = ldsr128(ar0 + 1024); SB();
        SMFA(ak1, A[0], wk[1][0]);
        A[4] = ldsr128(ar1); A[5] = ldsr128(ar1 + 1024); SB();
        SMFV(av, A[0], wv[0]);
        A[6] = ldsr128(ar2); A[7] = ldsr128(ar2 + 1024); SB();
        u32x2 tm = {0, 0};
#define POST3(C, acc, K, W, src, dst, WR) { float t0_, t1_; asm volatile( \
                "v_add_f32 %1, %10, %12\n\tv_smfmac_f32_16x16x64_bf16 %0, %3, %6, %9\n\tv_add_f32 %2, %11, %13\n\tv_smfmac_f32_16x16x64_bf16 %0, %4, %7, %9\n\t" \
                "v_cvt_pk_bf16_f32 %1, %1, %2\n\tv_smfmac_f32_16x16x64_bf16 %0, %5, %8, %9\n\tds_write2_b32 %14, %1, %1 " WR \
                : "+v"(acc), "=&v"(t0_), "=&v"(t1_) : "v"(A[K]), "v"(A[K + 1]), "v"(A[K + 2]), C(W[K]), C(W[K + 1]), C(W[K + 2]), "v"(sidx), \
                  "v"(src[0]), "v"(src[1]), "v"(src[2]), "v"(src[3]), "v"(dst) : "memory"); }
        if constexpr (!LAST) {
            SMFA(aq0, A[1], wq[0][1]);
            SMFA(aq1, A[1], wq[1][1]);
            tm = *(LAS u32x2*)(lds + L_TS + 768); SB();
            SMFA(ak0, A[1], wk[0][1]);
            SMFA(ak1, A[1], wk[1][1]); SMFV(av, A[1], wv[1]);
            TS(2);
            SMFA(aq0, A[2], wq[0][2]);
            SMFA(aq0, A[3], wq[0][3]); SMFA(aq0, A[4], wq[0][4]);
            SMFA(aq0, A[5], wq[0][5]); SMFA(aq0, A[6], wq[0][6]);
            SMFA(aq0, A[7], wq[0][7]);
            u32x4 vx[4];
            SMFA(aq1, A[2], wq[1][2]);
            vx[0] = ldsr128(qb + 8192); SB();
            SMFA(aq1, A[3], wq[1][3]);
            vx[2] = ldsr128(qb + 8192 + 2048); SB();
            SMFA(aq1, A[4], wq[1][4]);
            POST3("a", aq1, 5, wq[1], aq0, qtw, "offset0:0 offset1:1");
            TS(3);
            SMFA(ak0, A[2], wk[0][2]);
            SMFA(ak0, A[3], wk[0][3]);
            const u32x4 ones = ldsr128(lds + L_ONES + lane * 16); SB();
            SMFA(ak0, A[4], wk[0][4]);
            vx[1] = ldsr128(qb + 8192 + 1024); SB();
            POST3("a", ak0, 5, wk[0], aq1, qtw, "offset0:128 offset1:129");
            SMFA(ak1, A[2], wk[1][2]);
            SMFA(ak1, A[3], wk[1][3]);
            vx[3] = ldsr128(qb + 8192 + 3072); SB();
            SMFA(ak1, A[4], wk[1][4]);
            {
                float t0_, t1_;
                asm volatile("v_add_f32 %1, %10, %12\n\tv_smfmac_f32_16x16x64_bf16 %0, %3, %6, %9\n\tv_add_f32 %2, %11, %13\n\tv_smfmac_f32_16x16x64_bf16 %0, %4, %7, %9\n\t"
                             "v_cvt_pk_bf16_f32 %1, %1, %2\n\ts_waitcnt lgkmcnt(0)\n\ts_barrier\n\tv_smfmac_f32_16x16x64_bf16 %0, %5, %8, %9\n\tds_write2_b32 %14, %1, %1 offset0:0 offset1:1"
                             : "+v"(ak1), "=&v"(t0_), "=&v"(t1_) : "v"(A[5]), "v"(A[6]), "v"(A[7]), "a"(wk[1][5]), "a"(wk[1][6]), "a"(wk[1][7]), "v"(sidx),
                               "v"(ak0[0]), "v"(ak0[1]), "v"(ak0[2]), "v"(ak0[3]), "v"(qtw + 4096) : "memory");
            }
            TS(4);
            qf[0] = trf(0); SB();
            SMFV(av, A[2], wv[2]);
            qf[1] = trf(1024); SB();
            SMFV(av, A[3], wv[3]);
            qf[2] = trf(2048); SB();
            SMFV(av, A[4], wv[4]);
            qf[3] = trf(3072); SB();
            POST3("v", av, 5, wv, ak1, qtw + 4096, "offset0:128 offset1:129");
            TS(5);
            f32x4 s0, s1, s2, c[8];
            MF0(s1, kx[0][0], qf[0]);
            MF0(s2, kx[1][0], qf[0]);
            MF(s1, kx[0][1], qf[1]);
            {
                float t0_, t1_;
                asm volatile("v_add_f32 %2, %8, %10\n\tv_add_f32 %3, %9, %11\n\tv_mfma_f32_16x16x32_bf16 %1, %4, %6, %1\n\tv_cvt_pk_bf16_f32 %2, %2, %3\n\t"
                             "v_mfma_f32_16x16x32_bf16 %0, %5, %7, %0\n\ts_waitcnt lgkmcnt(0)\n\ts_barrier\n\tds_write_b32 %12, %2"
                             : "+v"(s1), "+v"(s2), "=&v"(t0_), "=&v"(t1_) : "v"(kx[1][1]), "v"(kx[0][2]), "v"(qf[1]), "v"(qf[2]), "v"(av[0]), "v"(av[1]), "v"(av[2]), "v"(av[3]), "v"(vtw) : "memory");
            }
            TS(6);
            kf[0] = trf(4096); SB();
            MF(s2, kx[1][2], qf[2]);
            kf[1] = trf(4096 + 1024); SB();
            MF(s1, kx[0][3], qf[3]);
            kf[2] = trf(4096 + 2048); SB();
            MF(s2, kx[1][3], qf[3]);
            kf[3] = trf(4096 + 3072);
            SB();
            ST(2);
            TS(7);
            float mx;
            {
                float t_;
                MF0(s0, kf[0], qf[0]);
                asm volatile("v_mfma_f32_16x16x32_bf16 %1, %2, %3, %1\n\tv_max3_f32 %0, %4, %5, %6" : "=&v"(mx), "+v"(s0) : "v"(kf[1]), "v"(qf[1]), "v"(s1[0]), "v"(s1[1]), "v"(s1[2]));
                asm volatile("v_mfma_f32_16x16x32_bf16 %2, %3, %4, %2\n\tv_max3_f32 %1, %5, %6, %7\n\tv_max3_f32 %0, %0, %8, %9"
                             : "+v"(mx), "=&v"(t_), "+v"(s0) : "v"(kf[2]), "v"(qf[2]), "v"(s1[3]), "v"(s2[0]), "v"(s2[1]), "v"(s2[2]), "v"(s2[3]));
                asm volatile("v_mfma_f32_16x16x32_bf16 %2, %3, %4, %2\n\tv_max_f32 %0, %0, %1\n\tv_mov_b32 %1, %0\n\t"
                             "s_waitcnt lgkmcnt(0)\n\ts_barrier\n\tv_permlane16_swap_b32 %0, %1\n\tv_max_f32 %0, %0, %1\n\t"
                             "v_mov_b32 %1, %0\n\ts_nop 1\n\tv_permlane32_swap_b32 %0, %1\n\tv_max_f32 %0, %0, %1\n\tv_mul_f32 %0, 0xbe0293ee, %0"
                             : "+v"(mx), "+v"(t_), "+v"(s0) : "v"(kf[3]), "v"(qf[3]) : "memory");
            }
            ST2(12);
            TS(8);
            u32x4 px, pm;
            float e0, e1, em;
            {
                float a_, b_, c_;
#define FE(d, src) "v_fmamk_f32 " d ", " src ", 0x3e0293ee, %20\n\t"
                asm volatile(FE("%7", "%10") FE("%8", "%11") "v_exp_f32 %7, %7\n\t" FE("%9", "%12") "v_exp_f32 %8, %8\n\t" FE("%1", "%13") "v_exp_f32 %9, %9\n\tv_cvt_pk_bf16_f32 %0, %7, %8\n\t"
                             "v_exp_f32 %1, %1\n\t" FE("%7", "%14") FE("%8", "%15") "v_exp_f32 %7, %7\n\tv_cvt_pk_bf16_f32 %1, %9, %1\n\tv_exp_f32 %8, %8\n\t" FE("%9", "%16") FE("%3", "%17")
                             "v_exp_f32 %9, %9\n\tv_cvt_pk_bf16_f32 %2, %7, %8\n\tv_exp_f32 %3, %3\n\t" FE("%4", "%18") FE("%5", "%19") "v_cvt_pk_bf16_f32 %3, %9, %3\n\tv_max_f32 %6, %4, %5"
                             : "=&v"(px.x), "=&v"(px.y), "=&v"(px.z), "=&v"(px.w), "=&v"(e0), "=&v"(e1), "=&v"(em), "=&v"(a_), "=&v"(b_), "=&v"(c_)
                             : "v"(s1[0]), "v"(s1[1]), "v"(s1[2]), "v"(s1[3]), "v"(s2[0]), "v"(s2[1]), "v"(s2[2]), "v"(s2[3]), "v"(s0[0]), "v"(s0[1]), "v"(mx));
            }
            const bool slow = __any(em > 0.f);
            SB();
            TS(9);
            f32x4 o[4], ls;
            MF0(o[0], vx[0], px);
            vx[0].x = *(LAS unsigned*)vtr;
            pm = __builtin_bit_cast(u32x4, zero4());
            MF0(o[2], vx[2], px);
            vx[2].x = *(LAS unsigned*)(vtr + 512);
            float up = 0.f;
            if (slow) {
                up = xmax(vmax(em, 0.f)); e0 -= up; e1 -= up;
            }
            SB();
            MF0(o[1], vx[1], px);
            vx[1].x = *(LAS unsigned*)(vtr + 256); SB();
            asm volatile("v_exp_f32 %1, %1\n\tv_mfma_f32_16x16x32_bf16 %0, %2, %3, 0" : "=&v"(o[3]), "+v"(e0) : "v"(vx[3]), "v"(px));
            vx[3].x = *(LAS unsigned*)(vtr + 768); SB();
            asm volatile("v_exp_f32 %2, %2\n\tv_mfma_f32_16x16x32_bf16 %0, %4, %5, 0\n\tv_cvt_pk_bf16_f32 %1, %3, %2" : "=&v"(ls), "=&v"(pm.x), "+v"(e1) : "v"(e0), "v"(ones), "v"(px));
            SB();
            if (slow) {
                asm volatile("s_nop 7" : "+v"(o[0]), "+v"(o[1]), "+v"(o[2]), "+v"(o[3]), "+v"(ls));
                const float al = __builtin_amdgcn_exp2f(-up);
                o[0] *= al; o[1] *= al; o[2] *= al; o[3] *= al; ls *= al;
            }
            c[0] = zero4();
            asm volatile("" : "+v"(pm), "+v"(o[0]), "+v"(o[1]), "+v"(o[2]), "+v"(o[3]), "+v"(ls));
            TS(10);
            MF(ls, ones, pm);
            c[1] = zero4();
            MF(o[0], vx[0], pm);
            c[2] = zero4();
            MF(o[2], vx[2], pm);
            c[3] = zero4();
            MF(o[1], vx[1], pm);
            c[4] = zero4();
            u32x4 o8;
            {
                float inv; f32x2 il, ih, t0_, t1_;
                asm volatile("v_mfma_f32_16x16x32_bf16 %0, %3, %4, %0\n\tv_rcp_f32 %1, %5\n\ts_nop 0\n\tv_cndmask_b32_e64 %2, 0, %1, %6\n\tv_cndmask_b32_e64 %1, %1, 0, %6"
                             : "+v"(o[3]), "=&v"(il.x), "=&v"(ih.x) : "v"(vx[3]), "v"(pm), "v"(ls[0]), "s"(himask));
                (void)inv; (void)t0_; (void)t1_;
                TS(11);
                c[5] = zero4();
                f32x4 oa = o[0] * il.x + o[2] * ih.x;
                o8.x = pk(oa[0], oa[1]); o8.y = pk(oa[2], oa[3]);
                asm volatile("" : "+v"(o8.x), "+v"(o8.y), "+v"(o[1]), "+v"(o[3]));
                const f32x4 ob = o[1] * il.x + o[3] * ih.x;
                o8.z = pk(ob[0], ob[1]); o8.w = pk(ob[2], ob[3]);
            }
            ST2(13);
            asm volatile("s_nop 1" : "+v"(o8));
            TS(12);
            SB();
            SMFV(c[0], o8, wo[0]); c[6] = zero4(); SMFV(c[1], o8, wo[1]); c[7] = zero4(); SMFV(c[2], o8, wo[2]); SMFV(c[3], o8, wo[3]);
            const unsigned slot = WS_PART + ring * 524288 + (16 * w * 64 + b) * 128;
            u32x4 pa;
            {
                float t_;
                asm volatile("v_add_f32 %4, %15, %17\n\tv_smfmac_f32_16x16x64_bf16 %0, %9, %10, %14\n\tv_add_f32 %8, %16, %18\n\tv_smfmac_f32_16x16x64_bf16 %1, %9, %11, %14\n\t"
                             "v_cvt_pk_bf16_f32 %4, %4, %8\n\tv_smfmac_f32_16x16x64_bf16 %2, %9, %12, %14\n\tv_add_f32 %5, %19, %21\n\tv_smfmac_f32_16x16x64_bf16 %3, %9, %13, %14\n\t"
                             "v_add_f32 %8, %20, %22\n\tv_cvt_pk_bf16_f32 %5, %5, %8\n\tv_add_f32 %6, %23, %25\n\tv_add_f32 %8, %24, %26\n\tv_cvt_pk_bf16_f32 %6, %6, %8"
                             : "+v"(c[4]), "+v"(c[5]), "+v"(c[6]), "+v"(c[7]), "=&v"(pa.x), "=&v"(pa.y), "=&v"(pa.z), "=&v"(pa.w), "=&v"(t_)
                             : "v"(o8), "v"(wo[4]), "v"(wo[5]), "v"(wo[6]), "v"(wo[7]), "v"(sidx),
                               "v"(c[0][0]), "v"(c[0][1]), "v"(c[0][2]), "v"(c[0][3]), "v"(c[1][0]), "v"(c[1][1]), "v"(c[1][2]), "v"(c[1][3]), "v"(c[2][0]), "v"(c[2][1]), "v"(c[2][2]), "v"(c[2][3]));
            }
            auto fold = [](f32x4 v) { const f32x2 t = (f32x2){v[0], v[1]} + (f32x2){v[2], v[3]}; return pk(t.x, t.y); };
            pa.w = fold(c[3]);
            st16(rw, pvo, pa, slot);
            TS(13);
            SB();
            st16(rw, pvo, (u32x4){fold(c[4]), fold(c[5]), fold(c[6]), fold(c[7])}, slot + 65536);
            TS(14);
            SB();
            const unsigned now = rt();
            ST(3);
            T1 = __builtin_amdgcn_readfirstlane(tm.x); T2 = __builtin_amdgcn_readfirstlane(tm.y);
            if (s == 0 || (int)(now + TV1 - T1) > 0) T1 = now + TV1;
        } else {
#pragma unroll
            for (int ks = 1; ks < 8; ++ks) { SMFA(ak0, A[ks], wk[0][ks]); SMFA(ak1, A[ks], wk[1][ks]); SMFV(av, A[ks], wv[ks]); }
            asm volatile("s_nop 7" : "+v"(av), "+v"(ak0), "+v"(ak1));
            unsigned d0 = pk(ak0[0] + ak0[2], ak0[1] + ak0[3]); dsw2<0, 1>(qtw + 4096, d0, d0);
            d0 = pk(ak1[0] + ak1[2], ak1[1] + ak1[3]); dsw2<128, 129>(qtw + 4096, d0, d0);
            ldsw32(vtw, pk(av[0] + av[2], av[1] + av[3]));
            bar();
        }
        {
            const u32x4 kv = trf(4096 + w * 1024);
            const u32x4 vv = {*(LAS unsigned*)vtr, *(LAS unsigned*)(vtr + 256), *(LAS unsigned*)(vtr + 512), *(LAS unsigned*)(vtr + 768)};
            const unsigned kb = WS_KVM + (unsigned)(s * 32 + h) * 4096u;
            st16(rw, kso, kv, kb + w * 512); st16(rw, tso, vv, kb + 2048 + hf * 1024);
        }
        if constexpr (LAST) return; else {
        ST(4);
        u32x4 p0, p1, nv[3]; unsigned qf_;
        const unsigned pb = WS_PART + ring * 524288 + b * 8192 + w * 2048;
        if (!qrdy) qkv_wait(s + 2);
        wait_rt(T1);
        ST(5);
        p0 = ld16(rw, l16, pb); p1 = ld16(rw, l16, pb + 1024);
        SB();
        qkv_issue(s + 1, nv);
        qf_ = ld4(rw, qflag);
        { const u32x4 z = sent4(); st16(rw, l16, z, pprev); st16(rw, l16, z, pprev + 1024); }
        SB();
        u32x4 sel = ldsr128(lds + L_SEL + lane * 16);
        asm volatile("" : "+v"(sel));
        SB();
        f32x4 rr;
        MF0(rr, sel, p0); MF(rr, sel, p1);
        if (!__all(mx4(p0, mx4(p1, 0u)) != SENT)) {
            for (unsigned spins = 0;; ++spins) {
                asm volatile("" ::: "memory");
                p0 = ld16(rw, l16, pb); p1 = ld16(rw, l16, pb + 1024);
                if (__all(mx4(p0, mx4(p1, 0u)) != SENT)) break;
                if (spins > cap) { cap = 0; break; }
            }
            MF0(rr, sel, p0); MF(rr, sel, p1);
            asm volatile("s_nop 7" : "+v"(rr));
        }
        ST(6);
        {
            asm volatile("" : "+v"(rr));
            unsigned q0, q1, q2, q3;
            asm volatile("v_add_f32_dpp %0, %4, %4 row_ror:8 row_mask:0xf bank_mask:0xf\n\tv_add_f32_dpp %1, %5, %5 row_ror:8 row_mask:0xf bank_mask:0xf\n\t"
                         "v_add_f32_dpp %2, %6, %6 row_ror:8 row_mask:0xf bank_mask:0xf\n\tv_add_f32_dpp %3, %7, %7 row_ror:8 row_mask:0xf bank_mask:0xf"
                         : "=&v"(q0), "=&v"(q1), "=&v"(q2), "=&v"(q3) : "v"(rr[0]), "v"(rr[1]), "v"(rr[2]), "v"(rr[3]));
            dsw2<0, 8>(rdw, q0, q1); dsw2<2, 10>(rdw, q2, q3);
        }
        bar();
        if (w == 0) {
            f32x2 a0, a1, a2, a3;
            asm volatile("ds_read_b64 %0, %4\n\tds_read_b64 %1, %4 offset:256\n\tds_read_b64 %2, %4 offset:512\n\tds_read_b64 %3, %4 offset:768\n\ts_waitcnt lgkmcnt(0)"
                         : "=&v"(a0), "=&v"(a1), "=&v"(a2), "=&v"(a3) : "v"(rdr) : "memory");
            const f32x2 t = (a0 + a1) + (a2 + a3);
            const unsigned mv = pk(t.x, t.y);
#pragma unroll
            for (int cp = 0; cp < 4; ++cp) st4(rw, mso, mv, WS_MEMF + cp * 2 * MCOPY + (s + 1) * MSLOT + b * 128);
        }
        {
            const unsigned now = rt();
            const bool late = s == 0 || (int)(now + TV2 - T2) > 0;
            if (b == 0 && w == 0) {
                const unsigned n1 = (late ? now + TV2 + TPEN : T2) + TX;
                const unsigned sv = min((lane & 1) ? n1 + TY : n1, SENT - 1);
#pragma unroll
                for (int cp = 0; cp < 4; ++cp) st4(rw, mso, sv, WS_MEMF + cp * 2 * MCOPY + (s + 1) * MSLOT + 8192);
            }
            if (late) T2 = now + TV2;
        }
        ST(7);
        qkv_store(s + 1, nv);
        bar();
        ring = ring == NRING - 1 ? 0 : ring + 1;
        qrdy = __all(qf_ >= (unsigned)min(s + 3, NSEG));
        ST(8);
        }
    };
    for (int s = 0; s < NSEG - 1; ++s) step(s, bool_c<false>{});
    step(NSEG - 1, bool_c<true>{});
    drain(); bar();
    if (tid == 0) st4(rw, WS_FLAG + (FL_KM + b) * 128, NSEG);
}

template <int ROLE>
DI void stream_role(const Params& p, lptr lds, const int j, const int tid) {
    const int w = __builtin_amdgcn_readfirstlane(tid >> 6), lane = tid & 63, n = lane & 15, g = lane >> 4, pp = lane & 3;
    const rsrc_t rw = mkrs(p.ws, WS_END);
    const float* mod = (const float*)(p.ws + WS_MOD);
    unsigned cap = 1u << 17;
    const int cgp = (j & 7) + 8 * (j >> 4), qtt = (j >> 3) & 1, qm = cgp >> 5, qh = cgp & 31;
    if (j < 128) {
        u32x4 xv[4];
#pragma unroll
        for (int r = 0; r < 4; ++r) xv[r] = *(const u32x4*)(p.x + (size_t)(j * 16 + w * 4 + r) * D + lane * 8);
        float t[8], sh[8];
#pragma unroll
        for (int i = 0; i < 8; ++i) { t[i] = rb(1.f + mod[512 + lane * 8 + i]); sh[i] = mod[lane * 8 + i]; }
#pragma unroll
        for (int r = 0; r < 4; ++r) {
            float v[8] = {bflo(xv[r].x), bfhi(xv[r].x), bflo(xv[r].y), bfhi(xv[r].y), bflo(xv[r].z), bfhi(xv[r].z), bflo(xv[r].w), bfhi(xv[r].w)};
            float sum = ((v[0] + v[1]) + (v[2] + v[3])) + ((v[4] + v[5]) + (v[6] + v[7]));
            sum += dpp<0xB1>(sum); sum += dpp<0x4E>(sum); sum += dpp<0x124>(sum); sum += dpp<0x128>(sum); sum = xsum(sum);
            const float mean = sum * (1.f / D); float var = 0.f;
#pragma unroll
            for (int i = 0; i < 8; ++i) { const float d = v[i] - mean; var += d * d; }
            var += dpp<0xB1>(var); var += dpp<0x4E>(var); var += dpp<0x124>(var); var += dpp<0x128>(var); var = xsum(var);
            const float mb = rb(mean), sd = rb(sqrtf(rb(rb(var * (1.f / D)) + 1e-5f)));
#pragma unroll
            for (int i = 0; i < 8; ++i) v[i] = lnmod(v[i], mb, sd, t[i], sh[i]);
            st16(rw, WS_H1 + (unsigned)(j * 16 + (lane >> 2)) * 1024u + ((w * 4 + r) * 4 + (lane & 3)) * 16, (u32x4){pk(v[0], v[1]), pk(v[2], v[3]), pk(v[4], v[5]), pk(v[6], v[7])});
        }
        drain(); bar();
        if (tid == 0) st4(rw, WS_FLAG + (FL_LN + j) * 128, 1);
    }
    u32x4 qa[16], qb[16];
    { const u16* W = (qm == 0 ? p.Wq : qm == 1 ? p.Wk : p.Wv) + qh * 128; panel(lds, W, W + 64, HD, tid, 32 * w + 8 * pp, 32 * w + 8 * pp + 4, qa, qb); }
    if (w == 0) wait_flags(rw, FL_LN + qtt, 64, 2, 1, lane, cap);
    bar();
    u32x4 hv[4];
    const unsigned h1off = WS_H1 + (unsigned)(qtt * 16 + w * 4) * 1024u + (n * 4 + g) * 16;
    auto h1_issue = [&](int s) {
#pragma unroll
        for (int i = 0; i < 4; ++i) hv[i] = ld16(rw, h1off + (unsigned)s * 32768u + i * 1024); };
    auto qkv_seg = [&](int s) {
#pragma unroll
        for (int i = 0; i < 4; ++i) ldsw128(lds + L_A + (w * 4 + i) * 1024 + lane * 16, hv[i]);
        if (s + 1 < NSEG) h1_issue(s + 1);
        bar();
        f32x4 ca, cb;
        if (qm < 2) tile_mma<false>(lds, lane, qa, qb, ca, cb); else tile_mma<true>(lds, lane, qa, qb, ca, cb);
        const unsigned blk = qm < 2 ? qm * 8 + qtt * 4 + w : 16 + w * 2 + qtt;
        st16(rw, WS_QKV + (unsigned)(s * 32 + qh) * 24576u + blk * 1024 + lane * 16, (u32x4){pk(ca[0], ca[1]), pk(ca[2], ca[3]), pk(cb[0], cb[1]), pk(cb[2], cb[3])});
    };
    auto publish = [&](unsigned line, unsigned v) { drain(); bar(); if (tid == 0) st4(rw, WS_FLAG + line * 128, v); };
    h1_issue(0);
    qkv_seg(0); publish(FL_QKV + j, 1);
    qkv_seg(1); publish(FL_QKV + j, 2);
    u32x4 sa[16], sb[16];
    const int r = ROLE == 0 ? j : ROLE == 1 ? j - 64 : ROLE == 2 ? j - 120 : ROLE == 3 ? j - 152 : j - 168;
    const int tt = ROLE == 3 ? r >> 3 : (r >> 3) & 1;
    const int cgx = (r & 7) + 8 * (r >> 4);
    if (ROLE == 0) { const u16* W = p.Wo + (size_t)(r & 7) * 512 * D + (r >> 4) * 128; panel(lds, W, W + 64, D, tid, 32 * w + 4 * pp, 32 * w + 16 + 4 * pp, sa, sb); }
    const int cc = r >> 1, ctt = r & 1;
    if (ROLE == 1) panel(lds, p.wg + cc * 64, p.wu + cc * 64, F, tid, 16 * w + 4 * pp, 64 + 16 * w + 4 * pp, sa, sb);
    if (ROLE == 2) {
        const rsrc_t rd = mkrs(p.wd + cgx * 32, (1791 * D + 32) * 2);
#pragma unroll
        for (int half = 0; half < 2; ++half) {
            u32x4 v[14];
#pragma unroll
            for (int i = 0; i < 14; ++i) v[i] = ldw(rd, ((tid >> 2) * D + (tid & 3) * 8) * 2, (half * 896 + i * 64) * D * 2);
            bar();
#pragma unroll
            for (int i = 0; i < 14; ++i) ldsw128(lds + ((tid >> 2) + 64 * i) * 80 + (tid & 3) * 16, v[i]);
            bar();
            if ((w >> 1) == half) {
                lptr pa = lds + ((w & 1) * 448 + 8 * g + (n >> 2)) * 80 + pp * 8;
#pragma unroll
                for (int ks = 0; ks < 14; ++ks) { sa[ks] = tr2(pa + ks * 32 * 80, 4 * 80); sb[ks] = tr2(pa + ks * 32 * 80 + 32, 4 * 80); }
            }
        }
        bar();
    }
    for (int s = 2; s < LEAD; ++s) { qkv_seg(s); publish(FL_QKV + j, s + 1); }
    for (int s = 0; s < NSEG; ++s) {
        const bool doq = s + LEAD < NSEG;
        if (doq) qkv_seg(s + LEAD);
        if (ROLE == 4 && !doq) break;
        if (w == 0) {
            if (ROLE == 0) wait_flags(rw, FL_A + (r & 7) * 2, 2, 1, s + 1, lane, cap);
            if (ROLE == 1) wait_flags(rw, FL_L + ctt * 8, 8, 1, s + 1, lane, cap);
            if (ROLE == 2) wait_flags(rw, FL_C + tt, 28, 2, s + 1, lane, cap);
            if (ROLE == 3) { const unsigned off = WS_FLAG + (FL_B + (lane & 7) + tt * 8 + ((lane >> 3) & 3) * 16) * 128;
                for (unsigned spins = 0;; ++spins) { asm volatile("" ::: "memory"); if (__all(ld4(rw, off) >= (unsigned)s + 1)) break; if (spins > cap) { cap = 0; break; } __builtin_amdgcn_s_sleep(2); } asm volatile("" ::: "memory"); }
            if (ROLE == 4) wait_flags(rw, FL_KM + (j & 63), 1, 1, s + 1, lane, cap);
            if (ROLE == 5) {
                const int hh = 2 * r + ((lane >> 1) & 1), cg3 = ((lane >> 2) & 3) * 32 - 32 + hh;
                const unsigned off = WS_FLAG + ((lane & 12) == 0 ? FL_KM + (hh & 7) + 16 * (hh >> 3) + 8 * (lane & 1) : FL_QKV + (cg3 & 7) + 16 * (cg3 >> 3) + 8 * (lane & 1)) * 128;
                for (unsigned spins = 0;; ++spins) { asm volatile("" ::: "memory"); if (__all(ld4(rw, off) >= (unsigned)s + 1)) break; if (spins > cap) { cap = 0; break; } __builtin_amdgcn_s_sleep(2); } asm volatile("" ::: "memory"); }
        }
        drain(); bar();
        if (doq && tid == 0) st4(rw, WS_FLAG + (FL_QKV + j) * 128, s + LEAD + 1);
        if (ROLE == 0 || ROLE == 1) {
            u32x4 a[4];
#pragma unroll
            for (int i = 0; i < 4; ++i) a[i] = ROLE == 0 ? ld16(rw, WS_O + (unsigned)((s * 2 + tt) * 128 + (r & 7) * 16 + w * 4 + i) * 1024u + lane * 16)
                                                          : ld16(rw, WS_H2 + (unsigned)((s * 2 + ctt) * 16 + w * 4 + i) * 1024u + (n * 4 + g) * 16);
#pragma unroll
            for (int i = 0; i < 4; ++i) ldsw128(lds + L_A + (w * 4 + i) * 1024 + lane * 16, a[i]);
            bar();
            f32x4 ca, cb; tile_mma<false>(lds, lane, sa, sb, ca, cb);
            if (ROLE == 0) {
                const unsigned o = WS_PB + (unsigned)(((s * 8 + (r & 7)) * 32 + (r >> 4) * 8 + w * 2) * 2 + tt) * 1024u + (n * 4 + g) * 16;
                st16(rw, o, __builtin_bit_cast(u32x4, ca)); st16(rw, o + 2048, __builtin_bit_cast(u32x4, cb));
                publish(FL_B + r, s + 1);
            } else {
                float q[4];
#pragma unroll
                for (int i = 0; i < 4; ++i) { const float gv = rb(ca[i]); q[i] = rb(gv / (1.f + __expf(-gv))) * rb(cb[i]); }
                st8(rw, WS_G + (unsigned)((s * 2 + ctt) * 56 + cc * 2 + (w >> 1)) * 1024u + (w & 1) * 512 + n * 32 + g * 8, (u32x2){pk(q[0], q[1]), pk(q[2], q[3])});
                publish(FL_C + r, s + 1);
            }
        }
        if (ROLE == 2) {
            u32x4 a[14];
            const unsigned off = WS_G + (unsigned)((s * 2 + tt) * 56 + w * 14) * 1024u + (g >> 1) * 512 + n * 32 + (g & 1) * 16;
#pragma unroll
            for (int ks = 0; ks < 14; ++ks) a[ks] = ld16(rw, off + ks * 1024);
            const unsigned row = s * 32 + tt * 16 + n, col = cgx * 32 + (w & 1) * 16 + 4 * g;
            const u32x2 xv = ld8(rw, WS_X1 + (row * D + col) * 2);
            const f32x4 gb = *(const f32x4*)(mod + 2560 + col);
            SB();
            f32x4 c0 = {0, 0, 0, 0}, c1 = c0;
            asm volatile("s_nop 1" : "+v"(c0), "+v"(c1));
#pragma unroll
            for (int ks = 0; ks < 14; ++ks) { MFWA(c0, sa[ks], a[ks]); MFWA(c1, sb[ks], a[ks]); }
            asm volatile("s_nop 7" : "+v"(c0), "+v"(c1));
            *(LAS f32x4*)(lds + L_A + (w * 2) * 1024 + lane * 16) = c0; *(LAS f32x4*)(lds + L_A + (w * 2 + 1) * 1024 + lane * 16) = c1;
            bar();
            if (w < 2) {
                lptr q = lds + L_A + w * 1024 + lane * 16;
                const f32x4 y = (*(LAS f32x4*)q + *(LAS f32x4*)(q + 2048)) + (*(LAS f32x4*)(q + 4096) + *(LAS f32x4*)(q + 6144));
                const float o0 = bflo(xv.x) + rb(gb[0] * rb(y[0])), o1 = bfhi(xv.x) + rb(gb[1] * rb(y[1])), o2 = bflo(xv.y) + rb(gb[2] * rb(y[2])), o3 = bfhi(xv.y) + rb(gb[3] * rb(y[3]));
                *(u32x2*)(p.out + (size_t)row * D + col) = (u32x2){pk(o0, o1), pk(o2, o3)};
            }
            bar();
        }
        if (ROLE == 5) {
            const int hh = 2 * r + (w >> 1), att = w & 1;
            const unsigned qb_ = WS_QKV + (unsigned)(s * 32 + hh) * 24576u, kb_ = WS_KVM + (unsigned)(s * 32 + hh) * 4096u, l16 = lane * 16;
            u32x4 xq[4], ka[4], kb[4], km[4], vb[8], vm[2];
#pragma unroll
            for (int ks = 0; ks < 4; ++ks) { km[ks] = ld16(rw, ((2 * (n >> 2) + (n & 1)) * 4 + g) * 16, kb_ + ks * 512); xq[ks] = ld16(rw, l16, qb_ + att * 4096 + ks * 1024); ka[ks] = ld16(rw, l16, qb_ + 8192 + ks * 1024); kb[ks] = ld16(rw, l16, qb_ + 12288 + ks * 1024); }
            vm[0] = ld16(rw, l16, kb_ + 2048); vm[1] = ld16(rw, l16, kb_ + 3072);
#pragma unroll
            for (int i = 0; i < 8; ++i) vb[i] = ld16(rw, l16, qb_ + 16384 + i * 1024);
            SB();
            f32x4 x0, x1, x2;
            MF0(x1, ka[0], xq[0]); MF0(x2, kb[0], xq[0]); MF0(x0, km[0], xq[0]);
#pragma unroll
            for (int ks = 1; ks < 4; ++ks) { MF(x1, ka[ks], xq[ks]); MF(x2, kb[ks], xq[ks]); MF(x0, km[ks], xq[ks]); }
            asm volatile("s_nop 7" : "+v"(x0), "+v"(x1), "+v"(x2));
            const float raw[10] = {x1[0], x1[1], x1[2], x1[3], x2[0], x2[1], x2[2], x2[3], x0[0], x0[1]};
            float sc[10];
            const int qi = 16 * att + n - 4 * g;
#pragma unroll
            for (int i = 0; i < 5; ++i) {
                const unsigned u = vcvt(raw[2 * i], raw[2 * i + 1]);
                const unsigned v = pk(bflo(u) * 0.08838834764831845f, bfhi(u) * 0.08838834764831845f);
                sc[2 * i] = bflo(v); sc[2 * i + 1] = bfhi(v);
            }
#pragma unroll
            for (int i = 0; i < 8; ++i) sc[i] = ((i & 3) + 16 * (i >> 2) <= qi) ? sc[i] : -1e9f;
            float mx = vmax3(vmax3(vmax3(sc[0], sc[1], sc[2]), vmax3(sc[3], sc[4], sc[5]), vmax(sc[6], sc[7])), sc[8], sc[9]);
            mx = xmax(mx) * -1.4426950408889634f;
#pragma unroll
            for (int i = 0; i < 10; ++i) sc[i] = __builtin_amdgcn_exp2f(__builtin_fmaf(sc[i], 1.4426950408889634f, mx));
            const float inv = __builtin_amdgcn_rcpf(xsum((((sc[0] + sc[1]) + (sc[2] + sc[3])) + ((sc[4] + sc[5]) + (sc[6] + sc[7]))) + (sc[8] + sc[9])));
            u32x4 xpx = {pk(sc[0] * inv, sc[1] * inv), pk(sc[2] * inv, sc[3] * inv), pk(sc[4] * inv, sc[5] * inv), pk(sc[6] * inv, sc[7] * inv)};
            u32x4 xpm = {pk(sc[8] * inv, sc[9] * inv), 0u, 0u, 0u};
#pragma unroll
            for (int e = 0; e < 4; ++e) {
                u32x4 fa = {vb[2 * e].x, vb[2 * e].y, vb[2 * e + 1].x, vb[2 * e + 1].y}, fb = {vb[2 * e].z, vb[2 * e].w, vb[2 * e + 1].z, vb[2 * e + 1].w};
                const unsigned m0_ = vm[e >> 1][2 * (e & 1)], m1_ = vm[e >> 1][2 * (e & 1) + 1];
                u32x4 ma = {m0_, m0_, m0_, m0_}, mb = {m1_, m1_, m1_, m1_};
                f32x4 o0, o1;
                asm volatile("s_nop 1" : "+v"(fa), "+v"(fb), "+v"(ma), "+v"(mb), "+v"(xpx), "+v"(xpm));
                MF0(o0, fa, xpx); MF0(o1, fb, xpx); MF(o0, ma, xpm); MF(o1, mb, xpm);
                asm volatile("s_nop 7" : "+v"(o0), "+v"(o1));
                st16(rw, l16, (u32x4){pk(o0[0], o0[1]), pk(o0[2], o0[3]), pk(o1[0], o1[1]), pk(o1[2], o1[3])}, WS_O + (unsigned)((s * 2 + att) * 128 + hh * 4 + e) * 1024u);
            }
            publish(FL_A + r, s + 1);
        }
        if (ROLE == 3) {
            LAS float* sm = (LAS float*)(lds + L_A);
            const int tk = (lane >> 2) & 1, tok = 2 * (r & 7) + tk, d0 = 128 * w + 16 * (lane >> 3) + 4 * (lane & 3), row = s * 32 + tt * 16 + tok;
            u32x4 v[8];
#pragma unroll
            for (int kg = 0; kg < 8; ++kg) v[kg] = ld16(rw, WS_PB + (unsigned)(((s * 8 + kg) * 32 + 8 * w + (lane >> 3)) * 2 + tt) * 1024u + (r & 7) * 128 + (lane & 7) * 16);
            const u32x2 xv = *(const u32x2*)(p.x + (size_t)row * D + d0);
            const f32x4 ga = *(const f32x4*)(mod + 1024 + d0), shb = *(const f32x4*)(mod + 1536 + d0), cb = *(const f32x4*)(mod + 2048 + d0);
            SB();
            f32x4 a = __builtin_bit_cast(f32x4, v[0]);
#pragma unroll
            for (int kg = 1; kg < 8; ++kg) a += __builtin_bit_cast(f32x4, v[kg]);
            float x1[4] = {bflo(xv.x), bfhi(xv.x), bflo(xv.y), bfhi(xv.y)};
            float sum = 0.f;
#pragma unroll
            for (int i = 0; i < 4; ++i) { x1[i] = rb(x1[i] + rb(ga[i] * rb(a[i]))); sum += x1[i]; }
            st8(rw, WS_X1 + (unsigned)(row * D + d0) * 2, (u32x2){pk(x1[0], x1[1]), pk(x1[2], x1[3])});
            sum += dpp<0xB1>(sum); sum += dpp<0x4E>(sum); sum += dpp<0x128>(sum); sum = xsum(sum);
            if (lane == 0 || lane == 4) sm[w * 2 + tk] = sum;
            bar();
            const float mean = ((sm[tk] + sm[2 + tk]) + (sm[4 + tk] + sm[6 + tk])) * (1.f / D);
            float var = 0.f;
#pragma unroll
            for (int i = 0; i < 4; ++i) { const float d = x1[i] - mean; var += d * d; }
            var += dpp<0xB1>(var); var += dpp<0x4E>(var); var += dpp<0x128>(var); var = xsum(var);
            if (lane == 0 || lane == 4) sm[8 + w * 2 + tk] = var;
            bar();
            var = ((sm[8 + tk] + sm[10 + tk]) + (sm[12 + tk] + sm[14 + tk])) * (1.f / D);
            const float mb = rb(mean), sd = rb(sqrtf(rb(rb(var) + 1e-5f)));
            float hh[4];
#pragma unroll
            for (int i = 0; i < 4; ++i) hh[i] = lnmod(x1[i], mb, sd, rb(1.f + cb[i]), shb[i]);
            st8(rw, WS_H2 + (unsigned)((s * 2 + tt) * 16 + (d0 >> 5)) * 1024u + tok * 64 + (d0 & 31) * 2, (u32x2){pk(hh[0], hh[1]), pk(hh[2], hh[3])});
            publish(FL_L + r, s + 1);
        }
    }
}

__global__ void __launch_bounds__(256, 1) k_main(Params p) {
    extern __shared__ __attribute__((aligned(16))) char smem[];
    const lptr lds = (lptr)smem;
    const int b = blockIdx.x, tid = threadIdx.x;
    if (b < 64) chain_role(p, lds, b, tid);
    else {
        const int j = b - 64;
        if (j < 64) stream_role<0>(p, lds, j, tid);
        else if (j < 120) stream_role<1>(p, lds, j, tid);
        else if (j < 152) stream_role<2>(p, lds, j, tid);
        else if (j < 168) stream_role<3>(p, lds, j, tid);
        else if (j < 184) stream_role<5>(p, lds, j, tid);
        else stream_role<4>(p, lds, j, tid);
    }
}

extern "C" void kernel_launch(void* const* d_in, const int* in_sizes, int n_in, void* d_out, int out_size, void* d_ws, size_t ws_size, hipStream_t stream) {
    Params p;
    p.x = (const u16*)d_in[0]; p.mem0 = (const u16*)d_in[1]; p.Wq = (const u16*)d_in[2]; p.Wk = (const u16*)d_in[3]; p.Wv = (const u16*)d_in[4]; p.Wo = (const u16*)d_in[5];
    p.wg = (const u16*)d_in[8]; p.wu = (const u16*)d_in[9]; p.wd = (const u16*)d_in[10]; p.c = (const u16*)d_in[11]; p.wmod = (const u16*)d_in[12];
    p.out = (u16*)d_out; p.ws = (char*)d_ws;
    constexpr int LDSB = 96 * 1024;
    (void)hipFuncSetAttribute((const void*)k_main, hipFuncAttributeMaxDynamicSharedMemorySize, LDSB);
    k_init<<<256, 256, 0, stream>>>(p);
    k_main<<<256, 256, LDSB, stream>>>(p);
}
```

```cpp
#include <hip/hip_runtime.h>
#include <hip/hip_bf16.h>
#include <stdint.h>

typedef unsigned short u16;
typedef __bf16 bf16x2 __attribute__((ext_vector_type(2)));
typedef __bf16 bf16x8 __attribute__((ext_vector_type(8)));
typedef short s16x4 __attribute__((ext_vector_type(4)));
typedef float f32x4 __attribute__((ext_vector_type(4)));
typedef float f32x2 __attribute__((ext_vector_type(2)));
typedef unsigned u32x2 __attribute__((ext_vector_type(2)));
typedef unsigned u32x4 __attribute__((ext_vector_type(4)));
typedef unsigned u32x8 __attribute__((ext_vector_type(8)));
#define LAS __attribute__((address_space(3)))
typedef LAS char* lptr;
#define DI __device__ __forceinline__

#define TX 154
#define TY 68
#define TV1 14
#define TV2 14
#define TPEN 30
#define TM 12
#define SIDE 0
#define SLP 5
#define NPRE 4
#define LEAD 16

constexpr int S = 2048, D = 512, NSEG = 64, F = 1792, HD = 4096;
constexpr unsigned SENT = 0xFFFFFFFFu;
constexpr int NRING = 3;
constexpr unsigned WS_MOD = 0;
constexpr unsigned WS_FLAG = 16384;
constexpr unsigned FL_LN = 0, FL_QKV = 128, FL_KM = 320, FL_B = 384, FL_L = 448, FL_C = 464, FL_A = 520, FL_N = 536;
constexpr unsigned WS_CFG = 12544;
#define NC 4
constexpr unsigned WS_HO = 122u << 20, HO_MEM = 256, MRS = NC + 1, HO_N = HO_MEM + 3 * MRS;
constexpr unsigned WS_DBG = 3u << 20;
constexpr unsigned WS_H1 = 4u << 20, WS_H2 = 6u << 20, WS_X1 = 8u << 20, WS_G = 10u << 20, WS_O = 18u << 20, WS_QKV = 34u << 20, WS_PB = 82u << 20, WS_END = 150u << 20;

DI float bflo(unsigned x) { return __uint_as_float(x << 16); }
DI float bfhi(unsigned x) { return __uint_as_float(x & 0xffff0000u); }
DI unsigned pk(float lo, float hi) { bf16x2 v = {(__bf16)lo, (__bf16)hi}; return __builtin_bit_cast(unsigned, v); }
DI float rb(float x) { return (float)(__bf16)x; }
DI void bar() { asm volatile("s_waitcnt lgkmcnt(0)\n\ts_barrier" ::: "memory"); }
DI void drain() { asm volatile("s_waitcnt vmcnt(0)" ::: "memory"); }
DI u32x2 trread(lptr p) { return __builtin_bit_cast(u32x2, __builtin_amdgcn_ds_read_tr16_b64_v4i16((LAS s16x4*)p)); }
DI u32x4 tr2(lptr p, int off) { const u32x2 a = trread(p), b = trread(p + off); return (u32x4){a.x, a.y, b.x, b.y}; }
DI u32x4 ldsr128(lptr p) { return *(LAS u32x4*)p; }
DI void ldsw128(lptr p, u32x4 v) { *(LAS u32x4*)p = v; }
DI void ldsw64(lptr p, u32x2 v) { *(LAS u32x2*)p = v; }
DI void ldsw32(lptr p, unsigned v) { *(LAS unsigned*)p = v; }
DI f32x4 mfma32(u32x4 a, u32x4 b, f32x4 c) { return __builtin_amdgcn_mfma_f32_16x16x32_bf16(__builtin_bit_cast(bf16x8, a), __builtin_bit_cast(bf16x8, b), c, 0, 0, 0); }
DI float swadd32(float a, float b) {
    auto r = __builtin_amdgcn_permlane32_swap(__float_as_uint(a), __float_as_uint(b), false, false);
    return __uint_as_float(r[0]) + __uint_as_float(r[1]);
}
DI float vmax(float a, float b) { float r; asm("v_max_f32 %0, %1, %2" : "=v"(r) : "v"(a), "v"(b)); return r; }
DI float vmax3(float a, float b, float c) { float r; asm("v_max3_f32 %0, %1, %2, %3" : "=v"(r) : "v"(a), "v"(b), "v"(c)); return r; }
DI float xmax(float x) {
    auto s = __builtin_amdgcn_permlane16_swap(__float_as_uint(x), __float_as_uint(x), false, false);
    x = vmax(__uint_as_float(s[0]), __uint_as_float(s[1]));
    auto t = __builtin_amdgcn_permlane32_swap(__float_as_uint(x), __float_as_uint(x), false, false);
    return vmax(__uint_as_float(t[0]), __uint_as_float(t[1]));
}
DI float xsum(float x) {
    auto s = __builtin_amdgcn_permlane16_swap(__float_as_uint(x), __float_as_uint(x), false, false);
    x = __uint_as_float(s[0]) + __uint_as_float(s[1]);
    auto t = __builtin_amdgcn_permlane32_swap(__float_as_uint(x), __float_as_uint(x), false, false);
    return __uint_as_float(t[0]) + __uint_as_float(t[1]);
}
template <int CTRL> DI float dpp(float x) { return __builtin_bit_cast(float, __builtin_amdgcn_mov_dpp(__builtin_bit_cast(int, x), CTRL, 0xf, 0xf, true)); }
template <int CTRL> DI int dppi(int x) { return __builtin_amdgcn_mov_dpp(x, CTRL, 0xf, 0xf, true); }
DI unsigned mx4(u32x4 v, unsigned m) { return max(max(max(v.x, v.y), max(v.z, v.w)), m); }
typedef __amdgpu_buffer_rsrc_t rsrc_t;
DI rsrc_t mkrs(const void* p, unsigned bytes) { return __builtin_amdgcn_make_buffer_rsrc((void*)p, 0, bytes, 0x00020000); }
DI u32x4 ld16(rsrc_t r, unsigned off, unsigned soff = 0) { return __builtin_amdgcn_raw_buffer_load_b128(r, off, soff, 16); }
DI u32x2 ld8(rsrc_t r, unsigned off) { return __builtin_amdgcn_raw_buffer_load_b64(r, off, 0, 16); }
DI unsigned ld4(rsrc_t r, unsigned off, unsigned soff = 0) { return __builtin_amdgcn_raw_buffer_load_b32(r, off, soff, 16); }
DI void st16(rsrc_t r, unsigned off, u32x4 v, unsigned soff = 0) { __builtin_amdgcn_raw_buffer_store_b128(v, r, off, soff, 16); }
DI void st8(rsrc_t r, unsigned off, u32x2 v) { __builtin_amdgcn_raw_buffer_store_b64(v, r, off, 0, 16); }
DI void st4(rsrc_t r, unsigned off, unsigned v, unsigned soff = 0) { __builtin_amdgcn_raw_buffer_store_b32(v, r, off, soff, 16); }
DI u32x4 ldw(rsrc_t r, unsigned voff, unsigned soff) { return __builtin_amdgcn_raw_buffer_load_b128(r, voff, soff, 0); }
DI unsigned rt() { return (unsigned)__builtin_amdgcn_s_memrealtime(); }
DI void wait_rt(unsigned t) { while ((int)(rt() - t) < 0) { } asm volatile("" ::: "memory"); }

template <bool V> struct bool_c { static constexpr bool value = V; };
DI unsigned ho_base(const char* ws) { return WS_HO + ((0u - (unsigned)(uintptr_t)ws - WS_HO) & 0x1FFFFFu); }
DI unsigned kvm_off(unsigned ho, unsigned cfg, unsigned i) { return ho + (8u << 20) + (i >> 1) * 16384 + ((cfg >> ((i >> 8) & 1)) & 1) * 8192 + (i & 1) * 4096; }
struct Params {
    const u16 *x, *mem0, *Wq, *Wk, *Wv, *Wo, *wg, *wu, *wd, *c, *wmod;
    u16* out; char* ws;
};

__global__ void __launch_bounds__(256) k_init(Params p) {
    __shared__ float red[4][64];
    const int tid = threadIdx.x, w = tid >> 6, lane = tid & 63;
    if (blockIdx.x < 48) {
        const int r8 = lane >> 3, cc = lane & 7, n0 = blockIdx.x * 64 + cc * 8;
        u32x4 wv[16]; float s[16];
#pragma unroll
        for (int it = 0; it < 16; ++it) wv[it] = *(const u32x4*)(p.wmod + (size_t)(w * 128 + it * 8 + r8) * 3072 + n0);
#pragma unroll
        for (int it = 0; it < 16; ++it) { const float cv = bflo(p.c[w * 128 + it * 8 + r8]); s[it] = rb(cv / (1.f + __expf(-cv))); }
        float acc[8] = {};
#pragma unroll
        for (int it = 0; it < 16; ++it) {
            acc[0] += s[it] * bflo(wv[it].x); acc[1] += s[it] * bfhi(wv[it].x); acc[2] += s[it] * bflo(wv[it].y); acc[3] += s[it] * bfhi(wv[it].y);
            acc[4] += s[it] * bflo(wv[it].z); acc[5] += s[it] * bfhi(wv[it].z); acc[6] += s[it] * bflo(wv[it].w); acc[7] += s[it] * bfhi(wv[it].w);
        }
#pragma unroll
        for (int i = 0; i < 8; ++i) { acc[i] += dpp<0x128>(acc[i]); acc[i] = xsum(acc[i]); }
        if (lane < 8) for (int i = 0; i < 8; ++i) red[w][cc * 8 + i] = acc[i];
        __syncthreads();
        if (tid < 64) ((float*)(p.ws + WS_MOD))[blockIdx.x * 64 + tid] = rb((red[0][tid] + red[1][tid]) + (red[2][tid] + red[3][tid]));
    } else if (blockIdx.x == 48) {
        if (w == 0) {
            const rsrc_t rw = mkrs(p.ws, WS_END);
            const unsigned a = ho_base(p.ws) + 240 * 16384;
            unsigned best[2] = {~0u, ~0u};
            for (int rep = 0; rep < 3; ++rep)
#pragma unroll
                for (int hlf = 0; hlf < 2; ++hlf) {
                    unsigned long long t0, t1; unsigned r;
                    st4(rw, (lane & 31) * 4, rep, a + hlf * 8192);
                    asm volatile("s_waitcnt vmcnt(0)\n\ts_memtime %0\n\ts_waitcnt lgkmcnt(0)" : "=s"(t0) :: "memory");
                    r = ld4(rw, (lane & 31) * 4, a + hlf * 8192);
                    asm volatile("s_waitcnt vmcnt(0)\n\ts_memtime %0\n\ts_waitcnt lgkmcnt(0)" : "=s"(t1), "+v"(r) :: "memory");
                    best[hlf] = min(best[hlf], (unsigned)(t1 - t0));
                }
            if (lane == 0) { *(unsigned*)(p.ws + WS_CFG) = best[1] < best[0] ? 2u : 1u;
            }
        }
    } else {
        const unsigned t = (blockIdx.x - 49) * 256 + tid, nt = (gridDim.x - 49) * 256;
        const u32x4 z = {SENT, SENT, SENT, SENT}, zero = {0, 0, 0, 0};
        for (unsigned i = t; i < FL_N * 8; i += nt) ((u32x4*)(p.ws + WS_FLAG))[i] = zero;
        u32x4* ho = (u32x4*)(p.ws + ho_base(p.ws));
        for (unsigned i = t; i < HO_N * 1024; i += nt) {
            u32x4 v = z;
            const unsigned blk = i >> 10, m = i & 511;
            if (blk >= 192 && blk < HO_MEM) continue;
            if (blk >= HO_MEM && blk < HO_MEM + NC) {
                const unsigned* mp = (const unsigned*)(p.mem0 + (2 * ((m >> 2) & 3) + (m & 1)) * D + 16 * (m >> 4) + (m & 2));
                v = (u32x4){min(mp[0], SENT - 1), min(mp[2], SENT - 1), min(mp[4], SENT - 1), min(mp[6], SENT - 1)};
            }
            if (blk == HO_MEM + NC) v = zero;
            ho[i] = v;
        }
    }
}

DI float lnmod(float v, float mb, float sd, float t, float sh) { return rb(rb(rb(v - mb) / sd) * t) + sh; }

DI void wait_flags(rsrc_t rw, unsigned first, int cnt, int stride, unsigned need, int lane, unsigned& cap) {
    const unsigned off = WS_FLAG + (first + (lane < cnt ? lane : 0) * stride) * 128;
    for (unsigned spins = 0;; ++spins) {
        asm volatile("" ::: "memory");
        if (__all(ld4(rw, off) >= need)) break;
        if (spins > cap) { cap = 0; break; }
        __builtin_amdgcn_s_sleep(2);
    }
    asm volatile("" ::: "memory");
}

constexpr int PSTR = 272;
DI void panel(lptr lds, const u16* srcA, const u16* srcB, int ld, int tid, int colA, int colB, u32x4 (&wa)[16], u32x4 (&wb)[16]) {
    const int lane = tid & 63, g = lane >> 4, q = (lane & 15) >> 2;
    const rsrc_t ra = mkrs(srcA, (511 * ld + 64) * 2), rb_ = mkrs(srcB, (511 * ld + 64) * 2);
    const unsigned voff = ((tid >> 3) * ld + (tid & 7) * 8) * 2;
    const lptr wp = lds + (tid >> 3) * PSTR + (tid & 7) * 16;
#pragma unroll
    for (int half = 0; half < 2; ++half) {
        u32x4 v[16];
#pragma unroll
        for (int i = 0; i < 8; ++i) { v[i] = ldw(ra, voff, (half * 256 + i * 32) * ld * 2); v[8 + i] = ldw(rb_, voff, (half * 256 + i * 32) * ld * 2); }
        bar();
#pragma unroll
        for (int i = 0; i < 8; ++i) { ldsw128(wp + i * 32 * PSTR, v[i]); ldsw128(wp + i * 32 * PSTR + 128, v[8 + i]); }
        bar();
        lptr pa = lds + (8 * g + q) * PSTR + colA * 2, pb = lds + (8 * g + q) * PSTR + colB * 2;
#pragma unroll
        for (int ks = 0; ks < 8; ++ks) { wa[half * 8 + ks] = tr2(pa + ks * 32 * PSTR, 4 * PSTR); wb[half * 8 + ks] = tr2(pb + ks * 32 * PSTR, 4 * PSTR); }
    }
    bar();
}
constexpr int L_A = 0, L_Q = 73728;
#define MFWA(acc, wgt, x) asm volatile("v_mfma_f32_16x16x32_bf16 %0, %1, %2, %0" : "+v"(acc) : "a"(wgt), "v"(x))
#define MFWB(acc, wgt, x) asm volatile("v_mfma_f32_16x16x32_bf16 %0, %2, %1, %0" : "+v"(acc) : "a"(wgt), "v"(x))
template <bool SWAP> DI void tile_mma(lptr lds, int lane, const u32x4 (&wa)[16], const u32x4 (&wb)[16], f32x4& ca, f32x4& cb) {
    ca = (f32x4){0, 0, 0, 0}; cb = ca;
    u32x4 a[16];
#pragma unroll
    for (int ks = 0; ks < 16; ++ks) a[ks] = ldsr128(lds + ks * 1024 + lane * 16);
    asm volatile("s_nop 1" : "+v"(ca), "+v"(cb));
    __builtin_amdgcn_sched_barrier(0);
#pragma unroll
    for (int ks = 0; ks < 16; ++ks) { if (SWAP) { MFWB(ca, wa[ks], a[ks]); MFWB(cb, wb[ks], a[ks]); } else { MFWA(ca, wa[ks], a[ks]); MFWA(cb, wb[ks], a[ks]); } }
    asm volatile("s_nop 7" : "+v"(ca), "+v"(cb));
}

constexpr int CSTR = 656, OSTR = 1040;
constexpr int L_MEMF = 0, L_QT = 8192, L_KT = 12288, L_VT = 16384, L_RED = 20480, L_TS = 21504, L_SEL = 23552, L_ONES = 24576, L_QKV = 25600;
constexpr float SC_L2E = 0.08838834764831845f * 1.4426950408889634f;
constexpr unsigned OOB = 0x80000000u;
#define SMFA(acc, a, w) asm volatile("v_smfmac_f32_16x16x64_bf16 %0, %1, %2, %3" : "+v"(acc) : "v"(a), "a"(w), "v"(sidx))
#define SMFV(acc, a, w) asm volatile("v_smfmac_f32_16x16x64_bf16 %0, %1, %2, %3" : "+v"(acc) : "v"(a), "v"(w), "v"(sidx))
#define MF0(acc, a, b) asm volatile("v_mfma_f32_16x16x32_bf16 %0, %1, %2, 0" : "=&v"(acc) : "v"(a), "v"(b))
#define MF(acc, a, b) asm volatile("v_mfma_f32_16x16x32_bf16 %0, %1, %2, %0" : "+v"(acc) : "v"(a), "v"(b))
#define NOP2() asm volatile("s_nop 1")
#define SB() __builtin_amdgcn_sched_barrier(0)
DI f32x4 zero4() { f32x2 a, b; asm volatile("v_mov_b64 %0, 0\n\tv_mov_b64 %1, 0" : "=v"(a), "=v"(b)); return (f32x4){a.x, a.y, b.x, b.y}; }
DI u32x4 sent4() { u32x2 a, b; asm volatile("v_mov_b64 %0, -1\n\tv_mov_b64 %1, -1" : "=v"(a), "=v"(b)); return (u32x4){a.x, a.y, b.x, b.y}; }
DI float vadd(float a, float b) { float r; asm volatile("v_add_f32 %0, %1, %2" : "=v"(r) : "v"(a), "v"(b)); return r; }
DI unsigned vcvt(float a, float b) { unsigned r; asm volatile("v_cvt_pk_bf16_f32 %0, %1, %2" : "=v"(r) : "v"(a), "v"(b)); return r; }
template <int O0, int O1> DI void dsw2(lptr p, unsigned a, unsigned b) { asm volatile("ds_write2_b32 %0, %1, %2 offset0:%3 offset1:%4" :: "v"(p), "v"(a), "v"(b), "i"(O0), "i"(O1) : "memory"); }
#define ST(k)
#define ST2(k)
#define TS(k)
DI int wave_max_rel(unsigned v, unsigned base) {
    int d = (int)(v - base);
    d = max(d, dppi<0xB1>(d)); d = max(d, dppi<0x4E>(d)); d = max(d, dppi<0x124>(d)); d = max(d, dppi<0x128>(d));
    auto s = __builtin_amdgcn_permlane16_swap((unsigned)d, (unsigned)d, false, false); d = max((int)s[0], (int)s[1]);
    auto t = __builtin_amdgcn_permlane32_swap((unsigned)d, (unsigned)d, false, false); d = max((int)t[0], (int)t[1]);
    return __builtin_amdgcn_readfirstlane(d);
}

DI void chain_role(const Params& p, lptr lds, const int b, const int tid) {
    const int w = __builtin_amdgcn_readfirstlane(tid >> 6), lane = tid & 63, n = lane & 15, g = lane >> 4;
    const int h = (b & 3) + 4 * (b >> 3), hf = (b >> 2) & 1;
    u32x8 wq[2][8], wk[2][8], wv[8], wo[8];
    {
        const int q = n >> 2, pp = lane & 3;
        constexpr int WSTR = 208;
        u32x4 v[2][12];
        const rsrc_t rq = mkrs(p.Wq + h * 128 + 32 * w, (511 * HD + 32) * 2), rk = mkrs(p.Wk + h * 128 + 32 * w, (511 * HD + 32) * 2), rv = mkrs(p.Wv + h * 128 + hf * 64 + 32 * (w >> 1), (511 * HD + 32) * 2);
        const unsigned vo = ((lane >> 2) * HD + (lane & 3) * 8) * 2;
        const lptr img = lds + w * 64 * WSTR, wp = img + (lane >> 2) * WSTR + (lane & 3) * 16;
        auto issue = [&](int j, u32x4 (&d)[12]) {
            const unsigned so = ((2 * w + j) & 7) * 64 * HD * 2;
#pragma unroll
            for (int i = 0; i < 4; ++i) { d[i] = ldw(rq, vo, so + i * 16 * HD * 2); d[4 + i] = ldw(rk, vo, so + i * 16 * HD * 2); d[8 + i] = ldw(rv, vo, so + i * 16 * HD * 2); } };
        issue(0, v[0]); issue(1, v[1]);
#pragma unroll
        for (int ks = 0; ks < 8; ++ks) {
#pragma unroll
            for (int i = 0; i < 4; ++i) { ldsw128(wp + i * 16 * WSTR, v[ks & 1][i]); ldsw128(wp + i * 16 * WSTR + 64, v[ks & 1][4 + i]); ldsw128(wp + i * 16 * WSTR + 128, v[ks & 1][8 + i]); }
            if (ks + 2 < 8) issue(ks + 2, v[ks & 1]);
            asm volatile("s_waitcnt lgkmcnt(0)" ::: "memory");
            lptr base = img + (8 * g + q) * WSTR;
            auto frag = [&](int colbyte) { u32x8 f;
#pragma unroll
                for (int m = 0; m < 4; ++m) { const u32x2 t = trread(base + (32 * (m >> 1) + 4 * (m & 1)) * WSTR + colbyte); f[2 * m] = t.x; f[2 * m + 1] = t.y; }
                return f; };
            wq[0][ks] = frag(8 * pp); wq[1][ks] = frag(32 + 8 * pp);
            wk[0][ks] = frag(64 + 8 * pp); wk[1][ks] = frag(96 + 8 * pp);
            wv[ks] = frag(128 + 16 * pp + 8 * (w & 1));
            asm volatile("s_waitcnt lgkmcnt(0)" : "+a"(wq[0][ks]), "+a"(wq[1][ks]), "+a"(wk[0][ks]), "+a"(wk[1][ks]), "+v"(wv[ks]) :: "memory");
        }
        {
            u32x4 u[16];
            const rsrc_t ro = mkrs(p.Wo + (size_t)(h * 128 + hf * 64) * D, 65536);
#pragma unroll
            for (int i = 0; i < 16; ++i) u[i] = ldw(ro, tid * 16, i * 4096);
            bar();
#pragma unroll
            for (int i = 0; i < 16; ++i) ldsw128(lds + ((tid >> 6) + 4 * i) * OSTR + (tid & 63) * 16, u[i]);
            bar();
            lptr base = lds + (32 * (q >> 1) + 4 * g + (q & 1)) * OSTR;
#pragma unroll
            for (int t = 0; t < 8; ++t) {
                const int col = 128 * w + 64 * (t >> 2) + 16 * pp + 4 * (t & 3);
#pragma unroll
                for (int m = 0; m < 4; ++m) { const u32x2 tv = trread(base + (16 * (m >> 1) + 2 * (m & 1)) * OSTR + col * 2); wo[t][2 * m] = tv.x; wo[t][2 * m + 1] = tv.y; }
            }
            bar();
        }
    }
    const rsrc_t rw = mkrs(p.ws, WS_END);
    const int hi = (n >> 1) & 1, rowi = 2 * (n >> 2) + (n & 1);
    const int sidx = hi ? 0xEEEE : 0x4444;
    unsigned cap = 1u << 17;
    const unsigned l16 = lane * 16;
    const unsigned cfg = __builtin_amdgcn_readfirstlane(*(const unsigned*)(p.ws + WS_CFG)), ho = ho_base(p.ws);
    const unsigned ho0 = ho + (cfg & 1) * 8192, ho1 = ho + ((cfg >> 1) & 1) * 8192;
    const unsigned xc = (b & 3) + 4 * ((b >> 2) % (NC / 4));
    auto pblk = [&](unsigned ring_, unsigned r) { return (ring_ == 2 ? ho1 : ho0) + (ring_ * 64 + r) * 16384; };
    const unsigned t0o = tid == 0 ? 0u : OOB;
    auto qkv_issue = [&](int s, u32x4 (&v)[3]) {
        const unsigned base = WS_QKV + (unsigned)(s * 32 + h) * 24576u;
        v[0] = ld16(rw, l16, base + 8192 + w * 2048); v[1] = ld16(rw, l16, base + 8192 + w * 2048 + 1024);
        v[2] = ld16(rw, l16, base + 16384 + hf * 4096 + w * 1024);
    };
    auto qkv_store = [&](int s, u32x4 (&v)[3]) {
        lptr d = lds + L_QKV + (s & 1) * 12288 + lane * 16;
        ldsw128(d + w * 2048, v[0]); ldsw128(d + w * 2048 + 1024, v[1]);
        ldsw64(d + 8192 + (w >> 1) * 2048 + (w & 1) * 8, (u32x2){v[2].x, v[2].y}); ldsw64(d + 8192 + (w >> 1) * 2048 + 1024 + (w & 1) * 8, (u32x2){v[2].z, v[2].w});
    };
    const unsigned qflag = WS_FLAG + (FL_QKV + (h & 7) + ((((lane >> 1) & 1) * 32 + 32 + h) >> 3) * 16 + (lane & 1) * 8) * 128;
    auto qkv_wait = [&](unsigned need) { for (unsigned spins = 0;; ++spins) { asm volatile("" ::: "memory"); if (__all(ld4(rw, qflag) >= need)) break; if (spins > cap) { cap = 0; break; } __builtin_amdgcn_s_sleep(1); } asm volatile("" ::: "memory"); };
    { qkv_wait(2); u32x4 v[3]; qkv_issue(0, v); qkv_store(0, v); }
    const lptr qtw = lds + L_QT + w * 1024 + (n >> 3) * 256 + (((n & 7) * 32) ^ ((n >> 3) * 128)) + g * 8;
    const lptr tr0 = lds + L_QT + g * 256 + (n >> 2) * 32 + (lane & 3) * 8 + (g & 1) * 128, tr1 = tr0 + ((g & 1) ? -128 : 128);
    auto trf = [&](int off) { const u32x2 a = trread(tr0 + off), c = trread(tr1 + off); return (u32x4){a.x, a.y, c.x, c.y}; };
    const lptr mw = lds + L_MEMF + tid * 16 + w * 1024, tsw = lds + L_TS + tid * 4, vtw = lds + L_VT + tid * 4, vtr = lds + L_VT + lane * 4;
    const lptr rdw = lds + L_RED + (w * 64 + 32 * ((n >> 2) & 1) + 16 * ((n >> 1) & 1) + (n & 1) + 4 * (g & 1)) * 4, rdr = lds + L_RED + (lane & 31) * 8;
    const unsigned tso = w == 3 ? l16 : OOB, sco = w == 3 ? (lane & 1) * 4 : OOB, mso = w < 2 ? (2 * w + (lane >> 5)) * 16384 + (lane & 31) * 4 : b == 0 ? NC * 16384 + (w - 2) * 256 + lane * 4 : OOB,
        tvo = lane < 16 ? 8192 + w * 2048 + lane * 128 : lane < 24 ? 16384 + hf * 4096 + w * 1024 + (lane - 16) * 128 : OOB, kso = (hf | hi) ? OOB : (rowi * 4 + g) * 16, pvo = (2 * (n >> 2) + (g >> 1)) * 16384 + ((n & 3) + 4 * (g & 1)) * 16;
    if (w == 1) ldsw128(lds + L_ONES + lane * 16, (u32x4){0x3f803f80u, 0x3f803f80u, 0x3f803f80u, 0x3f803f80u});
    if (w == 0) { const unsigned one = 0x3f80u << (16 * (n & 1)), k2 = (n >> 1) & 3; ldsw128(lds + L_SEL + lane * 16, (u32x4){k2 == 0 ? one : 0u, k2 == 1 ? one : 0u, k2 == 2 ? one : 0u, k2 == 3 ? one : 0u}); }
    const unsigned long long himask = 0xCCCCCCCCCCCCCCCCull;
    bar();
    unsigned T2 = rt(), T1 = 0, ring = 0; bool qrdy = false;
    auto step = [&](const int s, auto last_) {
        constexpr bool LAST = decltype(last_)::value;
        f32x4 aq0, aq1, ak0, ak1, av;
        const unsigned rprev = ring == 0 ? NRING - 1 : ring - 1, rnext = ring == NRING - 1 ? 0 : ring + 1, pprev = pblk(rprev, b) + w * 2048;
        const unsigned mo = ho0 + (HO_MEM + ring * MRS + xc) * 16384 + w * 2048, to = ho0 + (HO_MEM + ring * MRS + NC) * 16384 + xc * 128;
        u32x4 A[8], kx[2][4], qf[4], kf[4]; unsigned tsv = 0;
        wait_rt(T2);
        ST(0);
        A[0] = ld16(rw, l16, mo); A[1] = ld16(rw, l16, mo + 1024); tsv = ld4(rw, sco, to);
        SB();
        SB();
        lptr qb = lds + L_QKV + (s & 1) * 12288 + lane * 16;
        lptr ar0 = lds + L_MEMF + lane * 16 + ((2 * w + 2) & 7) * 1024, ar1 = lds + L_MEMF + lane * 16 + ((2 * w + 4) & 7) * 1024, ar2 = lds + L_MEMF + lane * 16 + ((2 * w + 6) & 7) * 1024;
        asm volatile("" : "+v"(qb), "+v"(ar0), "+v"(ar1), "+v"(ar2));
        if constexpr (!LAST) {
#pragma unroll
            for (int ks = 0; ks < NPRE; ++ks) { kx[0][ks] = ldsr128(qb + ks * 1024); kx[1][ks] = ldsr128(qb + 4096 + ks * 1024); }
        }
        aq0 = zero4(); aq1 = zero4(); ak0 = zero4(); ak1 = zero4(); av = zero4();
        if constexpr (!LAST) {
#pragma unroll
            for (int ks = 0; ks < NPRE; ++ks) asm volatile("" : "+v"(kx[0][ks]), "+v"(kx[1][ks]));
        }
        SB();
        ldsw128(mw, A[0]); ldsw128(mw + 1024, A[1]); ldsw32(tsw, tsv);
        SB();
        if (!__all(mx4(A[0], mx4(A[1], tsv)) != SENT)) {
            for (unsigned spins = 0;; ++spins) {
                asm volatile("" ::: "memory");
                A[0] = ld16(rw, l16, mo); A[1] = ld16(rw, l16, mo + 1024); tsv = ld4(rw, sco, to);
                if (__all(mx4(A[0], mx4(A[1], tsv)) != SENT)) break;
                if (spins > cap) { cap = 0; break; }
            }
            ldsw128(mw, A[0]); ldsw128(mw + 1024, A[1]); ldsw32(tsw, tsv);
        }
        ST(1);
        TS(0);
        NOP2();
        if constexpr (!LAST) { SMFA(aq0, A[0], wq[0][0]); SMFA(aq1, A[0], wq[1][0]); }
        SMFA(ak0, A[0], wk[0][0]);
        bar();
        TS(1);
        A[2] = ldsr128(ar0); A[3] = ldsr128(ar0 + 1024); SB();
        SMFA(ak1, A[0], wk[1][0]);
        A[4] = ldsr128(ar1); A[5] = ldsr128(ar1 + 1024); SB();
        SMFV(av, A[0], wv[0]);
        A[6] = ldsr128(ar2); A[7] = ldsr128(ar2 + 1024); SB();
        u32x2 tm = {0, 0}; unsigned qf_ = 0, touch = 0;
#define POST3(C, acc, K, W, src, dst, WR) { float t0_, t1_; asm volatile( \
                "v_add_f32 %1, %10, %12\n\tv_smfmac_f32_16x16x64_bf16 %0, %3, %6, %9\n\tv_add_f32 %2, %11, %13\n\tv_smfmac_f32_16x16x64_bf16 %0, %4, %7, %9\n\t" \
                "v_cvt_pk_bf16_f32 %1, %1, %2\n\tv_smfmac_f32_16x16x64_bf16 %0, %5, %8, %9\n\tds_write2_b32 %14, %1, %1 " WR \
                : "+v"(acc), "=&v"(t0_), "=&v"(t1_) : "v"(A[K]), "v"(A[K + 1]), "v"(A[K + 2]), C(W[K]), C(W[K + 1]), C(W[K + 2]), "v"(sidx), \
                  "v"(src[0]), "v"(src[1]), "v"(src[2]), "v"(src[3]), "v"(dst) : "memory"); }
        if constexpr (!LAST) {
            SMFA(aq0, A[1], wq[0][1]);
            SMFA(aq1, A[1], wq[1][1]);
            tm = *(LAS u32x2*)(lds + L_TS + 768); SB();
            SMFA(ak0, A[1], wk[0][1]);
            SMFA(ak1, A[1], wk[1][1]); SMFV(av, A[1], wv[1]);
            TS(2);
            const u32x4 z = sent4();
            SMFA(aq0, A[2], wq[0][2]);
            st16(rw, l16, z, pprev); SB();
            SMFA(aq0, A[3], wq[0][3]);
            st4(rw, mso, z.x, ho0 + (HO_MEM + rprev * MRS) * 16384 + (w < 2 ? b * 128 : 0)); SB();
            SMFA(aq0, A[4], wq[0][4]);
            st16(rw, l16, z, pprev + 1024); SB();
            SMFA(aq0, A[5], wq[0][5]);
            qf_ = ld4(rw, qflag); SB();
            SMFA(aq0, A[6], wq[0][6]);
            touch = ld4(rw, tvo, WS_QKV + (unsigned)(min(s + 2, NSEG - 1) * 32 + h) * 24576u); SB();
            SMFA(aq0, A[7], wq[0][7]);
            { unsigned sv = s; asm volatile("" : "+v"(sv)); st4(rw, t0o, sv, WS_FLAG + (FL_KM + b) * 128); } SB();
            u32x4 vx[4];
            SMFA(aq1, A[2], wq[1][2]);
            vx[0] = ldsr128(qb + 8192); SB();
            SMFA(aq1, A[3], wq[1][3]);
            vx[2] = ldsr128(qb + 8192 + 2048); SB();
            SMFA(aq1, A[4], wq[1][4]);
            POST3("a", aq1, 5, wq[1], aq0, qtw, "offset0:0 offset1:1");
            TS(3);
            SMFA(ak0, A[2], wk[0][2]);
            SMFA(ak0, A[3], wk[0][3]);
            const u32x4 ones = ldsr128(lds + L_ONES + lane * 16); SB();
            SMFA(ak0, A[4], wk[0][4]);
            vx[1] = ldsr128(qb + 8192 + 1024); SB();
            POST3("a", ak0, 5, wk[0], aq1, qtw, "offset0:128 offset1:129");
            SMFA(ak1, A[2], wk[1][2]);
            SMFA(ak1, A[3], wk[1][3]);
            vx[3] = ldsr128(qb + 8192 + 3072); SB();
            SMFA(ak1, A[4], wk[1][4]);
            {
                float t0_, t1_;
                asm volatile("v_add_f32 %1, %10, %12\n\tv_smfmac_f32_16x16x64_bf16 %0, %3, %6, %9\n\tv_add_f32 %2, %11, %13\n\tv_smfmac_f32_16x16x64_bf16 %0, %4, %7, %9\n\t"
                             "v_cvt_pk_bf16_f32 %1, %1, %2\n\ts_waitcnt lgkmcnt(0)\n\ts_barrier\n\tv_smfmac_f32_16x16x64_bf16 %0, %5, %8, %9\n\tds_write2_b32 %14, %1, %1 offset0:0 offset1:1"
                             : "+v"(ak1), "=&v"(t0_), "=&v"(t1_) : "v"(A[5]), "v"(A[6]), "v"(A[7]), "a"(wk[1][5]), "a"(wk[1][6]), "a"(wk[1][7]), "v"(sidx),
                               "v"(ak0[0]), "v"(ak0[1]), "v"(ak0[2]), "v"(ak0[3]), "v"(qtw + 4096) : "memory");
            }
            TS(4);
            qf[0] = trf(0); SB();
            SMFV(av, A[2], wv[2]);
            qf[1] = trf(1024); SB();
            SMFV(av, A[3], wv[3]);
            qf[2] = trf(2048); SB();
            SMFV(av, A[4], wv[4]);
            qf[3] = trf(3072); SB();
            POST3("v", av, 5, wv, ak1, qtw + 4096, "offset0:128 offset1:129");
            TS(5);
            f32x4 s0, s1, s2, c[8];
            MF0(s1, kx[0][0], qf[0]);
            MF0(s2, kx[1][0], qf[0]);
            MF(s1, kx[0][1], qf[1]);
            {
                float t0_, t1_;
                asm volatile("v_add_f32 %2, %8, %10\n\tv_add_f32 %3, %9, %11\n\tv_mfma_f32_16x16x32_bf16 %1, %4, %6, %1\n\tv_cvt_pk_bf16_f32 %2, %2, %3\n\t"
                             "v_mfma_f32_16x16x32_bf16 %0, %5, %7, %0\n\ts_waitcnt lgkmcnt(0)\n\ts_barrier\n\tds_write_b32 %12, %2"
                             : "+v"(s1), "+v"(s2), "=&v"(t0_), "=&v"(t1_) : "v"(kx[1][1]), "v"(kx[0][2]), "v"(qf[1]), "v"(qf[2]), "v"(av[0]), "v"(av[1]), "v"(av[2]), "v"(av[3]), "v"(vtw) : "memory");
            }
            TS(6);
            kf[0] = trf(4096); SB();
            MF(s2, kx[1][2], qf[2]);
            kf[1] = trf(4096 + 1024); SB();
            MF(s1, kx[0][3], qf[3]);
            kf[2] = trf(4096 + 2048); SB();
            MF(s2, kx[1][3], qf[3]);
            kf[3] = trf(4096 + 3072);
            SB();
            ST(2);
            TS(7);
            float mx;
            {
                float t_;
                MF0(s0, kf[0], qf[0]);
                asm volatile("v_mfma_f32_16x16x32_bf16 %1, %2, %3, %1\n\tv_max3_f32 %0, %4, %5, %6" : "=&v"(mx), "+v"(s0) : "v"(kf[1]), "v"(qf[1]), "v"(s1[0]), "v"(s1[1]), "v"(s1[2]));
                asm volatile("v_mfma_f32_16x16x32_bf16 %2, %3, %4, %2\n\tv_max3_f32 %1, %5, %6, %7\n\tv_max3_f32 %0, %0, %8, %9"
                             : "+v"(mx), "=&v"(t_), "+v"(s0) : "v"(kf[2]), "v"(qf[2]), "v"(s1[3]), "v"(s2[0]), "v"(s2[1]), "v"(s2[2]), "v"(s2[3]));
                asm volatile("v_mfma_f32_16x16x32_bf16 %2, %3, %4, %2\n\tv_max_f32 %0, %0, %1\n\tv_mov_b32 %1, %0\n\t"
                             "s_waitcnt lgkmcnt(0)\n\ts_barrier\n\tv_permlane16_swap_b32 %0, %1\n\tv_max_f32 %0, %0, %1\n\t"
                             "v_mov_b32 %1, %0\n\ts_nop 1\n\tv_permlane32_swap_b32 %0, %1\n\tv_max_f32 %0, %0, %1\n\tv_mul_f32 %0, 0xbe0293ee, %0"
                             : "+v"(mx), "+v"(t_), "+v"(s0) : "v"(kf[3]), "v"(qf[3]) : "memory");
            }
            ST2(12);
            TS(8);
            u32x4 px, pm;
            float e0, e1, em;
            {
                float a_, b_, c_;
#define FE(d, src) "v_fmamk_f32 " d ", " src ", 0x3e0293ee, %20\n\t"
                asm volatile(FE("%7", "%10") FE("%8", "%11") "v_exp_f32 %7, %7\n\t" FE("%9", "%12") "v_exp_f32 %8, %8\n\t" FE("%1", "%13") "v_exp_f32 %9, %9\n\tv_cvt_pk_bf16_f32 %0, %7, %8\n\t"
                             "v_exp_f32 %1, %1\n\t" FE("%7", "%14") FE("%8", "%15") "v_exp_f32 %7, %7\n\tv_cvt_pk_bf16_f32 %1, %9, %1\n\tv_exp_f32 %8, %8\n\t" FE("%9", "%16") FE("%3", "%17")
                             "v_exp_f32 %9, %9\n\tv_cvt_pk_bf16_f32 %2, %7, %8\n\tv_exp_f32 %3, %3\n\t" FE("%4", "%18") FE("%5", "%19") "v_cvt_pk_bf16_f32 %3, %9, %3\n\tv_max_f32 %6, %4, %5"
                             : "=&v"(px.x), "=&v"(px.y), "=&v"(px.z), "=&v"(px.w), "=&v"(e0), "=&v"(e1), "=&v"(em), "=&v"(a_), "=&v"(b_), "=&v"(c_)
                             : "v"(s1[0]), "v"(s1[1]), "v"(s1[2]), "v"(s1[3]), "v"(s2[0]), "v"(s2[1]), "v"(s2[2]), "v"(s2[3]), "v"(s0[0]), "v"(s0[1]), "v"(mx), "v"(s0));
            }
            const bool slow = __any(em > 0.f);
            SB();
            TS(9);
            f32x4 o[4], ls;
            MF0(o[0], vx[0], px);
            vx[0].x = *(LAS unsigned*)vtr;
            pm = __builtin_bit_cast(u32x4, zero4());
            MF0(o[2], vx[2], px);
            vx[2].x = *(LAS unsigned*)(vtr + 512);
            float up = 0.f;
            if (slow) {
                up = xmax(vmax(em, 0.f)); e0 -= up; e1 -= up;
            }
            SB();
            MF0(o[1], vx[1], px);
            vx[1].x = *(LAS unsigned*)(vtr + 256); SB();
            asm volatile("v_exp_f32 %1, %1\n\tv_mfma_f32_16x16x32_bf16 %0, %2, %3, 0" : "=&v"(o[3]), "+v"(e0) : "v"(vx[3]), "v"(px));
            vx[3].x = *(LAS unsigned*)(vtr + 768); SB();
            asm volatile("v_exp_f32 %2, %2\n\tv_mfma_f32_16x16x32_bf16 %0, %4, %5, 0\n\tv_cvt_pk_bf16_f32 %1, %3, %2" : "=&v"(ls), "=&v"(pm.x), "+v"(e1) : "v"(e0), "v"(ones), "v"(px));
            SB();
            if (slow) {
                asm volatile("s_nop 7" : "+v"(o[0]), "+v"(o[1]), "+v"(o[2]), "+v"(o[3]), "+v"(ls));
                const float al = __builtin_amdgcn_exp2f(-up);
                o[0] *= al; o[1] *= al; o[2] *= al; o[3] *= al; ls *= al;
            }
            c[0] = zero4();
            asm volatile("" : "+v"(pm), "+v"(o[0]), "+v"(o[1]), "+v"(o[2]), "+v"(o[3]), "+v"(ls));
            TS(10);
            MF(ls, ones, pm);
            c[1] = zero4();
            MF(o[0], vx[0], pm);
            c[2] = zero4();
            MF(o[2], vx[2], pm);
            c[3] = zero4();
            MF(o[1], vx[1], pm);
            c[4] = zero4();
            u32x4 o8;
            {
                float inv; f32x2 il, ih, t0_, t1_;
                asm volatile("v_mfma_f32_16x16x32_bf16 %0, %3, %4, %0\n\tv_rcp_f32 %1, %5\n\ts_nop 0\n\tv_cndmask_b32_e64 %2, 0, %1, %6\n\tv_cndmask_b32_e64 %1, %1, 0, %6"
                             : "+v"(o[3]), "=&v"(il.x), "=&v"(ih.x) : "v"(vx[3]), "v"(pm), "v"(ls[0]), "s"(himask), "v"(ls));
                (void)inv; (void)t0_; (void)t1_;
                TS(11);
                c[5] = zero4();
                f32x4 oa = o[0] * il.x + o[2] * ih.x;
                o8.x = pk(oa[0], oa[1]); o8.y = pk(oa[2], oa[3]);
                asm volatile("" : "+v"(o8.x), "+v"(o8.y), "+v"(o[1]), "+v"(o[3]));
                const f32x4 ob = o[1] * il.x + o[3] * ih.x;
                o8.z = pk(ob[0], ob[1]); o8.w = pk(ob[2], ob[3]);
            }
            ST2(13);
            asm volatile("s_nop 1" : "+v"(o8));
            TS(12);
            SB();
            SMFV(c[0], o8, wo[0]); c[6] = zero4(); SMFV(c[1], o8, wo[1]); c[7] = zero4(); SMFV(c[2], o8, wo[2]); SMFV(c[3], o8, wo[3]);
            const unsigned slot = pblk(ring, 16 * w) + b * 128;
            u32x4 pa;
            {
                float t_;
                asm volatile("v_add_f32 %4, %15, %17\n\tv_smfmac_f32_16x16x64_bf16 %0, %9, %10, %14\n\tv_add_f32 %8, %16, %18\n\tv_smfmac_f32_16x16x64_bf16 %1, %9, %11, %14\n\t"
                             "v_cvt_pk_bf16_f32 %4, %4, %8\n\tv_smfmac_f32_16x16x64_bf16 %2, %9, %12, %14\n\tv_add_f32 %5, %19, %21\n\tv_smfmac_f32_16x16x64_bf16 %3, %9, %13, %14\n\t"
                             "v_add_f32 %8, %20, %22\n\tv_cvt_pk_bf16_f32 %5, %5, %8\n\tv_add_f32 %6, %23, %25\n\tv_add_f32 %8, %24, %26\n\tv_cvt_pk_bf16_f32 %6, %6, %8"
                             : "+v"(c[4]), "+v"(c[5]), "+v"(c[6]), "+v"(c[7]), "=&v"(pa.x), "=&v"(pa.y), "=&v"(pa.z), "=&v"(pa.w), "=&v"(t_)
                             : "v"(o8), "v"(wo[4]), "v"(wo[5]), "v"(wo[6]), "v"(wo[7]), "v"(sidx),
                               "v"(c[0][0]), "v"(c[0][1]), "v"(c[0][2]), "v"(c[0][3]), "v"(c[1][0]), "v"(c[1][1]), "v"(c[1][2]), "v"(c[1][3]), "v"(c[2][0]), "v"(c[2][1]), "v"(c[2][2]), "v"(c[2][3]));
            }
            auto fold = [](f32x4 v) { const f32x2 t = (f32x2){v[0], v[1]} + (f32x2){v[2], v[3]}; return pk(t.x, t.y); };
            pa.w = fold(c[3]);
            st16(rw, pvo, pa, slot);
            TS(13);
            SB();
            st16(rw, pvo, (u32x4){fold(c[4]), fold(c[5]), fold(c[6]), fold(c[7])}, slot + 131072);
            TS(14);
            SB();
            const unsigned now = rt();
            qrdy = __all(qf_ >= (unsigned)min(s + 3, NSEG));
            ST(3);
            T1 = __builtin_amdgcn_readfirstlane(tm.x); T2 = __builtin_amdgcn_readfirstlane(tm.y);
            if (s == 0 || (int)(now + TV1 - T1) > 0) T1 = now + TV1 + TM;
        } else {
#pragma unroll
            for (int ks = 1; ks < 8; ++ks) { SMFA(ak0, A[ks], wk[0][ks]); SMFA(ak1, A[ks], wk[1][ks]); SMFV(av, A[ks], wv[ks]); }
            asm volatile("s_nop 7" : "+v"(av), "+v"(ak0), "+v"(ak1));
            unsigned d0 = pk(ak0[0] + ak0[2], ak0[1] + ak0[3]); dsw2<0, 1>(qtw + 4096, d0, d0);
            d0 = pk(ak1[0] + ak1[2], ak1[1] + ak1[3]); dsw2<128, 129>(qtw + 4096, d0, d0);
            ldsw32(vtw, pk(av[0] + av[2], av[1] + av[3]));
            bar();
        }
        {
            const u32x4 kv = trf(4096 + w * 1024);
            const u32x4 vv = {*(LAS unsigned*)vtr, *(LAS unsigned*)(vtr + 256), *(LAS unsigned*)(vtr + 512), *(LAS unsigned*)(vtr + 768)};
            const unsigned kb = kvm_off(ho, cfg, s * 32 + h);
            st16(rw, kso, kv, kb + w * 512); st16(rw, tso, vv, kb + 2048 + hf * 1024);
        }
        if constexpr (LAST) return; else {
        ST(4);
        u32x4 p0, p1, nv[3];
        const unsigned pb = pblk(ring, b) + w * 2048;
        if (!qrdy) qkv_wait(min(s + 3, NSEG));
        qkv_issue(s + 1, nv);
        wait_rt(T1);
        ST(5);
        p0 = ld16(rw, l16, pb); p1 = ld16(rw, l16, pb + 1024);
        SB();
        qkv_store(s + 1, nv);
        SB();
        u32x4 sel = ldsr128(lds + L_SEL + lane * 16);
        asm volatile("" : "+v"(sel));
        SB();
        f32x4 rr;
        MF0(rr, sel, p0); MF(rr, sel, p1);
        if (!__all(mx4(p0, mx4(p1, 0u)) != SENT)) {
            for (unsigned spins = 0;; ++spins) {
                asm volatile("" ::: "memory");
                p0 = ld16(rw, l16, pb); p1 = ld16(rw, l16, pb + 1024);
                if (__all(mx4(p0, mx4(p1, 0u)) != SENT)) break;
                if (spins > cap) { cap = 0; break; }
            }
            MF0(rr, sel, p0); MF(rr, sel, p1);
            asm volatile("s_nop 7" : "+v"(rr));
        }
        ST(6);
        {
            asm volatile("" : "+v"(rr));
            unsigned q0, q1, q2, q3;
            asm volatile("v_add_f32_dpp %0, %4, %4 row_ror:8 row_mask:0xf bank_mask:0xf\n\tv_add_f32_dpp %1, %5, %5 row_ror:8 row_mask:0xf bank_mask:0xf\n\t"
                         "v_add_f32_dpp %2, %6, %6 row_ror:8 row_mask:0xf bank_mask:0xf\n\tv_add_f32_dpp %3, %7, %7 row_ror:8 row_mask:0xf bank_mask:0xf"
                         : "=&v"(q0), "=&v"(q1), "=&v"(q2), "=&v"(q3) : "v"(rr[0]), "v"(rr[1]), "v"(rr[2]), "v"(rr[3]));
            dsw2<0, 8>(rdw, q0, q1); dsw2<2, 10>(rdw, q2, q3);
        }
        bar();
        {
            f32x2 a0, a1, a2, a3;
            asm volatile("ds_read_b64 %0, %4\n\tds_read_b64 %1, %4 offset:256\n\tds_read_b64 %2, %4 offset:512\n\tds_read_b64 %3, %4 offset:768\n\ts_waitcnt lgkmcnt(0)"
                         : "=&v"(a0), "=&v"(a1), "=&v"(a2), "=&v"(a3) : "v"(rdr) : "memory");
            const f32x2 t = (a0 + a1) + (a2 + a3);
            unsigned mv = pk(t.x, t.y);
            unsigned now = 0; bool late = false;
            if (w >= 2) {
                now = rt(); late = s == 0 || (int)(now + TV2 - T2) > 0;
                const unsigned n1 = min((late ? now + TV2 + TPEN : T2) + TX, SENT - 1 - TY);
                mv = (lane & 1) ? n1 + TY : n1;
            }
            st4(rw, mso, mv, ho0 + (HO_MEM + rnext * MRS) * 16384 + (w < 2 ? b * 128 : 0));
            if (w < 2) { now = rt(); late = s == 0 || (int)(now + TV2 - T2) > 0; }
            if (late) T2 = now + TV2 + TM;
        }
        ST(7);
        ring = rnext;
        asm volatile("" :: "v"(touch));
        ST(8);
        }
    };
    for (int s = 0; s < NSEG - 1; ++s) step(s, bool_c<false>{});
    step(NSEG - 1, bool_c<true>{});
    drain(); bar();
    if (tid == 0) st4(rw, WS_FLAG + (FL_KM + b) * 128, NSEG);
}

template <int ROLE>
DI void stream_role(const Params& p, lptr lds, const int j, const int tid) {
    const int w = __builtin_amdgcn_readfirstlane(tid >> 6), lane = tid & 63, n = lane & 15, g = lane >> 4, pp = lane & 3;
    const rsrc_t rw = mkrs(p.ws, WS_END);
    const float* mod = (const float*)(p.ws + WS_MOD);
    unsigned cap = 1u << 17;
    const unsigned l16 = lane * 16, t0o = tid == 64 ? 0u : OOB;
    const int cgp = (j & 7) + 8 * (j >> 4), qtt = (j >> 3) & 1, qm = cgp >> 5, qh = cgp & 31;
    if (j < 128) {
        u32x4 xv[4];
#pragma unroll
        for (int r = 0; r < 4; ++r) xv[r] = *(const u32x4*)(p.x + (size_t)(j * 16 + w * 4 + r) * D + lane * 8);
        float t[8], sh[8];
#pragma unroll
        for (int i = 0; i < 8; ++i) { t[i] = rb(1.f + mod[512 + lane * 8 + i]); sh[i] = mod[lane * 8 + i]; }
#pragma unroll
        for (int r = 0; r < 4; ++r) {
            float v[8] = {bflo(xv[r].x), bfhi(xv[r].x), bflo(xv[r].y), bfhi(xv[r].y), bflo(xv[r].z), bfhi(xv[r].z), bflo(xv[r].w), bfhi(xv[r].w)};
            float sum = ((v[0] + v[1]) + (v[2] + v[3])) + ((v[4] + v[5]) + (v[6] + v[7]));
            sum += dpp<0xB1>(sum); sum += dpp<0x4E>(sum); sum += dpp<0x124>(sum); sum += dpp<0x128>(sum); sum = xsum(sum);
            const float mean = sum * (1.f / D); float var = 0.f;
#pragma unroll
            for (int i = 0; i < 8; ++i) { const float d = v[i] - mean; var += d * d; }
            var += dpp<0xB1>(var); var += dpp<0x4E>(var); var += dpp<0x124>(var); var += dpp<0x128>(var); var = xsum(var);
            const float mb = rb(mean), sd = rb(sqrtf(rb(rb(var * (1.f / D)) + 1e-5f)));
#pragma unroll
            for (int i = 0; i < 8; ++i) v[i] = lnmod(v[i], mb, sd, t[i], sh[i]);
            st16(rw, WS_H1 + (unsigned)(j * 16 + (lane >> 2)) * 1024u + ((w * 4 + r) * 4 + (lane & 3)) * 16, (u32x4){pk(v[0], v[1]), pk(v[2], v[3]), pk(v[4], v[5]), pk(v[6], v[7])});
        }
        drain(); bar();
        if (tid == 0) st4(rw, WS_FLAG + (FL_LN + j) * 128, 1);
    }
    u32x4 qa[16], qb[16];
    { const u16* W = (qm == 0 ? p.Wq : qm == 1 ? p.Wk : p.Wv) + qh * 128; panel(lds, W, W + 64, HD, tid, 32 * w + 8 * pp, 32 * w + 8 * pp + 4, qa, qb); }
    if (w == 0) wait_flags(rw, FL_LN + qtt, 64, 2, 1, lane, cap);
    bar();
    u32x4 hv[4];
    const unsigned h1off = WS_H1 + (unsigned)(qtt * 16 + w * 4) * 1024u;
    auto h1_issue = [&](int s) {
#pragma unroll
        for (int i = 0; i < 4; ++i) hv[i] = ld16(rw, (n * 4 + g) * 16, h1off + (unsigned)s * 32768u + i * 1024); };
    auto qkv_in = [&](int s) {
#pragma unroll
        for (int i = 0; i < 4; ++i) ldsw128(lds + L_Q + (w * 4 + i) * 1024 + lane * 16, hv[i]);
        h1_issue(min(s + 1, NSEG - 1));
    };
    auto qkv_mm = [&](int s) {
        f32x4 ca, cb;
        if (qm < 2) tile_mma<false>(lds + L_Q, lane, qa, qb, ca, cb); else tile_mma<true>(lds + L_Q, lane, qa, qb, ca, cb);
        const unsigned blk = qm < 2 ? qm * 8 + qtt * 4 + w : 16 + w * 2 + qtt;
        st16(rw, l16, (u32x4){pk(ca[0], ca[1]), pk(ca[2], ca[3]), pk(cb[0], cb[1]), pk(cb[2], cb[3])}, WS_QKV + (unsigned)(s * 32 + qh) * 24576u + blk * 1024);
    };
    auto qkv_seg = [&](int s) { qkv_in(s); bar(); qkv_mm(s); };
    auto publish = [&](unsigned line, unsigned v) { drain(); bar(); st4(rw, t0o, v, WS_FLAG + line * 128); };
    h1_issue(0);
    qkv_seg(0); publish(FL_QKV + j, 1);
    qkv_seg(1); publish(FL_QKV + j, 2);
    u32x4 sa[16], sb[16];
    const int r = ROLE == 0 ? j : ROLE == 1 ? j - 64 : ROLE == 2 ? j - 120 : ROLE == 3 ? j - 152 : j - 168;
    const int tt = ROLE == 3 ? r >> 3 : ROLE == 1 ? r & 1 : (r >> 3) & 1;
    const int cgx = ROLE == 1 ? r >> 1 : (r & 7) + 8 * (r >> 4);
    if (ROLE == 0) { const u16* W = p.Wo + (size_t)(r & 7) * 512 * D + (r >> 4) * 128; panel(lds, W, W + 64, D, tid, 32 * w + 4 * pp, 32 * w + 16 + 4 * pp, sa, sb); }
    if (ROLE == 1) panel(lds, p.wg + cgx * 64, p.wu + cgx * 64, F, tid, 16 * w + 4 * pp, 64 + 16 * w + 4 * pp, sa, sb);
    if (ROLE == 2) {
        const rsrc_t rd = mkrs(p.wd + cgx * 32, (1791 * D + 32) * 2);
#pragma unroll
        for (int half = 0; half < 2; ++half) {
            u32x4 v[14];
#pragma unroll
            for (int i = 0; i < 14; ++i) v[i] = ldw(rd, ((tid >> 2) * D + (tid & 3) * 8) * 2, (half * 896 + i * 64) * D * 2);
            bar();
#pragma unroll
            for (int i = 0; i < 14; ++i) ldsw128(lds + ((tid >> 2) + 64 * i) * 80 + (tid & 3) * 16, v[i]);
            bar();
            if ((w >> 1) == half) {
                lptr pa = lds + ((w & 1) * 448 + 8 * g + (n >> 2)) * 80 + pp * 8;
#pragma unroll
                for (int ks = 0; ks < 14; ++ks) { sa[ks] = tr2(pa + ks * 32 * 80, 4 * 80); sb[ks] = tr2(pa + ks * 32 * 80 + 32, 4 * 80); }
            }
        }
        bar();
    }
    for (int s = 2; s < LEAD; s += 2) { qkv_seg(s); bar(); qkv_seg(s + 1); publish(FL_QKV + j, s + 2); }
    unsigned poff; unsigned ahead = 0;
    if (ROLE == 0) poff = FL_A + (r & 7) * 2 + (lane & 1);
    if (ROLE == 1) poff = FL_L + tt * 8 + (lane & 7);
    if (ROLE == 2) poff = FL_C + tt + 2 * (lane < 28 ? lane : 0);
    if (ROLE == 3) poff = FL_B + (lane & 7) + tt * 8 + ((lane >> 3) & 3) * 16;
    if (ROLE == 4) poff = FL_KM + (j & 63);
    if (ROLE == 5) {
        const int hh = 2 * r + ((lane >> 1) & 1), cg3 = ((lane >> 2) & 3) * 32 - 32 + hh;
        poff = (lane & 12) == 0 ? FL_KM + (hh & 3) + 8 * (hh >> 2) + 4 * (lane & 1) : FL_QKV + (cg3 & 7) + 16 * (cg3 >> 3) + 8 * (lane & 1);
        ahead = (lane & 12) != 0;
    }
    poff = WS_FLAG + poff * 128;
    unsigned seen = 0;
    auto wait_in = [&](unsigned need) {
        if (w == 0) {
            const unsigned nd = min(need + ahead, (unsigned)NSEG);
            for (unsigned spins = 0; !__all(seen >= nd); ++spins) {
                if (spins > cap) { cap = 0; break; }
                if (spins) __builtin_amdgcn_s_sleep(2);
                asm volatile("" ::: "memory");
                seen = ld4(rw, poff);
            }
        }
        bar();
    };
    const unsigned pof0 = w == 0 ? poff : OOB;
    const int hh = 2 * r + (w >> 1), att = w & 1;
    u32x4 xq[4], ka[4], kb[4], vb[8];
    const unsigned l16b = att ? l16 : OOB;
    auto a_issue_qk = [&](int s) {
        const unsigned qb_ = WS_QKV + (unsigned)(s * 32 + hh) * 24576u;
#pragma unroll
        for (int ks = 0; ks < 4; ++ks) { xq[ks] = ld16(rw, l16, qb_ + att * 4096 + ks * 1024); ka[ks] = ld16(rw, l16, qb_ + 8192 + ks * 1024); kb[ks] = ld16(rw, l16b, qb_ + 12288 + ks * 1024); }
    };
    auto a_issue_v = [&](int s) {
        const unsigned qb_ = WS_QKV + (unsigned)(s * 32 + hh) * 24576u;
#pragma unroll
        for (int i = 0; i < 8; ++i) vb[i] = ld16(rw, (i & 1) ? l16b : l16, qb_ + 16384 + i * 1024);
    };
    const unsigned cfg = __builtin_amdgcn_readfirstlane(*(const unsigned*)(p.ws + WS_CFG));
    if (ROLE == 5) { wait_in(0); a_issue_qk(0); a_issue_v(0); }
    const int d0 = 128 * w + 16 * (lane >> 3) + 4 * (lane & 3);
    f32x4 cga, cgb, cgc;
    if (ROLE == 3) { cga = *(const f32x4*)(mod + 1024 + d0); cgb = *(const f32x4*)(mod + 1536 + d0); cgc = *(const f32x4*)(mod + 2048 + d0);
#pragma unroll
        for (int i = 0; i < 4; ++i) cgc[i] = rb(1.f + cgc[i]); }
    if (ROLE == 2) cga = *(const f32x4*)(mod + 2560 + cgx * 32 + (w & 1) * 16 + 4 * g);
    qkv_in(LEAD);
    auto iter = [&](const int s, auto doq_) {
        constexpr bool doq = decltype(doq_)::value;
        u32x2 xv = {0, 0};
        wait_in(s + 1);
        u32x4 a[14];
        if (ROLE == 0) {
#pragma unroll
            for (int i = 0; i < 4; ++i) a[i] = ld16(rw, l16, WS_O + (unsigned)((s * 2 + tt) * 128 + (r & 7) * 16 + w * 4 + i) * 1024u);
        }
        if (ROLE == 1) {
#pragma unroll
            for (int i = 0; i < 4; ++i) a[i] = ld16(rw, (n * 4 + g) * 16, WS_H2 + (unsigned)((s * 2 + tt) * 16 + w * 4 + i) * 1024u);
        }
        if (ROLE == 2) {
#pragma unroll
            for (int ks = 0; ks < 14; ++ks) a[ks] = ld16(rw, (g >> 1) * 512 + n * 32 + (g & 1) * 16, WS_G + (unsigned)((s * 2 + tt) * 56 + w * 14 + ks) * 1024u);
            xv = ld8(rw, WS_X1 + ((s * 32 + tt * 16 + n) * D + cgx * 32 + (w & 1) * 16 + 4 * g) * 2);
        }
        if (ROLE == 3) {
#pragma unroll
            for (int kg = 0; kg < 8; ++kg) a[kg] = ld16(rw, (lane >> 3) * 2048 + (lane & 7) * 16, WS_PB + (unsigned)(((s * 8 + kg) * 32 + 8 * w) * 2 + tt) * 1024u + (r & 7) * 128);
            xv = *(const u32x2*)(p.x + (size_t)(s * 32 + tt * 16 + 2 * (r & 7) + ((lane >> 2) & 1)) * D + d0);
        }
        if (ROLE == 5) {
            const unsigned kb_ = kvm_off(ho_base(p.ws), cfg, s * 32 + hh);
#pragma unroll
            for (int ks = 0; ks < 4; ++ks) a[ks] = ld16(rw, ((2 * (n >> 2) + (n & 1)) * 4 + g) * 16, kb_ + ks * 512);
            a[4] = ld16(rw, l16, kb_ + 2048); a[5] = ld16(rw, l16, kb_ + 3072);
        }
        SB();
        if (doq) qkv_mm(s + LEAD);
        SB();
        if (ROLE == 0 || ROLE == 1) {
#pragma unroll
            for (int i = 0; i < 4; ++i) ldsw128(lds + L_A + (w * 4 + i) * 1024 + lane * 16, a[i]);
            bar();
            f32x4 ca, cb; tile_mma<false>(lds + L_A, lane, sa, sb, ca, cb);
            if (ROLE == 0) {
                const unsigned o = WS_PB + (unsigned)(((s * 8 + (r & 7)) * 32 + (r >> 4) * 8 + w * 2) * 2 + tt) * 1024u;
                st16(rw, (n * 4 + g) * 16, __builtin_bit_cast(u32x4, ca), o); st16(rw, (n * 4 + g) * 16, __builtin_bit_cast(u32x4, cb), o + 2048);
            } else {
                float q[4];
#pragma unroll
                for (int i = 0; i < 4; ++i) { const float gv = rb(ca[i]); q[i] = rb(gv / (1.f + __expf(-gv))) * rb(cb[i]); }
                st8(rw, WS_G + (unsigned)((s * 2 + tt) * 56 + cgx * 2 + (w >> 1)) * 1024u + (w & 1) * 512 + n * 32 + g * 8, (u32x2){pk(q[0], q[1]), pk(q[2], q[3])});
            }
        }
        if (ROLE == 2) {
            f32x4 c0 = {0, 0, 0, 0}, c1 = c0;
            asm volatile("s_nop 1" : "+v"(c0), "+v"(c1));
#pragma unroll
            for (int ks = 0; ks < 14; ++ks) { MFWA(c0, sa[ks], a[ks]); MFWA(c1, sb[ks], a[ks]); }
            asm volatile("s_nop 7" : "+v"(c0), "+v"(c1));
            *(LAS f32x4*)(lds + L_A + (w * 2) * 1024 + lane * 16) = c0; *(LAS f32x4*)(lds + L_A + (w * 2 + 1) * 1024 + lane * 16) = c1;
            bar();
            if (w < 2) {
                lptr q = lds + L_A + w * 1024 + lane * 16;
                const f32x4 y = (*(LAS f32x4*)q + *(LAS f32x4*)(q + 2048)) + (*(LAS f32x4*)(q + 4096) + *(LAS f32x4*)(q + 6144));
                const float o0 = bflo(xv.x) + rb(cga[0] * rb(y[0])), o1 = bfhi(xv.x) + rb(cga[1] * rb(y[1])), o2 = bflo(xv.y) + rb(cga[2] * rb(y[2])), o3 = bfhi(xv.y) + rb(cga[3] * rb(y[3]));
                *(u32x2*)(p.out + (size_t)(s * 32 + tt * 16 + n) * D + cgx * 32 + (w & 1) * 16 + 4 * g) = (u32x2){pk(o0, o1), pk(o2, o3)};
            }
        }
        if (ROLE == 5) {
            f32x4 x0, x1, x2;
            MF0(x1, ka[0], xq[0]); MF0(x2, kb[0], xq[0]);
#pragma unroll
            for (int ks = 1; ks < 4; ++ks) { MF(x1, ka[ks], xq[ks]); MF(x2, kb[ks], xq[ks]); }
            MF0(x0, a[0], xq[0]); MF(x0, a[1], xq[1]); MF(x0, a[2], xq[2]); MF(x0, a[3], xq[3]);
            a_issue_qk(min(s + 1, NSEG - 1));
            asm volatile("s_nop 7" : "+v"(x0), "+v"(x1), "+v"(x2));
            SB();
            const float raw[10] = {x1[0], x1[1], x1[2], x1[3], x2[0], x2[1], x2[2], x2[3], x0[0], x0[1]};
            float sc[10];
            const int qi = 16 * att + n - 4 * g;
#pragma unroll
            for (int i = 0; i < 5; ++i) {
                const unsigned u = vcvt(raw[2 * i], raw[2 * i + 1]);
                const unsigned v = pk(bflo(u) * 0.08838834764831845f, bfhi(u) * 0.08838834764831845f);
                sc[2 * i] = bflo(v); sc[2 * i + 1] = bfhi(v);
            }
#pragma unroll
            for (int i = 0; i < 8; ++i) sc[i] = ((i & 3) + 16 * (i >> 2) <= qi) ? sc[i] : -1e9f;
            float mx = vmax3(vmax3(vmax3(sc[0], sc[1], sc[2]), vmax3(sc[3], sc[4], sc[5]), vmax(sc[6], sc[7])), sc[8], sc[9]);
            mx = xmax(mx) * -1.4426950408889634f;
#pragma unroll
            for (int i = 0; i < 10; ++i) sc[i] = __builtin_amdgcn_exp2f(__builtin_fmaf(sc[i], 1.4426950408889634f, mx));
            const float inv = __builtin_amdgcn_rcpf(xsum((((sc[0] + sc[1]) + (sc[2] + sc[3])) + ((sc[4] + sc[5]) + (sc[6] + sc[7]))) + (sc[8] + sc[9])));
            u32x4 xpx = {pk(sc[0] * inv, sc[1] * inv), pk(sc[2] * inv, sc[3] * inv), pk(sc[4] * inv, sc[5] * inv), pk(sc[6] * inv, sc[7] * inv)};
            u32x4 xpm = {pk(sc[8] * inv, sc[9] * inv), 0u, 0u, 0u};
            f32x4 o[8];
#pragma unroll
            for (int e = 0; e < 4; ++e) {
                u32x4 fa = {vb[2 * e].x, vb[2 * e].y, vb[2 * e + 1].x, vb[2 * e + 1].y}, fb = {vb[2 * e].z, vb[2 * e].w, vb[2 * e + 1].z, vb[2 * e + 1].w};
                u32x4 ma = vb[2 * e], mb = vb[2 * e + 1];
                ma.x = a[4 + (e >> 1)][2 * (e & 1)]; mb.x = a[4 + (e >> 1)][2 * (e & 1) + 1];
                asm volatile("s_nop 1" : "+v"(fa), "+v"(fb), "+v"(ma), "+v"(mb), "+v"(xpx), "+v"(xpm));
                MF0(o[2 * e], fa, xpx); MF0(o[2 * e + 1], fb, xpx); MF(o[2 * e], ma, xpm); MF(o[2 * e + 1], mb, xpm);
            }
            asm volatile("s_nop 7" : "+v"(o[0]), "+v"(o[1]), "+v"(o[2]), "+v"(o[3]), "+v"(o[4]), "+v"(o[5]), "+v"(o[6]), "+v"(o[7]));
#pragma unroll
            for (int e = 0; e < 4; ++e)
                st16(rw, l16, (u32x4){pk(o[2 * e][0], o[2 * e][1]), pk(o[2 * e][2], o[2 * e][3]), pk(o[2 * e + 1][0], o[2 * e + 1][1]), pk(o[2 * e + 1][2], o[2 * e + 1][3])}, WS_O + (unsigned)((s * 2 + att) * 128 + hh * 4 + e) * 1024u);
            SB();
            seen = ld4(rw, pof0);
            a_issue_v(min(s + 1, NSEG - 1));
        }
        if (ROLE == 3) {
            LAS float* sm = (LAS float*)(lds + L_A);
            const int tk = (lane >> 2) & 1, tok = 2 * (r & 7) + tk, row = s * 32 + tt * 16 + tok;
            f32x4 acc = __builtin_bit_cast(f32x4, a[0]);
#pragma unroll
            for (int kg = 1; kg < 8; ++kg) acc += __builtin_bit_cast(f32x4, a[kg]);
            float x1[4] = {bflo(xv.x), bfhi(xv.x), bflo(xv.y), bfhi(xv.y)};
            float sum = 0.f;
#pragma unroll
            for (int i = 0; i < 4; ++i) { x1[i] = rb(x1[i] + rb(cga[i] * rb(acc[i]))); sum += x1[i]; }
            st8(rw, WS_X1 + (unsigned)(row * D + d0) * 2, (u32x2){pk(x1[0], x1[1]), pk(x1[2], x1[3])});
            sum += dpp<0xB1>(sum); sum += dpp<0x4E>(sum); sum += dpp<0x128>(sum); sum = xsum(sum);
            if (lane == 0 || lane == 4) sm[w * 2 + tk] = sum;
            bar();
            const float mean = ((sm[tk] + sm[2 + tk]) + (sm[4 + tk] + sm[6 + tk])) * (1.f / D);
            float var = 0.f;
#pragma unroll
            for (int i = 0; i < 4; ++i) { const float d = x1[i] - mean; var += d * d; }
            var += dpp<0xB1>(var); var += dpp<0x4E>(var); var += dpp<0x128>(var); var = xsum(var);
            if (lane == 0 || lane == 4) sm[8 + w * 2 + tk] = var;
            bar();
            var = ((sm[8 + tk] + sm[10 + tk]) + (sm[12 + tk] + sm[14 + tk])) * (1.f / D);
            const float mb = rb(mean), sd = rb(sqrtf(rb(rb(var) + 1e-5f)));
            float hh2[4];
#pragma unroll
            for (int i = 0; i < 4; ++i) hh2[i] = lnmod(x1[i], mb, sd, cgc[i], cgb[i]);
            st8(rw, WS_H2 + (unsigned)((s * 2 + tt) * 16 + (d0 >> 5)) * 1024u + tok * 64 + (d0 & 31) * 2, (u32x2){pk(hh2[0], hh2[1]), pk(hh2[2], hh2[3])});
        }
        if (ROLE == 5) asm volatile("s_waitcnt vmcnt(9)" ::: "memory"); else drain();
        bar();
        if (ROLE != 2 && ROLE != 4) st4(rw, t0o, s + 1, WS_FLAG + ((ROLE == 0 ? FL_B : ROLE == 1 ? FL_C : ROLE == 3 ? FL_L : FL_A) + r) * 128);
        if (doq) st4(rw, t0o, s + LEAD + 1, WS_FLAG + (FL_QKV + j) * 128);
        if (ROLE != 5) seen = ld4(rw, pof0);
        SB();
        if (doq) qkv_in(s + 1 + LEAD);
    };
    int s = 0;
    for (; s < NSEG - LEAD; ++s) iter(s, bool_c<true>{});
    if (ROLE != 4) for (; s < NSEG; ++s) iter(s, bool_c<false>{});
}

__global__ void __launch_bounds__(256, 1) k_main(Params p) {
    extern __shared__ __attribute__((aligned(16))) char smem[];
    const lptr lds = (lptr)smem;
    const int b = blockIdx.x, tid = threadIdx.x;
    if (b < 128 && (b & 7) < 4) chain_role(p, lds, (b >> 3) * 4 + (b & 3), tid);
    else {
        const int u = (b >> 3) * 4 + (b & 3);
        const int j = (b & 4) ? (u < 64 ? u : u + 56) : (u < 120 ? u : u + 64);
        if (j < 64) stream_role<0>(p, lds, j, tid);
        else if (j < 120) stream_role<1>(p, lds, j, tid);
        else if (j < 152) stream_role<2>(p, lds, j, tid);
        else if (j < 168) stream_role<3>(p, lds, j, tid);
        else if (j < 184) stream_role<5>(p, lds, j, tid);
        else stream_role<4>(p, lds, j, tid);
    }
}

extern "C" void kernel_launch(void* const* d_in, const int* in_sizes, int n_in, void* d_out, int out_size, void* d_ws, size_t ws_size, hipStream_t stream) {
    Params p;
    p.x = (const u16*)d_in[0]; p.mem0 = (const u16*)d_in[1]; p.Wq = (const u16*)d_in[2]; p.Wk = (const u16*)d_in[3]; p.Wv = (const u16*)d_in[4]; p.Wo = (const u16*)d_in[5];
    p.wg = (const u16*)d_in[8]; p.wu = (const u16*)d_in[9]; p.wd = (const u16*)d_in[10]; p.c = (const u16*)d_in[11]; p.wmod = (const u16*)d_in[12];
    p.out = (u16*)d_out; p.ws = (char*)d_ws;
    constexpr int LDSB = 96 * 1024;
    (void)hipFuncSetAttribute((const void*)k_main, hipFuncAttributeMaxDynamicSharedMemorySize, LDSB);
    k_init<<<256, 256, 0, stream>>>(p);
    k_main<<<256, 256, LDSB, stream>>>(p);
}
```

```cpp
#include <hip/hip_runtime.h>
#include <hip/hip_bf16.h>
#include <stdint.h>

typedef unsigned short u16;
typedef __bf16 bf16x2 __attribute__((ext_vector_type(2)));
typedef __bf16 bf16x8 __attribute__((ext_vector_type(8)));
typedef short s16x4 __attribute__((ext_vector_type(4)));
typedef float f32x4 __attribute__((ext_vector_type(4)));
typedef float f32x2 __attribute__((ext_vector_type(2)));
typedef unsigned u32x2 __attribute__((ext_vector_type(2)));
typedef unsigned u32x4 __attribute__((ext_vector_type(4)));
typedef unsigned u32x8 __attribute__((ext_vector_type(8)));
#define LAS __attribute__((address_space(3)))
typedef LAS char* lptr;
#define DI __device__ __forceinline__

#define TX 58
#define TY 46
#define TV1 14
#define TV2 14
#define TPEN 30
#define TM 12
#define SIDE 0
#define SLP 5
#define PREP 5
#define FDLY 0
#define CX 2150
#define CY 430
#define MUP 3
#define NPRE 4
#define LEAD 16

constexpr int S = 2048, D = 512, NSEG = 64, F = 1792, HD = 4096;
constexpr unsigned SENT = 0xFFFFFFFFu;
constexpr int NRING = 3;
constexpr unsigned WS_MOD = 0;
constexpr unsigned WS_FLAG = 131072;
constexpr unsigned FL_LN = 0, FL_QKV = 128, FL_L = 320, FL_KM = 384, FL_B = 448, FL_C = 512, FL_A = 576, FL_G = 600, FL_N = 640;
constexpr unsigned WS_CFG = 12544;
#define NC 4
constexpr unsigned WS_HO = 172u << 20, HO_MEM = 256, MRS = NC + 1, HO_N = HO_MEM + 3 * MRS;
constexpr unsigned WS_DBG = 3u << 20;
constexpr unsigned WS_H1 = 4u << 20, WS_X1 = 6u << 20, WS_H2 = 10u << 20, WS_G = 14u << 20, WS_O = 28u << 20, WS_QKV = 60u << 20, WS_PB = 108u << 20, WS_END = 198u << 20;

DI float bflo(unsigned x) { return __uint_as_float(x << 16); }
DI float bfhi(unsigned x) { return __uint_as_float(x & 0xffff0000u); }
DI unsigned pk(float lo, float hi) { bf16x2 v = {(__bf16)lo, (__bf16)hi}; return __builtin_bit_cast(unsigned, v); }
DI float rb(float x) { return (float)(__bf16)x; }
DI void bar() { asm volatile("s_waitcnt lgkmcnt(0)\n\ts_barrier" ::: "memory"); }
DI void drain() { asm volatile("s_waitcnt vmcnt(0)" ::: "memory"); }
DI u32x2 trread(lptr p) { return __builtin_bit_cast(u32x2, __builtin_amdgcn_ds_read_tr16_b64_v4i16((LAS s16x4*)p)); }
DI u32x4 tr2(lptr p, int off) { const u32x2 a = trread(p), b = trread(p + off); return (u32x4){a.x, a.y, b.x, b.y}; }
DI u32x4 ldsr128(lptr p) { return *(LAS u32x4*)p; }
DI void ldsw128(lptr p, u32x4 v) { *(LAS u32x4*)p = v; }
DI void ldsw64(lptr p, u32x2 v) { *(LAS u32x2*)p = v; }
DI void ldsw32(lptr p, unsigned v) { *(LAS unsigned*)p = v; }
DI f32x4 mfma32(u32x4 a, u32x4 b, f32x4 c) { return __builtin_amdgcn_mfma_f32_16x16x32_bf16(__builtin_bit_cast(bf16x8, a), __builtin_bit_cast(bf16x8, b), c, 0, 0, 0); }
DI float swadd32(float a, float b) {
    auto r = __builtin_amdgcn_permlane32_swap(__float_as_uint(a), __float_as_uint(b), false, false);
    return __uint_as_float(r[0]) + __uint_as_float(r[1]);
}
DI float vmax(float a, float b) { float r; asm("v_max_f32 %0, %1, %2" : "=v"(r) : "v"(a), "v"(b)); return r; }
DI float vmax3(float a, float b, float c) { float r; asm("v_max3_f32 %0, %1, %2, %3" : "=v"(r) : "v"(a), "v"(b), "v"(c)); return r; }
DI float xmax(float x) {
    auto s = __builtin_amdgcn_permlane16_swap(__float_as_uint(x), __float_as_uint(x), false, false);
    x = vmax(__uint_as_float(s[0]), __uint_as_float(s[1]));
    auto t = __builtin_amdgcn_permlane32_swap(__float_as_uint(x), __float_as_uint(x), false, false);
    return vmax(__uint_as_float(t[0]), __uint_as_float(t[1]));
}
DI float xsum(float x) {
    auto s = __builtin_amdgcn_permlane16_swap(__float_as_uint(x), __float_as_uint(x), false, false);
    x = __uint_as_float(s[0]) + __uint_as_float(s[1]);
    auto t = __builtin_amdgcn_permlane32_swap(__float_as_uint(x), __float_as_uint(x), false, false);
    return __uint_as_float(t[0]) + __uint_as_float(t[1]);
}
template <int CTRL> DI float dpp(float x) { return __builtin_bit_cast(float, __builtin_amdgcn_mov_dpp(__builtin_bit_cast(int, x), CTRL, 0xf, 0xf, true)); }
template <int CTRL> DI int dppi(int x) { return __builtin_amdgcn_mov_dpp(x, CTRL, 0xf, 0xf, true); }
DI unsigned mx4(u32x4 v, unsigned m) { return max(max(max(v.x, v.y), max(v.z, v.w)), m); }
typedef __amdgpu_buffer_rsrc_t rsrc_t;
DI rsrc_t mkrs(const void* p, unsigned bytes) { return __builtin_amdgcn_make_buffer_rsrc((void*)p, 0, bytes, 0x00020000); }
DI u32x4 ld16(rsrc_t r, unsigned off, unsigned soff = 0) { return __builtin_amdgcn_raw_buffer_load_b128(r, off, soff, 16); }
DI u32x2 ld8(rsrc_t r, unsigned off, unsigned soff) { return __builtin_amdgcn_raw_buffer_load_b64(r, off, soff, 16); }
DI unsigned ld4(rsrc_t r, unsigned off, unsigned soff = 0) { return __builtin_amdgcn_raw_buffer_load_b32(r, off, soff, 16); }
DI void st16(rsrc_t r, unsigned off, u32x4 v, unsigned soff = 0) { __builtin_amdgcn_raw_buffer_store_b128(v, r, off, soff, 16); }
DI void st8(rsrc_t r, unsigned off, u32x2 v, unsigned soff) { __builtin_amdgcn_raw_buffer_store_b64(v, r, off, soff, 16); }
DI void st4(rsrc_t r, unsigned off, unsigned v, unsigned soff = 0) { __builtin_amdgcn_raw_buffer_store_b32(v, r, off, soff, 16); }
DI u32x4 ldw(rsrc_t r, unsigned voff, unsigned soff) { return __builtin_amdgcn_raw_buffer_load_b128(r, voff, soff, 0); }
DI unsigned rt() { return (unsigned)__builtin_amdgcn_s_memrealtime(); }
DI void wait_rt(unsigned t) { while ((int)(rt() - t) < 0) { } asm volatile("" ::: "memory"); }

template <bool V> struct bool_c { static constexpr bool value = V; };
DI unsigned loc(unsigned base, unsigned off, unsigned cfg, unsigned far) { const unsigned x = base + ((off >> 13) << 14) + (off & 8191); return x + ((((cfg >> ((x >> 21) & 1)) ^ far) & 1) << 13); }
DI unsigned flg(unsigned line, unsigned cfg) { return loc(WS_FLAG, line * 128, cfg, line >= FL_KM); }
DI unsigned kvm_off(unsigned ho, unsigned cfg, unsigned i) { return ho + (8u << 20) + (i >> 1) * 16384 + ((cfg >> ((i >> 8) & 1)) & 1) * 8192 + (i & 1) * 4096; }
DI unsigned get_cfg(const char* ws) {
    const f32x4 a = *(const f32x4*)(ws + WS_CFG), b = *(const f32x4*)(ws + WS_CFG + 16);
    return __builtin_amdgcn_readfirstlane(fminf(fminf(b[0], b[1]), fminf(b[2], b[3])) < fminf(fminf(a[0], a[1]), fminf(a[2], a[3])) ? 2u : 1u);
}
struct Params {
    const u16 *x, *mem0, *Wq, *Wk, *Wv, *Wo, *wg, *wu, *wd, *c, *wmod;
    u16* out; char* ws;
};

__global__ void __launch_bounds__(256) k_init(Params p) {
    __shared__ float red[4][64];
    const int tid = threadIdx.x, w = tid >> 6, lane = tid & 63;
    if (blockIdx.x < 96) {
        const int r16 = lane >> 2, cc = lane & 3, n0 = blockIdx.x * 32 + cc * 8;
        u32x4 wv[8]; float s[8];
#pragma unroll
        for (int it = 0; it < 8; ++it) wv[it] = *(const u32x4*)(p.wmod + (size_t)(w * 128 + it * 16 + r16) * 3072 + n0);
#pragma unroll
        for (int it = 0; it < 8; ++it) { const float cv = bflo(p.c[w * 128 + it * 16 + r16]); s[it] = rb(cv / (1.f + __expf(-cv))); }
        float acc[8] = {};
#pragma unroll
        for (int it = 0; it < 8; ++it) {
            acc[0] += s[it] * bflo(wv[it].x); acc[1] += s[it] * bfhi(wv[it].x); acc[2] += s[it] * bflo(wv[it].y); acc[3] += s[it] * bfhi(wv[it].y);
            acc[4] += s[it] * bflo(wv[it].z); acc[5] += s[it] * bfhi(wv[it].z); acc[6] += s[it] * bflo(wv[it].w); acc[7] += s[it] * bfhi(wv[it].w);
        }
#pragma unroll
        for (int i = 0; i < 8; ++i) { acc[i] += dpp<0x124>(acc[i]); acc[i] += dpp<0x128>(acc[i]); acc[i] = xsum(acc[i]); }
        if (lane < 4) for (int i = 0; i < 8; ++i) red[w][cc * 8 + i] = acc[i];
        __syncthreads();
        if (tid < 32) ((float*)(p.ws + WS_MOD))[blockIdx.x * 32 + tid] = rb((red[0][tid] + red[1][tid]) + (red[2][tid] + red[3][tid]));
    } else if (blockIdx.x < 128 && (blockIdx.x & 7) == 0) {
        const unsigned a = WS_HO + 240 * 16384 + ((blockIdx.x >> 3) & 1) * 8192 + w * 1024 + (blockIdx.x & 16) * 16;
        unsigned best = ~0u;
        unsigned* ap = (unsigned*)(p.ws + a);
        for (int rep = 0; rep < PREP; ++rep) {
            unsigned long long t0, t1; unsigned r = 0;
            asm volatile("s_memtime %0\n\ts_waitcnt lgkmcnt(0)" : "=s"(t0) :: "memory");
            if (lane == 0) r = __hip_atomic_fetch_add(ap, 1u, __ATOMIC_RELAXED, __HIP_MEMORY_SCOPE_AGENT);
            asm volatile("s_waitcnt vmcnt(0)\n\ts_memtime %0\n\ts_waitcnt lgkmcnt(0)" : "=s"(t1), "+v"(r) :: "memory");
            best = min(best, (unsigned)(t1 - t0));
        }
        if (lane == 0) red[w][0] = (float)best;
        __syncthreads();
        if (tid < 2) ((float*)(p.ws + WS_CFG))[((blockIdx.x >> 3) & 1) * 4 + ((blockIdx.x >> 4) & 1) * 2 + tid] = fminf(red[tid][0], red[2 + tid][0]);
    } else {
        const unsigned t = (blockIdx.x < 128 ? blockIdx.x - 97 - ((blockIdx.x - 96) >> 3) : blockIdx.x - 100) * 256 + tid, nt = (gridDim.x - 100) * 256;
        for (int i = 0; i < FDLY; ++i) __builtin_amdgcn_s_sleep(16);
        const u32x4 z = {SENT, SENT, SENT, SENT}, zero = {0, 0, 0, 0};
        for (unsigned i = t; i < FL_N * 16; i += nt) ((u32x4*)(p.ws + WS_FLAG))[i] = zero;
        u32x4* ho = (u32x4*)(p.ws + WS_HO);
        for (unsigned i = t; i < HO_N * 1024; i += nt) {
            u32x4 v = z;
            const unsigned blk = i >> 10, m = i & 511;
            if (blk >= 192 && blk < HO_MEM) continue;
            if (blk >= HO_MEM && blk < HO_MEM + NC) {
                const unsigned* mp = (const unsigned*)(p.mem0 + (2 * ((m >> 2) & 3) + (m & 1)) * D + 16 * (m >> 4) + (m & 2));
                v = (u32x4){min(mp[0], SENT - 1), min(mp[2], SENT - 1), min(mp[4], SENT - 1), min(mp[6], SENT - 1)};
            }
            if (blk == HO_MEM + NC) v = zero;
            ho[i] = v;
        }
    }
}

DI float lnmod(float v, float mb, float isd, float t, float sh) { return rb(rb(rb(v - mb) * isd) * t) + sh; }

DI void wait_flags(rsrc_t rw, unsigned cfg, unsigned first, int cnt, int stride, unsigned need, int lane, unsigned& cap) {
    const unsigned off = flg(first + (lane < cnt ? lane : 0) * stride, cfg);
    for (unsigned spins = 0;; ++spins) {
        asm volatile("" ::: "memory");
        if (__all(ld4(rw, off) >= need)) break;
        if (spins > cap) { cap = 0; break; }
        __builtin_amdgcn_s_sleep(2);
    }
    asm volatile("" ::: "memory");
}

constexpr int PSTR = 272;
DI void panel_issue(const u16* srcA, const u16* srcB, int ld, int tid, u32x4 (&v)[32]) {
    const rsrc_t ra = mkrs(srcA, (511 * ld + 64) * 2), rb_ = mkrs(srcB, (511 * ld + 64) * 2);
    const unsigned voff = ((tid >> 3) * ld + (tid & 7) * 8) * 2;
#pragma unroll
    for (int i = 0; i < 16; ++i) { v[2 * i] = ldw(ra, voff, i * 32 * ld * 2); v[2 * i + 1] = ldw(rb_, voff, i * 32 * ld * 2); }
}
DI void panel_finish(lptr lds, int tid, int colA, int colB, u32x4 (&v)[32], u32x4 (&wa)[16], u32x4 (&wb)[16]) {
    const int lane = tid & 63, g = lane >> 4, q = (lane & 15) >> 2;
    const lptr wp = lds + (tid >> 3) * PSTR + (tid & 7) * 16;
#pragma unroll
    for (int half = 0; half < 2; ++half) {
        bar();
#pragma unroll
        for (int i = 0; i < 8; ++i) { ldsw128(wp + i * 32 * PSTR, v[16 * half + 2 * i]); ldsw128(wp + i * 32 * PSTR + 128, v[16 * half + 2 * i + 1]); }
        bar();
        lptr pa = lds + (8 * g + q) * PSTR + colA * 2, pb = lds + (8 * g + q) * PSTR + colB * 2;
#pragma unroll
        for (int ks = 0; ks < 8; ++ks) { wa[half * 8 + ks] = tr2(pa + ks * 32 * PSTR, 4 * PSTR); wb[half * 8 + ks] = tr2(pb + ks * 32 * PSTR, 4 * PSTR); asm volatile("" : "+a"(wa[half * 8 + ks]), "+a"(wb[half * 8 + ks])); }
    }
    bar();
}
constexpr int L_A = 0, L_Q = 73728;
#define MFWA(acc, wgt, x) asm volatile("v_mfma_f32_16x16x32_bf16 %0, %1, %2, %0" : "+v"(acc) : "a"(wgt), "v"(x))
#define MFWB(acc, wgt, x) asm volatile("v_mfma_f32_16x16x32_bf16 %0, %2, %1, %0" : "+v"(acc) : "a"(wgt), "v"(x))
template <bool SWAP> DI void tile_mma(lptr lds, int lane, const u32x4 (&wa)[16], const u32x4 (&wb)[16], f32x4& ca, f32x4& cb) {
    ca = (f32x4){0, 0, 0, 0}; cb = ca;
    u32x4 a[16];
#pragma unroll
    for (int ks = 0; ks < 4; ++ks) a[ks] = ldsr128(lds + ks * 1024 + lane * 16);
    asm volatile("s_nop 1" : "+v"(ca), "+v"(cb));
    __builtin_amdgcn_sched_barrier(0);
#pragma unroll
    for (int ks = 0; ks < 16; ++ks) {
        if (SWAP) MFWB(ca, wa[ks], a[ks]); else MFWA(ca, wa[ks], a[ks]);
        if (ks + 4 < 16) a[ks + 4] = ldsr128(lds + (ks + 4) * 1024 + lane * 16);
        __builtin_amdgcn_sched_barrier(0);
        if (SWAP) MFWB(cb, wb[ks], a[ks]); else MFWA(cb, wb[ks], a[ks]);
    }
    asm volatile("s_nop 7" : "+v"(ca), "+v"(cb));
}

constexpr int CSTR = 656, OSTR = 1040;
constexpr int L_MEMF = 0, L_QT = 8192, L_KT = 12288, L_VT = 16384, L_RED = 20480, L_TS = 21504, L_SEL = 23552, L_ONES = 24576, L_QKV = 25600;
constexpr float SC_L2E = 0.08838834764831845f * 1.4426950408889634f;
constexpr unsigned OOB = 0x80000000u;
#define SMFA(acc, a, w) asm volatile("v_smfmac_f32_16x16x64_bf16 %0, %1, %2, %3" : "+v"(acc) : "v"(a), "a"(w), "v"(sidx))
#define SMFV(acc, a, w) asm volatile("v_smfmac_f32_16x16x64_bf16 %0, %1, %2, %3" : "+v"(acc) : "v"(a), "v"(w), "v"(sidx))
#define MF0(acc, a, b) asm volatile("v_mfma_f32_16x16x32_bf16 %0, %1, %2, 0" : "=&v"(acc) : "v"(a), "v"(b))
#define MF(acc, a, b) asm volatile("v_mfma_f32_16x16x32_bf16 %0, %1, %2, %0" : "+v"(acc) : "v"(a), "v"(b))
#define NOP2() asm volatile("s_nop 1")

#define SB() __builtin_amdgcn_sched_barrier(0)
DI f32x4 zero4() { f32x2 a, b; asm volatile("v_mov_b64 %0, 0\n\tv_mov_b64 %1, 0" : "=v"(a), "=v"(b)); return (f32x4){a.x, a.y, b.x, b.y}; }
DI u32x4 sent4() { u32x2 a, b; asm volatile("v_mov_b64 %0, -1\n\tv_mov_b64 %1, -1" : "=v"(a), "=v"(b)); return (u32x4){a.x, a.y, b.x, b.y}; }
DI float vadd(float a, float b) { float r; asm volatile("v_add_f32 %0, %1, %2" : "=v"(r) : "v"(a), "v"(b)); return r; }
DI unsigned vcvt(float a, float b) { unsigned r; asm volatile("v_cvt_pk_bf16_f32 %0, %1, %2" : "=v"(r) : "v"(a), "v"(b)); return r; }
template <int O0, int O1> DI void dsw2(lptr p, unsigned a, unsigned b) { asm volatile("ds_write2_b32 %0, %1, %2 offset0:%3 offset1:%4" :: "v"(p), "v"(a), "v"(b), "i"(O0), "i"(O1) : "memory"); }
#define ST(k)
#define ST2(k)
#define TS(k)
#define SU(id, k)
DI int wave_max_rel(unsigned v, unsigned base) {
    int d = (int)(v - base);
    d = max(d, dppi<0xB1>(d)); d = max(d, dppi<0x4E>(d)); d = max(d, dppi<0x124>(d)); d = max(d, dppi<0x128>(d));
    auto s = __builtin_amdgcn_permlane16_swap((unsigned)d, (unsigned)d, false, false); d = max((int)s[0], (int)s[1]);
    auto t = __builtin_amdgcn_permlane32_swap((unsigned)d, (unsigned)d, false, false); d = max((int)t[0], (int)t[1]);
    return __builtin_amdgcn_readfirstlane(d);
}

DI void chain_role(const Params& p, lptr lds, const int b, const int tid) {
    const int w = __builtin_amdgcn_readfirstlane(tid >> 6), lane = tid & 63, n = lane & 15, g = lane >> 4;
    const int h = (b & 3) + 4 * (b >> 3), hf = (b >> 2) & 1;
    u32x8 wq[2][8], wk[2][8], wv[8], wo[8];
    {
        const int q = n >> 2, pp = lane & 3;
        constexpr int WSTR = 208;
        u32x4 v[4][12];
        const rsrc_t rq = mkrs(p.Wq + h * 128 + 32 * w, (511 * HD + 32) * 2), rk = mkrs(p.Wk + h * 128 + 32 * w, (511 * HD + 32) * 2), rv = mkrs(p.Wv + h * 128 + hf * 64 + 32 * (w >> 1), (511 * HD + 32) * 2);
        const unsigned vo = ((lane >> 2) * HD + (lane & 3) * 8) * 2;
        const lptr img = lds + w * 64 * WSTR, wp = img + (lane >> 2) * WSTR + (lane & 3) * 16;
        auto issue = [&](int j, u32x4 (&d)[12]) {
            const unsigned so = ((2 * w + j) & 7) * 64 * HD * 2;
#pragma unroll
            for (int i = 0; i < 4; ++i) { d[i] = ldw(rq, vo, so + i * 16 * HD * 2); d[4 + i] = ldw(rk, vo, so + i * 16 * HD * 2); d[8 + i] = ldw(rv, vo, so + i * 16 * HD * 2); } };
        issue(0, v[0]); issue(1, v[1]); issue(2, v[2]); issue(3, v[3]);
        u32x4 u[16];
        const rsrc_t ro = mkrs(p.Wo + (size_t)(h * 128 + hf * 64) * D, 65536);
#pragma unroll
        for (int ks = 0; ks < 8; ++ks) {
#pragma unroll
            for (int i = 0; i < 4; ++i) { ldsw128(wp + i * 16 * WSTR, v[ks & 3][i]); ldsw128(wp + i * 16 * WSTR + 64, v[ks & 3][4 + i]); ldsw128(wp + i * 16 * WSTR + 128, v[ks & 3][8 + i]); }
            if (ks + 4 < 8) issue(ks + 4, v[ks & 3]);
            if (ks == 4 || ks == 5) {
#pragma unroll
                for (int i = 0; i < 8; ++i) u[(ks - 4) * 8 + i] = ldw(ro, tid * 16, ((ks - 4) * 8 + i) * 4096);
            }
            asm volatile("s_waitcnt lgkmcnt(0)" ::: "memory");
            lptr base = img + (8 * g + q) * WSTR;
            auto frag = [&](int colbyte) { u32x8 f;
#pragma unroll
                for (int m = 0; m < 4; ++m) { const u32x2 t = trread(base + (32 * (m >> 1) + 4 * (m & 1)) * WSTR + colbyte); f[2 * m] = t.x; f[2 * m + 1] = t.y; }
                return f; };
            wq[0][ks] = frag(8 * pp); wq[1][ks] = frag(32 + 8 * pp);
            wk[0][ks] = frag(64 + 8 * pp); wk[1][ks] = frag(96 + 8 * pp);
            wv[ks] = frag(128 + 16 * pp + 8 * (w & 1));
            asm volatile("s_waitcnt lgkmcnt(0)" : "+a"(wq[0][ks]), "+a"(wq[1][ks]), "+a"(wk[0][ks]), "+a"(wk[1][ks]), "+v"(wv[ks]) :: "memory");
        }
        {
            bar();
#pragma unroll
            for (int i = 0; i < 16; ++i) ldsw128(lds + ((tid >> 6) + 4 * i) * OSTR + (tid & 63) * 16, u[i]);
            bar();
            lptr base = lds + (32 * (q >> 1) + 4 * g + (q & 1)) * OSTR;
#pragma unroll
            for (int t = 0; t < 8; ++t) {
                const int col = 128 * w + 64 * (t >> 2) + 16 * pp + 4 * (t & 3);
#pragma unroll
                for (int m = 0; m < 4; ++m) { const u32x2 tv = trread(base + (16 * (m >> 1) + 2 * (m & 1)) * OSTR + col * 2); wo[t][2 * m] = tv.x; wo[t][2 * m + 1] = tv.y; }
            }
            bar();
        }
    }
    const rsrc_t rw = mkrs(p.ws, WS_END);
    const int hi = (n >> 1) & 1, rowi = 2 * (n >> 2) + (n & 1);
    const int sidx = hi ? 0xEEEE : 0x4444;
    unsigned cap = 1u << 17;
    const unsigned l16 = lane * 16;
    const unsigned cfg = get_cfg(p.ws), ho = WS_HO;
    const unsigned ho0 = ho + (cfg & 1) * 8192, ho1 = ho + ((cfg >> 1) & 1) * 8192;
    const unsigned xc = (b & 3) + 4 * ((b >> 2) % (NC / 4));
    auto pblk = [&](unsigned ring_, unsigned r) { return (ring_ == 2 ? ho1 : ho0) + (ring_ * 64 + r) * 16384; };
    const unsigned t0o = tid == 0 ? 0u : OOB;
    auto qkv_issue = [&](int s, u32x4 (&v)[3]) {
        const unsigned base = WS_QKV + (unsigned)(s * 32 + h) * 24576u;
        v[0] = ld16(rw, l16, base + 8192 + w * 2048); v[1] = ld16(rw, l16, base + 8192 + w * 2048 + 1024);
        v[2] = ld16(rw, l16, base + 16384 + hf * 4096 + w * 1024);
    };
    auto qkv_store = [&](int s, u32x4 (&v)[3]) {
        lptr d = lds + L_QKV + (s & 1) * 12288 + lane * 16;
        ldsw128(d + w * 2048, v[0]); ldsw128(d + w * 2048 + 1024, v[1]);
        ldsw64(d + 8192 + (w >> 1) * 2048 + (w & 1) * 8, (u32x2){v[2].x, v[2].y}); ldsw64(d + 8192 + (w >> 1) * 2048 + 1024 + (w & 1) * 8, (u32x2){v[2].z, v[2].w});
    };
    const unsigned qflag = flg(FL_QKV + (h & 7) + ((((lane >> 1) & 1) * 32 + 32 + h) >> 3) * 16 + (lane & 1) * 8, cfg), kflag = flg(FL_KM + b, cfg), gflag = flg(FL_G, cfg);
    auto qkv_wait = [&](unsigned need) { for (unsigned spins = 0;; ++spins) { asm volatile("" ::: "memory"); if (__all(ld4(rw, qflag) >= need)) break; if (spins > cap) { cap = 0; break; } __builtin_amdgcn_s_sleep(1); } asm volatile("" ::: "memory"); };
    SU(b, 0);
    { qkv_wait(1); SU(b, 1); u32x4 v[3]; qkv_issue(0, v); qkv_store(0, v); }
    const lptr qtw = lds + L_QT + w * 1024 + (n >> 3) * 256 + (((n & 7) * 32) ^ ((n >> 3) * 128)) + g * 8;
    const lptr tr0 = lds + L_QT + g * 256 + (n >> 2) * 32 + (lane & 3) * 8 + (g & 1) * 128, tr1 = tr0 + ((g & 1) ? -128 : 128);
    auto trf = [&](int off) { const u32x2 a = trread(tr0 + off), c = trread(tr1 + off); return (u32x4){a.x, a.y, c.x, c.y}; };
    const lptr mw = lds + L_MEMF + tid * 16 + w * 1024, tsw = lds + L_TS + tid * 4, vtw = lds + L_VT + tid * 4, vtr = lds + L_VT + lane * 4;
    const lptr rdw = lds + L_RED + (w * 64 + 32 * ((n >> 2) & 1) + 16 * ((n >> 1) & 1) + (n & 1) + 4 * (g & 1)) * 4, rdr = lds + L_RED + (lane & 31) * 8;
    const unsigned tso = w == 3 ? l16 : OOB, sco = w == 3 ? (lane & 1) * 4 : OOB, mso = w < 2 ? (2 * w + (lane >> 5)) * 16384 + (lane & 31) * 4 : b == 0 ? NC * 16384 + (w - 2) * 256 + lane * 4 : OOB,
        tinc = (w == 0 || (w == 1 && lane < 32)) ? 786432u : 0u, kso = (hf | hi) ? OOB : (rowi * 4 + g) * 16, pvo = (2 * (n >> 2) + (g >> 1)) * 16384 + ((n & 3) + 4 * (g & 1)) * 16;
    if (w == 1) ldsw128(lds + L_ONES + lane * 16, (u32x4){0x3f803f80u, 0x3f803f80u, 0x3f803f80u, 0x3f803f80u});
    if (w == 0) { const unsigned one = 0x3f80u << (16 * (n & 1)), k2 = (n >> 1) & 3; ldsw128(lds + L_SEL + lane * 16, (u32x4){k2 == 0 ? one : 0u, k2 == 1 ? one : 0u, k2 == 2 ? one : 0u, k2 == 3 ? one : 0u}); }
    if (tid == 0) *(LAS unsigned*)(lds + L_TS + 1024) = 0;
    const unsigned long long himask = 0xCCCCCCCCCCCCCCCCull;
    bar();
    unsigned tvo = w == 0 ? WS_QKV + (64 + h) * 24576u + 8192 + lane * 128 : w == 1 ? (lane < 32 ? WS_QKV + (64 + h) * 24576u + 16384 + hf * 4096 + lane * 128 : qflag) : OOB;
    unsigned T2 = rt(), T1 = 0, ring = 0;
    unsigned pc = (unsigned)__builtin_amdgcn_s_memtime(), pt = T2, X = TX + CX / 24, Y = TY + CY / 24, mg = 0;
    auto step = [&](const int s, auto last_) {
        constexpr bool LAST = decltype(last_)::value;
        f32x4 aq0, aq1, ak0, ak1, av;
        const unsigned rnext = ring == NRING - 1 ? 0 : ring + 1;
        const unsigned mo = ho0 + (HO_MEM + ring * MRS + xc) * 16384 + w * 2048, to = ho0 + (HO_MEM + ring * MRS + NC) * 16384 + xc * 128;
        u32x4 A[8], kx[2][4], qf[4], kf[4]; unsigned tsv = 0;
        wait_rt(T2);
        ST(0);
        A[0] = ld16(rw, l16, mo); A[1] = ld16(rw, l16, mo + 1024); tsv = ld4(rw, sco, to);
        SB();
        SB();
        lptr qb = lds + L_QKV + (s & 1) * 12288 + lane * 16;
        lptr ar0 = lds + L_MEMF + lane * 16 + ((2 * w + 2) & 7) * 1024, ar1 = lds + L_MEMF + lane * 16 + ((2 * w + 4) & 7) * 1024, ar2 = lds + L_MEMF + lane * 16 + ((2 * w + 6) & 7) * 1024;
        asm volatile("" : "+v"(qb), "+v"(ar0), "+v"(ar1), "+v"(ar2));
        if constexpr (!LAST) {
#pragma unroll
            for (int ks = 0; ks < NPRE; ++ks) { kx[0][ks] = ldsr128(qb + ks * 1024); kx[1][ks] = ldsr128(qb + 4096 + ks * 1024); }
        }
        aq0 = zero4(); aq1 = zero4(); ak0 = zero4(); ak1 = zero4(); av = zero4();
        if constexpr (!LAST) {
#pragma unroll
            for (int ks = 0; ks < NPRE; ++ks) asm volatile("" : "+v"(kx[0][ks]), "+v"(kx[1][ks]));
        }
        SB();
        ldsw128(mw, A[0]); ldsw128(mw + 1024, A[1]); ldsw32(tsw, tsv);
        SB();
        if (__builtin_expect(!__all(mx4(A[0], mx4(A[1], tsv)) != SENT), 0)) {
            for (unsigned spins = 0;; ++spins) {
                asm volatile("" ::: "memory");
                A[0] = ld16(rw, l16, mo); A[1] = ld16(rw, l16, mo + 1024); tsv = ld4(rw, sco, to);
                if (__all(mx4(A[0], mx4(A[1], tsv)) != SENT)) break;
                if (spins > cap) { cap = 0; break; }
            }
            ldsw128(mw, A[0]); ldsw128(mw + 1024, A[1]); ldsw32(tsw, tsv);
        }
        ST(1);
        TS(0);
        NOP2();
        if constexpr (!LAST) { SMFA(aq0, A[0], wq[0][0]); SMFA(aq1, A[0], wq[1][0]); }
        SMFA(ak0, A[0], wk[0][0]);
        bar();
        ST2(9);
        TS(1);
        A[2] = ldsr128(ar0); A[3] = ldsr128(ar0 + 1024); SB();
        SMFA(ak1, A[0], wk[1][0]);
        A[4] = ldsr128(ar1); A[5] = ldsr128(ar1 + 1024); SB();
        SMFV(av, A[0], wv[0]);
        A[6] = ldsr128(ar2); A[7] = ldsr128(ar2 + 1024); SB();
        u32x2 tm = {0, 0}; unsigned look = 0;
#define POST3(C, acc, K, W, src, dst, WR) { float t0_, t1_; asm volatile( \
                "v_add_f32 %1, %10, %12\n\tv_smfmac_f32_16x16x64_bf16 %0, %3, %6, %9\n\tv_add_f32 %2, %11, %13\n\tv_smfmac_f32_16x16x64_bf16 %0, %4, %7, %9\n\t" \
                "v_cvt_pk_bf16_f32 %1, %1, %2\n\tv_smfmac_f32_16x16x64_bf16 %0, %5, %8, %9\n\tds_write2_b32 %14, %1, %1 " WR \
                : "+v"(acc), "=&v"(t0_), "=&v"(t1_) : "v"(A[K]), "v"(A[K + 1]), "v"(A[K + 2]), C(W[K]), C(W[K + 1]), C(W[K + 2]), "v"(sidx), \
                  "v"(src[0]), "v"(src[1]), "v"(src[2]), "v"(src[3]), "v"(dst) : "memory"); }
        if constexpr (!LAST) {
            SMFA(aq0, A[1], wq[0][1]);
            SMFA(aq1, A[1], wq[1][1]);
            tm = *(LAS u32x2*)(lds + L_TS + 768); SB();
            SMFA(ak0, A[1], wk[0][1]);
            SMFA(ak1, A[1], wk[1][1]); SMFV(av, A[1], wv[1]);
            TS(2);
            SMFA(aq0, A[2], wq[0][2]); SMFA(aq0, A[3], wq[0][3]); SMFA(aq0, A[4], wq[0][4]); SMFA(aq0, A[5], wq[0][5]);
            look = ld4(rw, tvo); SB();
            SMFA(aq0, A[6], wq[0][6]);
            if (__builtin_expect(b == 0, 0)) {
                unsigned sv = s;
                asm volatile("buffer_store_dword %0, %1, %2, %3 offen sc1" :: "v"(sv), "v"(t0o), "s"(rw), "s"(gflag) : "memory");
            }
            SMFA(aq0, A[7], wq[0][7]);
            u32x4 vx[4];
            SMFA(aq1, A[2], wq[1][2]);
            vx[0] = ldsr128(qb + 8192); SB();
            SMFA(aq1, A[3], wq[1][3]);
            vx[2] = ldsr128(qb + 8192 + 2048); SB();
            SMFA(aq1, A[4], wq[1][4]);
            POST3("a", aq1, 5, wq[1], aq0, qtw, "offset0:0 offset1:1");
            TS(3);
            SMFA(ak0, A[2], wk[0][2]);
            SMFA(ak0, A[3], wk[0][3]);
            const u32x4 ones = ldsr128(lds + L_ONES + lane * 16); SB();
            SMFA(ak0, A[4], wk[0][4]);
            vx[1] = ldsr128(qb + 8192 + 1024); SB();
            POST3("a", ak0, 5, wk[0], aq1, qtw, "offset0:128 offset1:129");
            SMFA(ak1, A[2], wk[1][2]);
            SMFA(ak1, A[3], wk[1][3]);
            vx[3] = ldsr128(qb + 8192 + 3072); SB();
            SMFA(ak1, A[4], wk[1][4]);
            {
                float t0_, t1_;
                asm volatile("v_add_f32 %1, %10, %12\n\tv_smfmac_f32_16x16x64_bf16 %0, %3, %6, %9\n\tv_add_f32 %2, %11, %13\n\tv_smfmac_f32_16x16x64_bf16 %0, %4, %7, %9\n\t"
                             "v_cvt_pk_bf16_f32 %1, %1, %2\n\ts_waitcnt lgkmcnt(0)\n\ts_barrier\n\tv_smfmac_f32_16x16x64_bf16 %0, %5, %8, %9\n\tds_write2_b32 %14, %1, %1 offset0:0 offset1:1"
                             : "+v"(ak1), "=&v"(t0_), "=&v"(t1_) : "v"(A[5]), "v"(A[6]), "v"(A[7]), "a"(wk[1][5]), "a"(wk[1][6]), "a"(wk[1][7]), "v"(sidx),
                               "v"(ak0[0]), "v"(ak0[1]), "v"(ak0[2]), "v"(ak0[3]), "v"(qtw + 4096) : "memory");
            }
                TS(4);
            qf[0] = trf(0); SB();
            SMFV(av, A[2], wv[2]);
            qf[1] = trf(1024); SB();
            SMFV(av, A[3], wv[3]);
            qf[2] = trf(2048); SB();
            SMFV(av, A[4], wv[4]);
            qf[3] = trf(3072); SB();
            POST3("v", av, 5, wv, ak1, qtw + 4096, "offset0:128 offset1:129");
            TS(5);
            f32x4 s0, s1, s2, c[8];
            MF0(s1, kx[0][0], qf[0]);
            MF0(s2, kx[1][0], qf[0]);
            MF(s1, kx[0][1], qf[1]);
            {
                float t0_, t1_;
                asm volatile("v_add_f32 %2, %8, %10\n\tv_add_f32 %3, %9, %11\n\tv_mfma_f32_16x16x32_bf16 %1, %4, %6, %1\n\tv_cvt_pk_bf16_f32 %2, %2, %3\n\t"
                             "v_mfma_f32_16x16x32_bf16 %0, %5, %7, %0\n\ts_waitcnt lgkmcnt(0)\n\ts_barrier\n\tds_write_b32 %12, %2"
                             : "+v"(s1), "+v"(s2), "=&v"(t0_), "=&v"(t1_) : "v"(kx[1][1]), "v"(kx[0][2]), "v"(qf[1]), "v"(qf[2]), "v"(av[0]), "v"(av[1]), "v"(av[2]), "v"(av[3]), "v"(vtw) : "memory");
            }
                TS(6);
            kf[0] = trf(4096); SB();
            MF(s2, kx[1][2], qf[2]);
            kf[1] = trf(4096 + 1024); SB();
            MF(s1, kx[0][3], qf[3]);
            kf[2] = trf(4096 + 2048); SB();
            MF(s2, kx[1][3], qf[3]);
            kf[3] = trf(4096 + 3072);
            SB();
            ST(2);
            TS(7);
            u32x4 pm;
            float mx;
            {
                float t_;
                MF0(s0, kf[0], qf[0]);
                pm = __builtin_bit_cast(u32x4, zero4());
                asm volatile("v_mfma_f32_16x16x32_bf16 %1, %2, %3, %1\n\tv_max3_f32 %0, %4, %5, %6" : "=&v"(mx), "+v"(s0) : "v"(kf[1]), "v"(qf[1]), "v"(s1[0]), "v"(s1[1]), "v"(s1[2]));
                asm volatile("v_mfma_f32_16x16x32_bf16 %2, %3, %4, %2\n\tv_max3_f32 %1, %5, %6, %7\n\tv_max3_f32 %0, %0, %8, %9"
                             : "+v"(mx), "=&v"(t_), "+v"(s0) : "v"(kf[2]), "v"(qf[2]), "v"(s1[3]), "v"(s2[0]), "v"(s2[1]), "v"(s2[2]), "v"(s2[3]));
                asm volatile("v_mfma_f32_16x16x32_bf16 %2, %3, %4, %2\n\tv_max_f32 %0, %0, %1\n\tv_mov_b32 %1, %0\n\t"
                             "s_waitcnt lgkmcnt(0)\n\ts_barrier\n\tv_permlane16_swap_b32 %0, %1\n\tv_max_f32 %0, %0, %1\n\t"
                             "v_mov_b32 %1, %0\n\ts_nop 1\n\tv_permlane32_swap_b32 %0, %1\n\tv_max_f32 %0, %0, %1\n\tv_mul_f32 %0, 0xbe0293ee, %0"
                             : "+v"(mx), "+v"(t_), "+v"(s0) : "v"(kf[3]), "v"(qf[3]) : "memory");
            }
                ST2(12);
            TS(8);
            u32x4 px;
            float e0, e1, em;
            {
                float a_, b_, c_;
#define FE(d, src) "v_fmamk_f32 " d ", " src ", 0x3e0293ee, %20\n\t"
                asm volatile(FE("%7", "%10") FE("%8", "%11") "v_exp_f32 %7, %7\n\t" FE("%9", "%12") "v_exp_f32 %8, %8\n\t" FE("%1", "%13") "v_exp_f32 %9, %9\n\tv_cvt_pk_bf16_f32 %0, %7, %8\n\t"
                             "v_exp_f32 %1, %1\n\t" FE("%7", "%14") FE("%8", "%15") "v_exp_f32 %7, %7\n\tv_cvt_pk_bf16_f32 %1, %9, %1\n\tv_exp_f32 %8, %8\n\t" FE("%9", "%16") FE("%3", "%17")
                             "v_exp_f32 %9, %9\n\tv_cvt_pk_bf16_f32 %2, %7, %8\n\tv_exp_f32 %3, %3\n\t" FE("%4", "%18") FE("%5", "%19") "v_cvt_pk_bf16_f32 %3, %9, %3\n\tv_max_f32 %6, %4, %5"
                             : "=&v"(px.x), "=&v"(px.y), "=&v"(px.z), "=&v"(px.w), "=&v"(e0), "=&v"(e1), "=&v"(em), "=&v"(a_), "=&v"(b_), "=&v"(c_)
                             : "v"(s1[0]), "v"(s1[1]), "v"(s1[2]), "v"(s1[3]), "v"(s2[0]), "v"(s2[1]), "v"(s2[2]), "v"(s2[3]), "v"(s0[0]), "v"(s0[1]), "v"(mx), "v"(s0));
            }
            const bool slow = __any(em > 0.f);
            SB();
            TS(9);
            f32x4 o[4], ls;
            MF0(o[0], vx[0], px);
            kf[0].x = *(LAS unsigned*)vtr; SB(); kf[2].x = *(LAS unsigned*)(vtr + 512); SB();
            MF0(o[2], vx[2], px);
            kf[1].x = *(LAS unsigned*)(vtr + 256); SB(); kf[3].x = *(LAS unsigned*)(vtr + 768); SB();
            float up = 0.f;
            if (__builtin_expect(slow, 0)) {
                up = xmax(vmax(em, 0.f)); e0 -= up; e1 -= up;
            }
            SB();
            MF0(o[1], vx[1], px);
            asm volatile("v_exp_f32 %1, %1\n\tv_mfma_f32_16x16x32_bf16 %0, %2, %3, 0" : "=&v"(o[3]), "+v"(e0) : "v"(vx[3]), "v"(px));
            asm volatile("v_exp_f32 %2, %2\n\tv_mfma_f32_16x16x32_bf16 %0, %4, %5, 0\n\tv_cvt_pk_bf16_f32 %1, %3, %2" : "=&v"(ls), "=&v"(pm.x), "+v"(e1) : "v"(e0), "v"(ones), "v"(px));
            SB();
            if (__builtin_expect(slow, 0)) {
                asm volatile("s_nop 7" : "+v"(o[0]), "+v"(o[1]), "+v"(o[2]), "+v"(o[3]), "+v"(ls));
                const float al = __builtin_amdgcn_exp2f(-up);
                o[0] *= al; o[1] *= al; o[2] *= al; o[3] *= al; ls *= al;
            }
            c[0] = zero4();
            asm volatile("" : "+v"(pm), "+v"(o[0]), "+v"(o[1]), "+v"(o[2]), "+v"(o[3]), "+v"(ls));
            TS(10);
            MF(ls, ones, pm);
            c[1] = zero4();
            MF(o[0], kf[0], pm);
            c[2] = zero4();
            MF(o[2], kf[2], pm);
            c[3] = zero4();
            MF(o[1], kf[1], pm);
            c[4] = zero4();
            u32x4 o8;
            {
                float inv; f32x2 il, ih, t0_, t1_;
                asm volatile("v_mfma_f32_16x16x32_bf16 %0, %3, %4, %0\n\tv_rcp_f32 %1, %5\n\ts_nop 0\n\tv_cndmask_b32_e64 %2, 0, %1, %6\n\tv_cndmask_b32_e64 %1, %1, 0, %6"
                             : "+v"(o[3]), "=&v"(il.x), "=&v"(ih.x) : "v"(kf[3]), "v"(pm), "v"(ls[0]), "s"(himask), "v"(ls));
                (void)inv; (void)t0_; (void)t1_;
                TS(11);
                c[5] = zero4();
                f32x4 oa = o[0] * il.x + o[2] * ih.x;
                o8.x = pk(oa[0], oa[1]); o8.y = pk(oa[2], oa[3]);
                asm volatile("" : "+v"(o8.x), "+v"(o8.y), "+v"(o[1]), "+v"(o[3]));
                const f32x4 ob = o[1] * il.x + o[3] * ih.x;
                o8.z = pk(ob[0], ob[1]); o8.w = pk(ob[2], ob[3]);
            }
            ST2(13);
            asm volatile("s_nop 1" : "+v"(o8));
            TS(12);
            SB();
            SMFV(c[0], o8, wo[0]); c[6] = zero4(); SMFV(c[1], o8, wo[1]); c[7] = zero4(); SMFV(c[2], o8, wo[2]); SMFV(c[3], o8, wo[3]);
            const unsigned slot = pblk(ring, 16 * w) + b * 128;
            u32x4 pa;
            {
                float t_;
                asm volatile("v_add_f32 %4, %15, %17\n\tv_smfmac_f32_16x16x64_bf16 %0, %9, %10, %14\n\tv_add_f32 %8, %16, %18\n\tv_smfmac_f32_16x16x64_bf16 %1, %9, %11, %14\n\t"
                             "v_cvt_pk_bf16_f32 %4, %4, %8\n\tv_smfmac_f32_16x16x64_bf16 %2, %9, %12, %14\n\tv_add_f32 %5, %19, %21\n\tv_smfmac_f32_16x16x64_bf16 %3, %9, %13, %14\n\t"
                             "v_add_f32 %8, %20, %22\n\tv_cvt_pk_bf16_f32 %5, %5, %8\n\tv_add_f32 %6, %23, %25\n\tv_add_f32 %8, %24, %26\n\tv_cvt_pk_bf16_f32 %6, %6, %8"
                             : "+v"(c[4]), "+v"(c[5]), "+v"(c[6]), "+v"(c[7]), "=&v"(pa.x), "=&v"(pa.y), "=&v"(pa.z), "=&v"(pa.w), "=&v"(t_)
                             : "v"(o8), "v"(wo[4]), "v"(wo[5]), "v"(wo[6]), "v"(wo[7]), "v"(sidx),
                               "v"(c[0][0]), "v"(c[0][1]), "v"(c[0][2]), "v"(c[0][3]), "v"(c[1][0]), "v"(c[1][1]), "v"(c[1][2]), "v"(c[1][3]), "v"(c[2][0]), "v"(c[2][1]), "v"(c[2][2]), "v"(c[2][3]));
            }
            auto fold = [](f32x4 v) { const f32x2 t = (f32x2){v[0], v[1]} + (f32x2){v[2], v[3]}; return pk(t.x, t.y); };
            pa.w = fold(c[3]);
            st16(rw, pvo, pa, slot);
            TS(13);
            SB();
            st16(rw, pvo, (u32x4){fold(c[4]), fold(c[5]), fold(c[6]), fold(c[7])}, slot + 131072);
            TS(14);
            SB();
            const unsigned now = rt();
            ST(3);
            T1 = __builtin_amdgcn_readfirstlane(tm.x); T2 = __builtin_amdgcn_readfirstlane(tm.y);
            if (s == 0 || (int)(now + TV1 - T1) > 0) T1 = now + TV1 + TM;
        } else {
#pragma unroll
            for (int ks = 1; ks < 8; ++ks) { SMFA(ak0, A[ks], wk[0][ks]); SMFA(ak1, A[ks], wk[1][ks]); SMFV(av, A[ks], wv[ks]); }
            asm volatile("s_nop 7" : "+v"(av), "+v"(ak0), "+v"(ak1));
            unsigned d0 = pk(ak0[0] + ak0[2], ak0[1] + ak0[3]); dsw2<0, 1>(qtw + 4096, d0, d0);
            d0 = pk(ak1[0] + ak1[2], ak1[1] + ak1[3]); dsw2<128, 129>(qtw + 4096, d0, d0);
            ldsw32(vtw, pk(av[0] + av[2], av[1] + av[3]));
            bar();
        }
        {
            const u32x4 kv = trf(4096 + w * 1024);
            const u32x4 vv = {*(LAS unsigned*)vtr, *(LAS unsigned*)(vtr + 256), *(LAS unsigned*)(vtr + 512), *(LAS unsigned*)(vtr + 768)};
            const unsigned kb = kvm_off(ho, cfg, s * 32 + h);
            st16(rw, kso, kv, kb + w * 512); st16(rw, tso, vv, kb + 2048 + hf * 1024);
        }
        if constexpr (LAST) return; else {
        ST(4);
        u32x4 p0, p1, nv[3];
        const unsigned pb = pblk(ring, b) + w * 2048;
        if (__builtin_expect((unsigned)s + 2 > (unsigned)__builtin_amdgcn_readfirstlane(*(LAS unsigned*)(lds + L_TS + 1024)), 0)) qkv_wait(s + 2);
        qkv_issue(s + 1, nv);
        if (s < NSEG - 3) tvo += tinc;
        const unsigned qmin = min(min(__builtin_amdgcn_readlane(look, 32), __builtin_amdgcn_readlane(look, 33)), min(__builtin_amdgcn_readlane(look, 34), __builtin_amdgcn_readlane(look, 35)));
        if (b == 0 && (s & 3) == 3) {
            const unsigned c = (unsigned)__builtin_amdgcn_s_memtime(), t = rt();
            const float tpc = (float)(t - pt) * __builtin_amdgcn_rcpf((float)(c - pc));
            X = TX + __builtin_amdgcn_readfirstlane((int)(CX * tpc + 0.5f)); Y = TY + __builtin_amdgcn_readfirstlane((int)(CY * tpc + 0.5f));
            pc = c; pt = t; mg -= mg > 0;
        }
        wait_rt(T1);
        ST(5);
        p0 = ld16(rw, l16, pb); p1 = ld16(rw, l16, pb + 1024);
        SB();
        qkv_store(s + 1, nv);
        SB();
        u32x4 sel = ldsr128(lds + L_SEL + lane * 16);
        asm volatile("" : "+v"(sel));
        SB();
        f32x4 rr;
        MF0(rr, sel, p0); MF(rr, sel, p1);
        SB();
        if (__builtin_expect(!__all(mx4(p0, mx4(p1, 0u)) != SENT), 0)) {
            for (unsigned spins = 0;; ++spins) {
                asm volatile("" ::: "memory");
                p0 = ld16(rw, l16, pb); p1 = ld16(rw, l16, pb + 1024);
                if (__all(mx4(p0, mx4(p1, 0u)) != SENT)) break;
                if (spins > cap) { cap = 0; break; }
            }
            MF0(rr, sel, p0); MF(rr, sel, p1);
            asm volatile("s_nop 7" : "+v"(rr));
        }
        ST(6);
        {
            asm volatile("s_nop 0" : "+v"(rr));
            unsigned q0, q1, q2, q3;
            asm volatile("v_add_f32_dpp %0, %4, %4 row_ror:8 row_mask:0xf bank_mask:0xf\n\tv_add_f32_dpp %1, %5, %5 row_ror:8 row_mask:0xf bank_mask:0xf\n\t"
                         "v_add_f32_dpp %2, %6, %6 row_ror:8 row_mask:0xf bank_mask:0xf\n\tv_add_f32_dpp %3, %7, %7 row_ror:8 row_mask:0xf bank_mask:0xf"
                         : "=&v"(q0), "=&v"(q1), "=&v"(q2), "=&v"(q3) : "v"(rr[0]), "v"(rr[1]), "v"(rr[2]), "v"(rr[3]));
            dsw2<0, 8>(rdw, q0, q1); dsw2<2, 10>(rdw, q2, q3);
        }
        bar();
        {
            f32x2 a0, a1, a2, a3;
            asm volatile("ds_read_b64 %0, %4\n\tds_read_b64 %1, %4 offset:256\n\tds_read_b64 %2, %4 offset:512\n\tds_read_b64 %3, %4 offset:768\n\ts_waitcnt lgkmcnt(0)"
                         : "=&v"(a0), "=&v"(a1), "=&v"(a2), "=&v"(a3) : "v"(rdr) : "memory");
            const f32x2 t = (a0 + a1) + (a2 + a3);
            unsigned mv = pk(t.x, t.y);
            unsigned now = 0; bool late = false;
            if (w >= 2) {
                now = rt(); late = s == 0 || (int)(now + TV2 - T2) > 0;
                if (late && s > 1) mg = min(mg + MUP, 24u);
                const unsigned n1 = min((late ? now + TV2 + TPEN : T2) + X + mg, SENT - 1024);
                mv = (lane & 1) ? n1 + Y + (mg >> 1) : n1;
            }
            st4(rw, mso, mv, ho0 + (HO_MEM + rnext * MRS) * 16384 + (w < 2 ? b * 128 : 0));
            if (w < 2) { now = rt(); late = s == 0 || (int)(now + TV2 - T2) > 0; }
            SB();
            {
                const u32x4 z = sent4();
                st16(rw, l16, z, pb); st16(rw, l16, z, pb + 1024);
                st4(rw, mso, z.x, ho0 + (HO_MEM + ring * MRS) * 16384 + (w < 2 ? b * 128 : 0));
            }
            if (late) T2 = now + TV2 + TM;
        }
        if (w == 1) *(LAS unsigned*)(lds + L_TS + 1024) = qmin;
        ST(7);
        ring = rnext;
        ST(8);
        }
    };
    for (int s = 0; s < NSEG - 1; ++s) step(s, bool_c<false>{});
    step(NSEG - 1, bool_c<true>{});
    drain(); bar();
    st4(rw, t0o, NSEG, kflag);
}

template <int ROLE>
DI void stream_role(const Params& p, lptr lds, const int j, const int tid) {
    const int w = __builtin_amdgcn_readfirstlane(tid >> 6), lane = tid & 63, n = lane & 15, g = lane >> 4, pp = lane & 3;
    const rsrc_t rw = mkrs(p.ws, WS_END);
    const float* mod = (const float*)(p.ws + WS_MOD);
    const unsigned cfg = get_cfg(p.ws);
    unsigned cap = 1u << 17;
    const unsigned l16 = lane * 16, t0o = tid == 64 ? 0u : OOB;
    const int cgp = (j & 7) + 8 * (j >> 4), qtt = (j >> 3) & 1, qm = cgp >> 5, qh = cgp & 31;
    u32x4 xv[4], pv[32]; f32x4 mv[4];
#pragma unroll
    for (int r = 0; r < 4; ++r) xv[r] = *(const u32x4*)(p.x + (size_t)((j & 127) * 16 + w * 4 + r) * D + lane * 8);
    mv[0] = *(const f32x4*)(mod + lane * 8); mv[1] = *(const f32x4*)(mod + lane * 8 + 4); mv[2] = *(const f32x4*)(mod + 512 + lane * 8); mv[3] = *(const f32x4*)(mod + 516 + lane * 8);
    const u16* Wsrc = (qm == 0 ? p.Wq : qm == 1 ? p.Wk : p.Wv) + qh * 128;
    if (j >= 128) panel_issue(Wsrc, Wsrc + 64, HD, tid, pv);
    if (j < 128) {
        float t[8], sh[8];
#pragma unroll
        for (int i = 0; i < 8; ++i) { t[i] = rb(1.f + mv[2 + (i >> 2)][i & 3]); sh[i] = mv[i >> 2][i & 3]; }
#pragma unroll
        for (int r = 0; r < 4; ++r) {
            float v[8] = {bflo(xv[r].x), bfhi(xv[r].x), bflo(xv[r].y), bfhi(xv[r].y), bflo(xv[r].z), bfhi(xv[r].z), bflo(xv[r].w), bfhi(xv[r].w)};
            float sum = ((v[0] + v[1]) + (v[2] + v[3])) + ((v[4] + v[5]) + (v[6] + v[7]));
            sum += dpp<0xB1>(sum); sum += dpp<0x4E>(sum); sum += dpp<0x124>(sum); sum += dpp<0x128>(sum); sum = xsum(sum);
            const float mean = sum * (1.f / D); float var = 0.f;
#pragma unroll
            for (int i = 0; i < 8; ++i) { const float d = v[i] - mean; var += d * d; }
            var += dpp<0xB1>(var); var += dpp<0x4E>(var); var += dpp<0x124>(var); var += dpp<0x128>(var); var = xsum(var);
            const float mb = rb(mean), sd = 1.f / rb(sqrtf(rb(rb(var * (1.f / D)) + 1e-5f)));
#pragma unroll
            for (int i = 0; i < 8; ++i) v[i] = lnmod(v[i], mb, sd, t[i], sh[i]);
            st16(rw, WS_H1 + (unsigned)(j * 16 + (lane >> 2)) * 1024u + ((w * 4 + r) * 4 + (lane & 3)) * 16, (u32x4){pk(v[0], v[1]), pk(v[2], v[3]), pk(v[4], v[5]), pk(v[6], v[7])});
        }
    }
    SU(64 + j, 0);
    u32x4 qa[16], qb[16];
    if (j < 128) {
        SB();
        panel_issue(Wsrc, Wsrc + 64, HD, tid, pv);
        asm volatile("s_waitcnt vmcnt(32)" ::: "memory"); bar();
        st4(rw, t0o, 1, flg(FL_LN + j, cfg));
    }
    panel_finish(lds, tid, 32 * w + 8 * pp, 32 * w + 8 * pp + 4, pv, qa, qb);
    SU(64 + j, 1);
    if (w == 0) wait_flags(rw, cfg, FL_LN + qtt, 64, 2, 1, lane, cap);
    bar();
    SU(64 + j, 2);
    u32x4 hv[4];
    const unsigned h1off = WS_H1 + (unsigned)(qtt * 16 + w * 4) * 1024u;
    auto h1_issue = [&](int s) {
#pragma unroll
        for (int i = 0; i < 4; ++i) hv[i] = ld16(rw, (n * 4 + g) * 16, h1off + (unsigned)s * 32768u + i * 1024); };
    auto qkv_in = [&](int s) {
#pragma unroll
        for (int i = 0; i < 4; ++i) ldsw128(lds + L_Q + (w * 4 + i) * 1024 + lane * 16, hv[i]);
    };
    auto qkv_mm = [&](int s) {
        f32x4 ca, cb;
        if (qm < 2) tile_mma<false>(lds + L_Q, lane, qa, qb, ca, cb); else tile_mma<true>(lds + L_Q, lane, qa, qb, ca, cb);
        const unsigned blk = qm < 2 ? qm * 8 + qtt * 4 + w : 16 + w * 2 + qtt;
        st16(rw, l16, (u32x4){pk(ca[0], ca[1]), pk(ca[2], ca[3]), pk(cb[0], cb[1]), pk(cb[2], cb[3])}, WS_QKV + (unsigned)(s * 32 + qh) * 24576u + blk * 1024);
    };
    auto qkv_seg = [&](int s) { qkv_in(s); h1_issue(min(s + 1, NSEG - 1)); bar(); qkv_mm(s); };
    auto publish = [&](unsigned line, unsigned v) { drain(); bar(); st4(rw, t0o, v, flg(line, cfg)); };
    h1_issue(0);
    const int r = ROLE == 0 ? j : ROLE == 1 ? j - 64 : ROLE == 2 ? j - 120 : ROLE == 3 ? j - 152 : j - 168;
    const int tt = ROLE == 3 ? r >> 3 : ROLE == 1 ? r & 1 : (r >> 3) & 1;
    const int cgx = ROLE == 1 ? r >> 1 : (r & 7) + 8 * (r >> 4);
    qkv_seg(0); publish(FL_QKV + j, 1);
    SU(64 + j, 3);
    for (int s = 1; s < 7; s += 2) { qkv_seg(s); bar(); qkv_seg(s + 1); publish(FL_QKV + j, s + 2); }
    SU(64 + j, 4);
    if (ROLE == 0) { const u16* W = p.Wo + (size_t)(r & 7) * 512 * D + (r >> 4) * 128; panel_issue(W, W + 64, D, tid, pv); }
    if (ROLE == 1) panel_issue(p.wg + cgx * 64, p.wu + cgx * 64, F, tid, pv);
    SB();
    qkv_seg(7); publish(FL_QKV + j, 8);
    u32x4 sa[16], sb[16];
    if (ROLE == 0) panel_finish(lds, tid, 32 * w + 4 * pp, 32 * w + 16 + 4 * pp, pv, sa, sb);
    if (ROLE == 1) panel_finish(lds, tid, 16 * w + 4 * pp, 64 + 16 * w + 4 * pp, pv, sa, sb);
    if (ROLE == 2) {
        const rsrc_t rd = mkrs(p.wd + cgx * 32, (1791 * D + 32) * 2);
#pragma unroll
        for (int half = 0; half < 2; ++half) {
            u32x4 v[14];
#pragma unroll
            for (int i = 0; i < 14; ++i) v[i] = ldw(rd, ((tid >> 2) * D + (tid & 3) * 8) * 2, (half * 896 + i * 64) * D * 2);
            bar();
#pragma unroll
            for (int i = 0; i < 14; ++i) ldsw128(lds + ((tid >> 2) + 64 * i) * 80 + (tid & 3) * 16, v[i]);
            bar();
            if ((w >> 1) == half) {
                lptr pa = lds + ((w & 1) * 448 + 8 * g + (n >> 2)) * 80 + pp * 8;
#pragma unroll
                for (int ks = 0; ks < 14; ++ks) { sa[ks] = tr2(pa + ks * 32 * 80, 4 * 80); sb[ks] = tr2(pa + ks * 32 * 80 + 32, 4 * 80); }
            }
        }
        bar();
    }
    for (int s = 8; s < LEAD; s += 2) { qkv_seg(s); bar(); qkv_seg(s + 1); publish(FL_QKV + j, s + 2); }
    SU(64 + j, 5);
    unsigned poff; unsigned ahead = 0;
    if (ROLE == 0) poff = FL_A + (r & 7) * 2 + (lane & 1);
    if (ROLE == 1) poff = FL_L + tt * 8 + (lane & 7);
    if (ROLE == 2) poff = FL_C + tt + 2 * (lane < 28 ? lane : 0);
    if (ROLE == 3) poff = FL_B + (lane & 7) + tt * 8 + ((lane >> 3) & 3) * 16;
    if (ROLE == 4) poff = FL_G;
    if (ROLE == 5) {
        const int hh = 2 * r + ((lane >> 1) & 1), cg3 = ((lane >> 2) & 3) * 32 - 32 + hh;
        poff = (lane & 12) == 0 ? FL_KM + (hh & 3) + 8 * (hh >> 2) + 4 * (lane & 1) : FL_QKV + (cg3 & 7) + 16 * (cg3 >> 3) + 8 * (lane & 1);
        ahead = (lane & 12) != 0;
        if (lane == 63) { poff = FL_G; ahead = 0; }
    }
    poff = flg(poff, cfg);
    const unsigned oflag = flg((ROLE == 0 ? FL_B : ROLE == 1 ? FL_C : ROLE == 3 ? FL_L : FL_A) + r, cfg), qflag = flg(FL_QKV + j, cfg);
    unsigned seen = 0;
    auto ready = [&](unsigned nd) {
        if (ROLE != 5) return __all(seen >= nd);
        const unsigned gl = __builtin_amdgcn_readlane(seen, 63);
        return __all(((lane & 12) == 0 ? max(seen, gl) : lane == 63 ? nd : seen) >= nd);
    };
    auto wait_in = [&](unsigned need) {
        if (w == 0) {
            const unsigned nd = min(need + ahead, (unsigned)NSEG);
            for (unsigned spins = 0; !ready(nd); ++spins) {
                if (spins > cap) { cap = 0; break; }
                if (spins) __builtin_amdgcn_s_sleep(2);
                asm volatile("" ::: "memory");
                seen = ld4(rw, poff);
            }
        }
        bar();
    };
    const unsigned pof0 = w == 0 ? poff : OOB;
    const int hh = 2 * r + (w >> 1), att = w & 1;
    u32x4 xq[4], ka[4], kb[4], vb[8];
    const unsigned l16b = att ? l16 : OOB;
    auto a_issue_qk = [&](int s) {
        const unsigned qb_ = WS_QKV + (unsigned)(s * 32 + hh) * 24576u;
#pragma unroll
        for (int ks = 0; ks < 4; ++ks) { xq[ks] = ld16(rw, l16, qb_ + att * 4096 + ks * 1024); ka[ks] = ld16(rw, l16, qb_ + 8192 + ks * 1024); kb[ks] = ld16(rw, l16b, qb_ + 12288 + ks * 1024); }
    };
    auto a_issue_v = [&](int s) {
        const unsigned qb_ = WS_QKV + (unsigned)(s * 32 + hh) * 24576u;
#pragma unroll
        for (int i = 0; i < 8; ++i) vb[i] = ld16(rw, (i & 1) ? l16b : l16, qb_ + 16384 + i * 1024);
    };
    if (ROLE == 5) { wait_in(0); a_issue_qk(0); a_issue_v(0); }
    const int d0 = 128 * w + 16 * (lane >> 3) + 4 * (lane & 3);
    f32x4 cga, cgb, cgc;
    if (ROLE == 3) { cga = *(const f32x4*)(mod + 1024 + d0); cgb = *(const f32x4*)(mod + 1536 + d0); cgc = *(const f32x4*)(mod + 2048 + d0);
#pragma unroll
        for (int i = 0; i < 4; ++i) cgc[i] = rb(1.f + cgc[i]); }
    if (ROLE == 2) cga = *(const f32x4*)(mod + 2560 + cgx * 32 + (w & 1) * 16 + 4 * g);
    qkv_in(LEAD);
    auto iter = [&](const int s, auto doq_) {
        constexpr bool doq = decltype(doq_)::value;
        u32x2 xv = {0, 0};
        wait_in(s + 1);
        u32x4 a[14];
        if (ROLE == 0) {
#pragma unroll
            for (int i = 0; i < 4; ++i) a[i] = ld16(rw, l16, loc(WS_O, (unsigned)((s * 2 + tt) * 128 + (r & 7) * 16 + w * 4) * 1024u, cfg, 1) + i * 1024);
        }
        if (ROLE == 1) {
#pragma unroll
            for (int i = 0; i < 4; ++i) a[i] = ld16(rw, (n * 4 + g) * 16, loc(WS_H2, (unsigned)((s * 2 + tt) * 16 + w * 4) * 1024u, cfg, 0) + i * 1024);
        }
        if (ROLE == 2) {
#pragma unroll
            for (int ks = 0; ks < 14; ++ks) a[ks] = ld16(rw, (g >> 1) * 512 + n * 32 + (g & 1) * 16, loc(WS_G, (unsigned)((s * 2 + tt) * 56 + w * 14 + ks) * 1024u, cfg, 1));
            xv = ld8(rw, (n >> 3) * 16384 + (n & 7) * 1024 + ((w & 1) * 16 + 4 * g) * 2, loc(WS_X1, (s * 2 + tt) * 16384, cfg, 1) + cgx * 64);
        }
        if (ROLE == 3) {
#pragma unroll
            for (int kg = 0; kg < 8; ++kg) a[kg] = ld16(rw, (lane >> 3) * 1024 + (lane & 7) * 16, loc(WS_PB, (unsigned)(((s * 8 + kg) * 2 + tt) * 32 + 8 * w) * 1024u, cfg, 1) + (r & 7) * 128);
            xv = *(const u32x2*)(p.x + (size_t)(s * 32 + tt * 16 + 2 * (r & 7) + ((lane >> 2) & 1)) * D + d0);
        }
        if (ROLE == 5) {
            const unsigned kb_ = kvm_off(WS_HO, cfg, s * 32 + hh);
#pragma unroll
            for (int ks = 0; ks < 4; ++ks) a[ks] = ld16(rw, ((2 * (n >> 2) + (n & 1)) * 4 + g) * 16, kb_ + ks * 512);
            a[4] = ld16(rw, l16, kb_ + 2048); a[5] = ld16(rw, l16, kb_ + 3072);
        }
        seen = ld4(rw, pof0);
        if (doq) h1_issue(min(s + LEAD + 1, NSEG - 1));
        SB();
        if (doq) qkv_mm(s + LEAD);
        SB();
        if (ROLE == 0 || ROLE == 1) {
#pragma unroll
            for (int i = 0; i < 4; ++i) ldsw128(lds + L_A + (w * 4 + i) * 1024 + lane * 16, a[i]);
            bar();
            f32x4 ca, cb; tile_mma<false>(lds + L_A, lane, sa, sb, ca, cb);
            if (ROLE == 0) {
                const unsigned o = loc(WS_PB, (unsigned)(((s * 8 + (r & 7)) * 2 + tt) * 32 + (r >> 4) * 8 + w * 2) * 1024u, cfg, 1);
                st16(rw, (n * 4 + g) * 16, __builtin_bit_cast(u32x4, ca), o); st16(rw, (n * 4 + g) * 16, __builtin_bit_cast(u32x4, cb), o + 1024);
            } else {
                float q[4];
#pragma unroll
                for (int i = 0; i < 4; ++i) { const float gv = rb(ca[i]); q[i] = rb(gv * __builtin_amdgcn_rcpf(1.f + __expf(-gv))) * rb(cb[i]); }
                st8(rw, n * 32 + g * 8, (u32x2){pk(q[0], q[1]), pk(q[2], q[3])}, loc(WS_G, (unsigned)((s * 2 + tt) * 56 + cgx * 2 + (w >> 1)) * 1024u, cfg, 1) + (w & 1) * 512);
            }
        }
        if (ROLE == 2) {
            f32x4 c0 = {0, 0, 0, 0}, c1 = c0;
            asm volatile("s_nop 1" : "+v"(c0), "+v"(c1));
#pragma unroll
            for (int ks = 0; ks < 14; ++ks) { MFWA(c0, sa[ks], a[ks]); MFWA(c1, sb[ks], a[ks]); }
            asm volatile("s_nop 7" : "+v"(c0), "+v"(c1));
            *(LAS f32x4*)(lds + L_A + (w * 2) * 1024 + lane * 16) = c0; *(LAS f32x4*)(lds + L_A + (w * 2 + 1) * 1024 + lane * 16) = c1;
            bar();
            if (w < 2) {
                lptr q = lds + L_A + w * 1024 + lane * 16;
                const f32x4 y = (*(LAS f32x4*)q + *(LAS f32x4*)(q + 2048)) + (*(LAS f32x4*)(q + 4096) + *(LAS f32x4*)(q + 6144));
                const float o0 = bflo(xv.x) + rb(cga[0] * rb(y[0])), o1 = bfhi(xv.x) + rb(cga[1] * rb(y[1])), o2 = bflo(xv.y) + rb(cga[2] * rb(y[2])), o3 = bfhi(xv.y) + rb(cga[3] * rb(y[3]));
                *(u32x2*)(p.out + (size_t)(s * 32 + tt * 16 + n) * D + cgx * 32 + (w & 1) * 16 + 4 * g) = (u32x2){pk(o0, o1), pk(o2, o3)};
            }
        }
        if (ROLE == 5) {
            f32x4 x0, x1, x2;
            MF0(x1, ka[0], xq[0]); MF0(x2, kb[0], xq[0]);
#pragma unroll
            for (int ks = 1; ks < 4; ++ks) { MF(x1, ka[ks], xq[ks]); MF(x2, kb[ks], xq[ks]); }
            MF0(x0, a[0], xq[0]); MF(x0, a[1], xq[1]); MF(x0, a[2], xq[2]); MF(x0, a[3], xq[3]);
            a_issue_qk(min(s + 1, NSEG - 1));
            asm volatile("s_nop 7" : "+v"(x0), "+v"(x1), "+v"(x2));
            SB();
            const float raw[10] = {x1[0], x1[1], x1[2], x1[3], x2[0], x2[1], x2[2], x2[3], x0[0], x0[1]};
            float sc[10];
            const int qi = 16 * att + n - 4 * g;
#pragma unroll
            for (int i = 0; i < 5; ++i) {
                const unsigned u = vcvt(raw[2 * i], raw[2 * i + 1]);
                const unsigned v = pk(bflo(u) * 0.08838834764831845f, bfhi(u) * 0.08838834764831845f);
                sc[2 * i] = bflo(v); sc[2 * i + 1] = bfhi(v);
            }
#pragma unroll
            for (int i = 0; i < 8; ++i) sc[i] = ((i & 3) + 16 * (i >> 2) <= qi) ? sc[i] : -1e9f;
            float mx = vmax3(vmax3(vmax3(sc[0], sc[1], sc[2]), vmax3(sc[3], sc[4], sc[5]), vmax(sc[6], sc[7])), sc[8], sc[9]);
            mx = xmax(mx) * -1.4426950408889634f;
#pragma unroll
            for (int i = 0; i < 10; ++i) sc[i] = __builtin_amdgcn_exp2f(__builtin_fmaf(sc[i], 1.4426950408889634f, mx));
            const float inv = __builtin_amdgcn_rcpf(xsum((((sc[0] + sc[1]) + (sc[2] + sc[3])) + ((sc[4] + sc[5]) + (sc[6] + sc[7]))) + (sc[8] + sc[9])));
            u32x4 xpx = {pk(sc[0] * inv, sc[1] * inv), pk(sc[2] * inv, sc[3] * inv), pk(sc[4] * inv, sc[5] * inv), pk(sc[6] * inv, sc[7] * inv)};
            u32x4 xpm = {pk(sc[8] * inv, sc[9] * inv), 0u, 0u, 0u};
            f32x4 o[8];
#pragma unroll
            for (int e = 0; e < 4; ++e) {
                u32x4 fa = {vb[2 * e].x, vb[2 * e].y, vb[2 * e + 1].x, vb[2 * e + 1].y}, fb = {vb[2 * e].z, vb[2 * e].w, vb[2 * e + 1].z, vb[2 * e + 1].w};
                u32x4 ma = vb[2 * e], mb = vb[2 * e + 1];
                ma.x = a[4 + (e >> 1)][2 * (e & 1)]; mb.x = a[4 + (e >> 1)][2 * (e & 1) + 1];
                asm volatile("s_nop 1" : "+v"(fa), "+v"(fb), "+v"(ma), "+v"(mb), "+v"(xpx), "+v"(xpm));
                MF0(o[2 * e], fa, xpx); MF0(o[2 * e + 1], fb, xpx); MF(o[2 * e], ma, xpm); MF(o[2 * e + 1], mb, xpm);
            }
            asm volatile("s_nop 7" : "+v"(o[0]), "+v"(o[1]), "+v"(o[2]), "+v"(o[3]), "+v"(o[4]), "+v"(o[5]), "+v"(o[6]), "+v"(o[7]));
#pragma unroll
            for (int e = 0; e < 4; ++e)
                st16(rw, l16, (u32x4){pk(o[2 * e][0], o[2 * e][1]), pk(o[2 * e][2], o[2 * e][3]), pk(o[2 * e + 1][0], o[2 * e + 1][1]), pk(o[2 * e + 1][2], o[2 * e + 1][3])}, loc(WS_O, (unsigned)((s * 2 + att) * 128 + hh * 4) * 1024u, cfg, 1) + e * 1024);
            SB();
            a_issue_v(min(s + 1, NSEG - 1));
        }
        if (ROLE == 3) {
            LAS float* sm = (LAS float*)(lds + L_A);
            const int tk = (lane >> 2) & 1, tok = 2 * (r & 7) + tk;
            f32x4 acc = __builtin_bit_cast(f32x4, a[0]);
#pragma unroll
            for (int kg = 1; kg < 8; ++kg) acc += __builtin_bit_cast(f32x4, a[kg]);
            float x1[4] = {bflo(xv.x), bfhi(xv.x), bflo(xv.y), bfhi(xv.y)};
            float sum = 0.f;
#pragma unroll
            for (int i = 0; i < 4; ++i) { x1[i] = rb(x1[i] + rb(cga[i] * rb(acc[i]))); sum += x1[i]; }
            st8(rw, tk * 1024 + d0 * 2, (u32x2){pk(x1[0], x1[1]), pk(x1[2], x1[3])}, loc(WS_X1, (s * 32 + tt * 16 + 2 * (r & 7)) * 1024, cfg, 1));
            sum += dpp<0xB1>(sum); sum += dpp<0x4E>(sum); sum += dpp<0x128>(sum); sum = xsum(sum);
            if (lane == 0 || lane == 4) sm[w * 2 + tk] = sum;
            bar();
            const float mean = ((sm[tk] + sm[2 + tk]) + (sm[4 + tk] + sm[6 + tk])) * (1.f / D);
            float var = 0.f;
#pragma unroll
            for (int i = 0; i < 4; ++i) { const float d = x1[i] - mean; var += d * d; }
            var += dpp<0xB1>(var); var += dpp<0x4E>(var); var += dpp<0x128>(var); var = xsum(var);
            if (lane == 0 || lane == 4) sm[8 + w * 2 + tk] = var;
            bar();
            var = ((sm[8 + tk] + sm[10 + tk]) + (sm[12 + tk] + sm[14 + tk])) * (1.f / D);
            const float mb = rb(mean), sd = 1.f / rb(sqrtf(rb(rb(var) + 1e-5f)));
            float hh2[4];
#pragma unroll
            for (int i = 0; i < 4; ++i) hh2[i] = lnmod(x1[i], mb, sd, cgc[i], cgb[i]);
            st8(rw, ((d0 >> 5) & 3) * 1024 + tok * 64 + (d0 & 31) * 2, (u32x2){pk(hh2[0], hh2[1]), pk(hh2[2], hh2[3])}, loc(WS_H2, (unsigned)((s * 2 + tt) * 16 + 4 * w) * 1024u, cfg, 0));
        }
        if (ROLE == 5) asm volatile("s_waitcnt vmcnt(8)" ::: "memory"); else drain();
        bar();
        if (ROLE != 2 && ROLE != 4) st4(rw, t0o, s + 1, oflag);
        if (doq) st4(rw, t0o, s + LEAD + 1, qflag);
        if (doq) qkv_in(s + 1 + LEAD);
    };
    int s = 0;
    for (; s < NSEG - LEAD; ++s) iter(s, bool_c<true>{});
    if (ROLE != 4) for (; s < NSEG; ++s) iter(s, bool_c<false>{});
}

__global__ void __launch_bounds__(256, 1) k_main(Params p) {
    extern __shared__ __attribute__((aligned(16))) char smem[];
    const lptr lds = (lptr)smem;
    const int b = blockIdx.x, tid = threadIdx.x;
    if (b < 128 && (b & 7) < 4) chain_role(p, lds, (b >> 3) * 4 + (b & 3), tid);
    else {
        const int u = (b >> 3) * 4 + (b & 3);
        const int j = (b & 4) ? (u < 64 ? u : u + 56) : (u < 120 ? u : u + 64);
        if (j < 64) stream_role<0>(p, lds, j, tid);
        else if (j < 120) stream_role<1>(p, lds, j, tid);
        else if (j < 152) stream_role<2>(p, lds, j, tid);
        else if (j < 168) stream_role<3>(p, lds, j, tid);
        else if (j < 184) stream_role<5>(p, lds, j, tid);
        else stream_role<4>(p, lds, j, tid);
    }
}

extern "C" void kernel_launch(void* const* d_in, const int* in_sizes, int n_in, void* d_out, int out_size, void* d_ws, size_t ws_size, hipStream_t stream) {
    Params p;
    p.x = (const u16*)d_in[0]; p.mem0 = (const u16*)d_in[1]; p.Wq = (const u16*)d_in[2]; p.Wk = (const u16*)d_in[3]; p.Wv = (const u16*)d_in[4]; p.Wo = (const u16*)d_in[5];
    p.wg = (const u16*)d_in[8]; p.wu = (const u16*)d_in[9]; p.wd = (const u16*)d_in[10]; p.c = (const u16*)d_in[11]; p.wmod = (const u16*)d_in[12];
    p.out = (u16*)d_out; p.ws = (char*)d_ws + ((0x200000 - ((uintptr_t)d_ws & 0x1FFFFF)) & 0x1FFFFF);
    constexpr int LDSB = 96 * 1024;
    (void)hipFuncSetAttribute((const void*)k_main, hipFuncAttributeMaxDynamicSharedMemorySize, LDSB);
    k_init<<<512, 256, 0, stream>>>(p);
    k_main<<<256, 256, LDSB, stream>>>(p);
}
```

```cpp
#include <hip/hip_runtime.h>
#include <hip/hip_bf16.h>
#include <stdint.h>

typedef unsigned short u16;
typedef __bf16 bf16x2 __attribute__((ext_vector_type(2)));
typedef __bf16 bf16x8 __attribute__((ext_vector_type(8)));
typedef short s16x4 __attribute__((ext_vector_type(4)));
typedef float f32x4 __attribute__((ext_vector_type(4)));
typedef float f32x2 __attribute__((ext_vector_type(2)));
typedef unsigned u32x2 __attribute__((ext_vector_type(2)));
typedef unsigned u32x4 __attribute__((ext_vector_type(4)));
typedef unsigned u32x8 __attribute__((ext_vector_type(8)));
#define LAS __attribute__((address_space(3)))
typedef LAS char* lptr;
#define DI __device__ __forceinline__

#define TX 50
#define TY 42
#define TV1 12
#define TV2 12
#define TPEN 20
#define TM 10
#define SIDE 0
#define SLP 5
#define PREP 5
#define FDLY 0
#define CX 2150
#define CY 430
#define MUP 2
#define PSL 1
#define TS_ 6
#ifndef NVX
#define NVX 3
#endif
#define FW ""
#define PRE 2
#define NPRE 4
#define LEAD 16

constexpr int S = 2048, D = 512, NSEG = 64, F = 1792, HD = 4096;
constexpr unsigned SENT = 0xFFFFFFFFu;
constexpr int NRING = 3;
constexpr unsigned WS_MOD = 0;
constexpr unsigned WS_FLAG = 131072;
constexpr unsigned FL_LN = 0, FL_QKV = 128, FL_L = 320, FL_KM = 384, FL_B = 448, FL_C = 512, FL_A = 576, FL_G = 600, FL_N = 640;
constexpr unsigned WS_CFG = 12544;
#define NC 4
constexpr unsigned WS_HO = 220u << 20, HO_MEM = 256, MRS = NC + 1, HO_N = HO_MEM + 3 * MRS;
constexpr unsigned WS_DBG = 3u << 20;
constexpr unsigned WS_H1 = 4u << 20, WS_X1 = 6u << 20, WS_H2 = 10u << 20, WS_G = 14u << 20, WS_O = 28u << 20, WS_QKV = 60u << 20, WS_PB = 156u << 20, WS_END = 246u << 20;

DI float bflo(unsigned x) { return __uint_as_float(x << 16); }
DI float bfhi(unsigned x) { return __uint_as_float(x & 0xffff0000u); }
DI unsigned pk(float lo, float hi) { bf16x2 v = {(__bf16)lo, (__bf16)hi}; return __builtin_bit_cast(unsigned, v); }
DI float rb(float x) { return (float)(__bf16)x; }
DI void bar() { asm volatile("s_waitcnt lgkmcnt(0)\n\ts_barrier" ::: "memory"); }
DI void drain() { asm volatile("s_waitcnt vmcnt(0)" ::: "memory"); }
DI u32x2 trread(lptr p) { return __builtin_bit_cast(u32x2, __builtin_amdgcn_ds_read_tr16_b64_v4i16((LAS s16x4*)p)); }
DI u32x4 tr2(lptr p, int off) { const u32x2 a = trread(p), b = trread(p + off); return (u32x4){a.x, a.y, b.x, b.y}; }
DI u32x4 ldsr128(lptr p) { return *(LAS u32x4*)p; }
DI void ldsw128(lptr p, u32x4 v) { *(LAS u32x4*)p = v; }
DI void ldsw64(lptr p, u32x2 v) { *(LAS u32x2*)p = v; }
DI void ldsw32(lptr p, unsigned v) { *(LAS unsigned*)p = v; }
DI f32x4 mfma32(u32x4 a, u32x4 b, f32x4 c) { return __builtin_amdgcn_mfma_f32_16x16x32_bf16(__builtin_bit_cast(bf16x8, a), __builtin_bit_cast(bf16x8, b), c, 0, 0, 0); }
DI float swadd32(float a, float b) {
    auto r = __builtin_amdgcn_permlane32_swap(__float_as_uint(a), __float_as_uint(b), false, false);
    return __uint_as_float(r[0]) + __uint_as_float(r[1]);
}
DI float vmax(float a, float b) { float r; asm("v_max_f32 %0, %1, %2" : "=v"(r) : "v"(a), "v"(b)); return r; }
DI float vmax3(float a, float b, float c) { float r; asm("v_max3_f32 %0, %1, %2, %3" : "=v"(r) : "v"(a), "v"(b), "v"(c)); return r; }
DI float xmax(float x) {
    auto s = __builtin_amdgcn_permlane16_swap(__float_as_uint(x), __float_as_uint(x), false, false);
    x = vmax(__uint_as_float(s[0]), __uint_as_float(s[1]));
    auto t = __builtin_amdgcn_permlane32_swap(__float_as_uint(x), __float_as_uint(x), false, false);
    return vmax(__uint_as_float(t[0]), __uint_as_float(t[1]));
}
DI float xsum(float x) {
    auto s = __builtin_amdgcn_permlane16_swap(__float_as_uint(x), __float_as_uint(x), false, false);
    x = __uint_as_float(s[0]) + __uint_as_float(s[1]);
    auto t = __builtin_amdgcn_permlane32_swap(__float_as_uint(x), __float_as_uint(x), false, false);
    return __uint_as_float(t[0]) + __uint_as_float(t[1]);
}
template <int CTRL> DI float dpp(float x) { return __builtin_bit_cast(float, __builtin_amdgcn_mov_dpp(__builtin_bit_cast(int, x), CTRL, 0xf, 0xf, true)); }
template <int CTRL> DI int dppi(int x) { return __builtin_amdgcn_mov_dpp(x, CTRL, 0xf, 0xf, true); }
DI unsigned mx4(u32x4 v, unsigned m) { return max(max(max(v.x, v.y), max(v.z, v.w)), m); }
typedef __amdgpu_buffer_rsrc_t rsrc_t;
DI rsrc_t mkrs(const void* p, unsigned bytes) { return __builtin_amdgcn_make_buffer_rsrc((void*)p, 0, bytes, 0x00020000); }
DI u32x4 ld16(rsrc_t r, unsigned off, unsigned soff = 0) { return __builtin_amdgcn_raw_buffer_load_b128(r, off, soff, 16); }
DI u32x2 ld8(rsrc_t r, unsigned off, unsigned soff) { return __builtin_amdgcn_raw_buffer_load_b64(r, off, soff, 16); }
DI unsigned ld4(rsrc_t r, unsigned off, unsigned soff = 0) { return __builtin_amdgcn_raw_buffer_load_b32(r, off, soff, 16); }
DI void st16(rsrc_t r, unsigned off, u32x4 v, unsigned soff = 0) { __builtin_amdgcn_raw_buffer_store_b128(v, r, off, soff, 16); }
DI void st8(rsrc_t r, unsigned off, u32x2 v, unsigned soff) { __builtin_amdgcn_raw_buffer_store_b64(v, r, off, soff, 16); }
DI void st4(rsrc_t r, unsigned off, unsigned v, unsigned soff = 0) { __builtin_amdgcn_raw_buffer_store_b32(v, r, off, soff, 16); }
DI u32x4 ldw(rsrc_t r, unsigned voff, unsigned soff) { return __builtin_amdgcn_raw_buffer_load_b128(r, voff, soff, 0); }
DI unsigned rt() { return (unsigned)__builtin_amdgcn_s_memrealtime(); }
DI void wait_rt(unsigned t, unsigned q = 1) {
    int d;
    do d = (int)(t - rt()); while (d >= (int)q && d < 4096);
    d = min(d, 8);
    for (; d > 0; --d) asm volatile("");
    asm volatile("" ::: "memory");
}

template <bool V> struct bool_c { static constexpr bool value = V; };
DI unsigned loc(unsigned base, unsigned off, unsigned cfg, unsigned far) { const unsigned x = base + ((off >> 13) << 14) + (off & 8191); return x + ((((cfg >> ((x >> 21) & 1)) ^ far) & 1) << 13); }
DI unsigned qkv_off(unsigned i, unsigned sec, unsigned cfg) { return loc(WS_QKV, (i * 3 + sec) * 8192u, cfg, 1); }
DI unsigned flg(unsigned line, unsigned cfg) { return loc(WS_FLAG, line * 128, cfg, line >= FL_KM); }
DI unsigned kvm_off(unsigned ho, unsigned cfg, unsigned i) { return ho + (8u << 20) + (i >> 1) * 16384 + ((cfg >> ((i >> 8) & 1)) & 1) * 8192 + (i & 1) * 4096; }
DI unsigned get_cfg(const char* ws) {
    const f32x4 a = *(const f32x4*)(ws + WS_CFG), b = *(const f32x4*)(ws + WS_CFG + 16);
    return __builtin_amdgcn_readfirstlane(fminf(fminf(b[0], b[1]), fminf(b[2], b[3])) < fminf(fminf(a[0], a[1]), fminf(a[2], a[3])) ? 2u : 1u);
}
struct Params {
    const u16 *x, *mem0, *Wq, *Wk, *Wv, *Wo, *wg, *wu, *wd, *c, *wmod;
    u16* out; char* ws;
};

DI float lnmod(float v, float mb, float isd, float t, float sh) { return rb(rb(rb(v - mb) * isd) * t) + sh; }

DI void wait_flags(rsrc_t rw, unsigned cfg, unsigned first, int cnt, int stride, unsigned need, int lane, unsigned& cap) {
    const unsigned off = flg(first + (lane < cnt ? lane : 0) * stride, cfg);
    for (unsigned spins = 0;; ++spins) {
        asm volatile("" ::: "memory");
        if (__all(ld4(rw, off) >= need)) break;
        if (spins > cap) { cap = 0; break; }
        __builtin_amdgcn_s_sleep(2);
    }
    asm volatile("" ::: "memory");
}

constexpr int PSTR = 272;
DI void panel_issue(const u16* srcA, const u16* srcB, int ld, int tid, u32x4 (&v)[32]) {
    const rsrc_t ra = mkrs(srcA, (511 * ld + 64) * 2), rb_ = mkrs(srcB, (511 * ld + 64) * 2);
    const unsigned voff = ((tid >> 3) * ld + (tid & 7) * 8) * 2;
#pragma unroll
    for (int i = 0; i < 16; ++i) { v[2 * i] = ldw(ra, voff, i * 32 * ld * 2); v[2 * i + 1] = ldw(rb_, voff, i * 32 * ld * 2); }
}
DI void panel_finish(lptr lds, int tid, int colA, int colB, u32x4 (&v)[32], u32x4 (&wa)[16], u32x4 (&wb)[16]) {
    const int lane = tid & 63, g = lane >> 4, q = (lane & 15) >> 2;
    const lptr wp = lds + (tid >> 3) * PSTR + (tid & 7) * 16;
#pragma unroll
    for (int half = 0; half < 2; ++half) {
        bar();
#pragma unroll
        for (int i = 0; i < 8; ++i) { ldsw128(wp + i * 32 * PSTR, v[16 * half + 2 * i]); ldsw128(wp + i * 32 * PSTR + 128, v[16 * half + 2 * i + 1]); }
        bar();
        lptr pa = lds + (8 * g + q) * PSTR + colA * 2, pb = lds + (8 * g + q) * PSTR + colB * 2;
#pragma unroll
        for (int ks = 0; ks < 8; ++ks) { wa[half * 8 + ks] = tr2(pa + ks * 32 * PSTR, 4 * PSTR); wb[half * 8 + ks] = tr2(pb + ks * 32 * PSTR, 4 * PSTR); asm volatile("" : "+a"(wa[half * 8 + ks]), "+a"(wb[half * 8 + ks])); }
    }
    bar();
}
constexpr int L_A = 0, L_Q = 73728;
#define MFWA(acc, wgt, x) asm volatile("v_mfma_f32_16x16x32_bf16 %0, %1, %2, %0" : "+v"(acc) : "a"(wgt), "v"(x))
#define MFWB(acc, wgt, x) asm volatile("v_mfma_f32_16x16x32_bf16 %0, %2, %1, %0" : "+v"(acc) : "a"(wgt), "v"(x))
template <bool SWAP> DI void tile_mma(lptr lds, int lane, const u32x4 (&wa)[16], const u32x4 (&wb)[16], f32x4& ca, f32x4& cb) {
    ca = (f32x4){0, 0, 0, 0}; cb = ca;
    u32x4 a[16];
#pragma unroll
    for (int ks = 0; ks < 4; ++ks) a[ks] = ldsr128(lds + ks * 1024 + lane * 16);
    asm volatile("s_nop 1" : "+v"(ca), "+v"(cb));
    __builtin_amdgcn_sched_barrier(0);
#pragma unroll
    for (int ks = 0; ks < 16; ++ks) {
        if (SWAP) MFWB(ca, wa[ks], a[ks]); else MFWA(ca, wa[ks], a[ks]);
        if (ks + 4 < 16) a[ks + 4] = ldsr128(lds + (ks + 4) * 1024 + lane * 16);
        __builtin_amdgcn_sched_barrier(0);
        if (SWAP) MFWB(cb, wb[ks], a[ks]); else MFWA(cb, wb[ks], a[ks]);
    }
    asm volatile("s_nop 7" : "+v"(ca), "+v"(cb));
}

constexpr int CSTR = 656, OSTR = 1040;
constexpr int L_MEMF = 0, L_QT = 8192, L_KT = 12288, L_VT = 16384, L_RED = 20480, L_TS = 21504, L_SEL = 23552, L_ONES = 24576, L_QKV = 25600;
constexpr float SC_L2E = 0.08838834764831845f * 1.4426950408889634f;
constexpr unsigned OOB = 0x80000000u;
#define SMFA(acc, a, w) asm volatile("v_smfmac_f32_16x16x64_bf16 %0, %1, %2, %3" : "+v"(acc) : "v"(a), "a"(w), "v"(sidx))
#define SMFV(acc, a, w) asm volatile("v_smfmac_f32_16x16x64_bf16 %0, %1, %2, %3" : "+v"(acc) : "v"(a), "v"(w), "v"(sidx))
#define MF0(acc, a, b) asm volatile("v_mfma_f32_16x16x32_bf16 %0, %1, %2, 0" : "=&v"(acc) : "v"(a), "v"(b))
#define MF(acc, a, b) asm volatile("v_mfma_f32_16x16x32_bf16 %0, %1, %2, %0" : "+v"(acc) : "v"(a), "v"(b))
#define NOP2() asm volatile("s_nop 1")

#define SB() __builtin_amdgcn_sched_barrier(0)
DI f32x4 zero4() { f32x2 a, b; asm volatile("v_mov_b64 %0, 0\n\tv_mov_b64 %1, 0" : "=v"(a), "=v"(b)); return (f32x4){a.x, a.y, b.x, b.y}; }
DI u32x4 sent4() { u32x2 a, b; asm volatile("v_mov_b64 %0, -1\n\tv_mov_b64 %1, -1" : "=v"(a), "=v"(b)); return (u32x4){a.x, a.y, b.x, b.y}; }
DI float vadd(float a, float b) { float r; asm volatile("v_add_f32 %0, %1, %2" : "=v"(r) : "v"(a), "v"(b)); return r; }
DI unsigned vcvt(float a, float b) { unsigned r; asm volatile("v_cvt_pk_bf16_f32 %0, %1, %2" : "=v"(r) : "v"(a), "v"(b)); return r; }
template <int O0, int O1> DI void dsw2(lptr p, unsigned a, unsigned b) { asm volatile("ds_write2_b32 %0, %1, %2 offset0:%3 offset1:%4" :: "v"(p), "v"(a), "v"(b), "i"(O0), "i"(O1) : "memory"); }
#define ST(k)
#define ST2(k)
#define TS(k)
#define SU(id, k)
DI int wave_max_rel(unsigned v, unsigned base) {
    int d = (int)(v - base);
    d = max(d, dppi<0xB1>(d)); d = max(d, dppi<0x4E>(d)); d = max(d, dppi<0x124>(d)); d = max(d, dppi<0x128>(d));
    auto s = __builtin_amdgcn_permlane16_swap((unsigned)d, (unsigned)d, false, false); d = max((int)s[0], (int)s[1]);
    auto t = __builtin_amdgcn_permlane32_swap((unsigned)d, (unsigned)d, false, false); d = max((int)t[0], (int)t[1]);
    return __builtin_amdgcn_readfirstlane(d);
}

DI void chain_role(const Params& p, lptr lds, const int b, const int tid) {
    const int w = __builtin_amdgcn_readfirstlane(tid >> 6), lane = tid & 63, n = lane & 15, g = lane >> 4;
    const int h = (b & 3) + 4 * (b >> 3), hf = (b >> 2) & 1;
    u32x8 wq[2][8], wk[2][8], wv[8], wo[8];
    {
        const int q = n >> 2, pp = lane & 3;
        constexpr int WSTR = 272;
        u32x4 v[3][16];
        const unsigned vo = ((tid >> 3) * HD + (tid & 7) * 8) * 2;
        auto issue = [&](int r, u32x4 (&d)[16]) {
            if (r < 5) {
                const rsrc_t rs = mkrs((r < 2 ? p.Wq : r < 4 ? p.Wk : p.Wv) + h * 128 + (r < 4 ? (r & 1) : hf) * 64, (511 * HD + 64) * 2);
#pragma unroll
                for (int i = 0; i < 16; ++i) d[i] = ldw(rs, vo, i * 32 * HD * 2);
            } else {
                const rsrc_t rs = mkrs(p.Wo + (size_t)(h * 128 + hf * 64) * D, 65536);
#pragma unroll
                for (int i = 0; i < 16; ++i) d[i] = ldw(rs, tid * 16, i * 4096);
            } };
        issue(0, v[0]); issue(1, v[1]); issue(2, v[2]);
        const lptr wp = lds + (tid >> 3) * WSTR + (tid & 7) * 16;
        auto stage = [&](u32x4 (&d)[16], int half) {
#pragma unroll
            for (int i = 0; i < 16; ++i) ldsw128(wp + i * 32 * WSTR + half * 128, d[i]);
        };
        auto frag = [&](int j, int colbyte) { u32x8 f;
            const lptr base = lds + (((2 * w + j) & 7) * 64 + 8 * g + q) * WSTR + colbyte;
#pragma unroll
            for (int m = 0; m < 4; ++m) { const u32x2 t = trread(base + (32 * (m >> 1) + 4 * (m & 1)) * WSTR); f[2 * m] = t.x; f[2 * m + 1] = t.y; }
            return f; };
        auto take = [&](u32x8 (&dst)[2][8]) {
#pragma unroll
            for (int j = 0; j < 8; ++j) { dst[0][j] = frag(j, w * 64 + 8 * pp); dst[1][j] = frag(j, w * 64 + 32 + 8 * pp); asm volatile("" : "+a"(dst[0][j]), "+a"(dst[1][j])); } };
        stage(v[0], 0); issue(3, v[0]); stage(v[1], 1); issue(4, v[1]); bar(); take(wq); bar();
        stage(v[2], 0); issue(5, v[2]); stage(v[0], 1); bar(); take(wk); bar();
        stage(v[1], 0); bar();
#pragma unroll
        for (int j = 0; j < 8; ++j) wv[j] = frag(j, (32 * (w >> 1) + 8 * pp + 4 * (w & 1)) * 2);
        bar();
#pragma unroll
        for (int i = 0; i < 16; ++i) ldsw128(lds + ((tid >> 6) + 4 * i) * OSTR + (tid & 63) * 16, v[2][i]);
        bar();
        lptr base = lds + (32 * (q >> 1) + 4 * g + (q & 1)) * OSTR;
#pragma unroll
        for (int t = 0; t < 8; ++t) {
            const int col = 128 * w + 64 * (t >> 2) + 16 * pp + 4 * (t & 3);
#pragma unroll
            for (int m = 0; m < 4; ++m) { const u32x2 tv = trread(base + (16 * (m >> 1) + 2 * (m & 1)) * OSTR + col * 2); wo[t][2 * m] = tv.x; wo[t][2 * m + 1] = tv.y; }
        }
        bar();
    }
    const rsrc_t rw = mkrs(p.ws, WS_END);
    const int hi = (n >> 1) & 1, rowi = 2 * (n >> 2) + (n & 1);
    const int sidx = hi ? 0xEEEE : 0x4444;
    unsigned cap = 1u << 17;
    const unsigned l16 = lane * 16;
    const unsigned cfg = get_cfg(p.ws), ho = WS_HO;
    const unsigned ho0 = ho + (cfg & 1) * 8192, ho1 = ho + ((cfg >> 1) & 1) * 8192;
    const unsigned xc = (b & 3) + 4 * ((b >> 2) % (NC / 4));
    auto pblk = [&](unsigned ring_, unsigned r) { return (ring_ == 2 ? ho1 : ho0) + (ring_ * 64 + r) * 16384; };
    const unsigned t0o = tid == 0 ? 0u : OOB;
    auto qkv_issue = [&](int s, u32x4 (&v)[3]) {
        const unsigned kb_ = qkv_off(s * 32 + h, 1, cfg) + w * 2048;
        v[0] = ld16(rw, l16, kb_); v[1] = ld16(rw, l16, kb_ + 1024);
        v[2] = ld16(rw, l16, qkv_off(s * 32 + h, 2, cfg) + hf * 4096 + w * 1024);
    };
    auto qkv_store = [&](int s, u32x4 (&v)[3]) {
        lptr d = lds + L_QKV + (s & 1) * 12288 + lane * 16;
        ldsw128(d + w * 2048, v[0]); ldsw128(d + w * 2048 + 1024, v[1]);
        ldsw64(d + 8192 + (w >> 1) * 2048 + (w & 1) * 8, (u32x2){v[2].x, v[2].y}); ldsw64(d + 8192 + (w >> 1) * 2048 + 1024 + (w & 1) * 8, (u32x2){v[2].z, v[2].w});
    };
    const unsigned qflag = flg(FL_QKV + (h & 7) + ((((lane >> 1) & 1) * 32 + 32 + h) >> 3) * 16 + (lane & 1) * 8, cfg), kflag = __builtin_amdgcn_readfirstlane(flg(FL_KM + b, cfg)), gflag = __builtin_amdgcn_readfirstlane(flg(FL_G, cfg));
    auto qkv_wait = [&](unsigned need) { for (unsigned spins = 0;; ++spins) { asm volatile("" ::: "memory"); if (__all(ld4(rw, qflag) >= need)) break; if (spins > cap) { cap = 0; break; } __builtin_amdgcn_s_sleep(1); } asm volatile("" ::: "memory"); };
    SU(b, 0);
    { qkv_wait(1); SU(b, 1); u32x4 v[3]; qkv_issue(0, v); qkv_store(0, v); }
    const lptr qtw = lds + L_QT + w * 1024 + (n >> 3) * 256 + (((n & 7) * 32) ^ ((n >> 3) * 128)) + g * 8;
    const lptr tr0 = lds + L_QT + g * 256 + (n >> 2) * 32 + (lane & 3) * 8 + (g & 1) * 128, tr1 = tr0 + ((g & 1) ? -128 : 128);
    auto trf = [&](int off) { const u32x2 a = trread(tr0 + off), c = trread(tr1 + off); return (u32x4){a.x, a.y, c.x, c.y}; };
    const lptr mw = lds + L_MEMF + tid * 16 + w * 1024, tsw = lds + L_TS + tid * 4, vtw = lds + L_VT + tid * 4, vtr = lds + L_VT + lane * 4;
    const lptr rdw = lds + L_RED + (w * 64 + 32 * ((n >> 2) & 1) + 16 * ((n >> 1) & 1) + (n & 1) + 4 * (g & 1)) * 4, rdr = lds + L_RED + (lane & 31) * 8;
    const unsigned tso = w == 3 ? l16 : OOB, sco = w == 3 ? (lane & 1) * 4 : OOB, mso = w < 2 ? (2 * w + (lane >> 5)) * 16384 + (lane & 31) * 4 : b == 0 ? NC * 16384 + (w - 2) * 256 + lane * 4 : OOB,
         kso = (hf | hi) ? OOB : (rowi * 4 + g) * 16, pvo = (2 * (n >> 2) + (g >> 1)) * 16384 + ((n & 3) + 4 * (g & 1)) * 16;
    if (w == 1) ldsw128(lds + L_ONES + lane * 16, (u32x4){0x3f803f80u, 0x3f803f80u, 0x3f803f80u, 0x3f803f80u});
    if (w == 0) { const unsigned one = 0x3f80u << (16 * (n & 1)), k2 = (n >> 1) & 3; ldsw128(lds + L_SEL + lane * 16, (u32x4){k2 == 0 ? one : 0u, k2 == 1 ? one : 0u, k2 == 2 ? one : 0u, k2 == 3 ? one : 0u}); }
    if (tid == 0) *(LAS unsigned*)(lds + L_TS + 1024) = 0;
    const unsigned long long himask = 0xCCCCCCCCCCCCCCCCull;
    bar();
    auto look_at = [&](int sg) { const unsigned o = qkv_off(sg * 32 + h, w == 0 ? 1 : 2, cfg) + (w == 0 ? 0 : hf * 4096); return w >= 2 ? OOB : (w == 1 && lane >= 32) ? qflag : o + l16 * 8; };
    unsigned tvo = look_at(2);
    unsigned pold = pblk(2, b) + w * 2048 + 1024;
    unsigned quant;
    { const unsigned a = rt(); unsigned c, e; do c = rt(); while (c == a); do e = rt(); while (e == c); quant = min(e - c, 8u); }
    unsigned T2 = rt(), T1 = 0, ring = 0;
    unsigned pc = (unsigned)__builtin_amdgcn_s_memtime(), pt = T2, X = TX + CX / 24, Y = TY + CY / 24, mg = 0;
    auto step = [&](const int s, auto last_) {
        constexpr bool LAST = decltype(last_)::value;
        f32x4 aq0, aq1, ak0, ak1, av;
        const unsigned rnext = ring == NRING - 1 ? 0 : ring + 1;
        unsigned mo = ho0 + (HO_MEM + ring * MRS + xc) * 16384 + w * 2048, mo1 = mo + 1024, to = ho0 + (HO_MEM + ring * MRS + NC) * 16384 + xc * 128;
        asm volatile("" : "+s"(mo), "+s"(mo1), "+s"(to));
        u32x4 A[8], kx[2][4], qf[4], kf[4], vx[4], ones; unsigned tsv = 0;
        wait_rt(T2, quant);
        ST(0);
        A[0] = ld16(rw, l16, mo); A[1] = ld16(rw, l16, mo1); tsv = ld4(rw, sco, to);
        SB();
        SB();
        lptr qb = lds + L_QKV + (s & 1) * 12288 + lane * 16;
        lptr ar0 = lds + L_MEMF + lane * 16 + ((2 * w + 2) & 7) * 1024, ar1 = lds + L_MEMF + lane * 16 + ((2 * w + 4) & 7) * 1024, ar2 = lds + L_MEMF + lane * 16 + ((2 * w + 6) & 7) * 1024;
        asm volatile("" : "+v"(qb), "+v"(ar0), "+v"(ar1), "+v"(ar2));
        if constexpr (!LAST) {
#pragma unroll
            for (int ks = 0; ks < NPRE; ++ks) { kx[0][ks] = ldsr128(qb + ks * 1024); kx[1][ks] = ldsr128(qb + 4096 + ks * 1024); }
#pragma unroll
            for (int T = 0; T < NVX; ++T) vx[T] = ldsr128(qb + 8192 + T * 1024);
        }
        aq0 = zero4(); aq1 = zero4(); ak0 = zero4(); ak1 = zero4(); av = zero4();
        if constexpr (!LAST) {
#pragma unroll
            for (int ks = 0; ks < NPRE; ++ks) asm volatile("" : "+v"(kx[0][ks]), "+v"(kx[1][ks]));
#pragma unroll
            for (int T = 0; T < NVX; ++T) asm volatile("" : "+v"(vx[T]));
        }
        SB();
        ldsw128(mw, A[0]); ldsw128(mw + 1024, A[1]); ldsw32(tsw, tsv);
        SB();
        if (__builtin_expect(!__all(mx4(A[0], mx4(A[1], tsv)) != SENT), 0)) {
            for (unsigned spins = 0;; ++spins) {
                asm volatile("" ::: "memory");
                A[0] = ld16(rw, l16, mo); A[1] = ld16(rw, l16, mo1); tsv = ld4(rw, sco, to);
                if (__all(mx4(A[0], mx4(A[1], tsv)) != SENT)) break;
                if (spins > cap) { cap = 0; break; }
            }
            ldsw128(mw, A[0]); ldsw128(mw + 1024, A[1]); ldsw32(tsw, tsv);
        }
        ST(1);
        TS(0);
        NOP2();
        if constexpr (!LAST) { SMFA(aq0, A[0], wq[0][0]); SMFA(aq1, A[0], wq[1][0]); }
        SMFA(ak0, A[0], wk[0][0]);
        bar();
        ST2(9);
        TS(1);
        A[2] = ldsr128(ar0); A[3] = ldsr128(ar0 + 1024); SB();
        SMFA(ak1, A[0], wk[1][0]);
        A[4] = ldsr128(ar1); A[5] = ldsr128(ar1 + 1024); SB();
        SMFV(av, A[0], wv[0]);
        A[6] = ldsr128(ar2); A[7] = ldsr128(ar2 + 1024); SB();
        u32x2 tm = {0, 0}; unsigned look = 0;
#define SM_(a, w) "v_smfmac_f32_16x16x64_bf16 %0, %" #a ", %" #w ", %15\n\t"
#define POST6(C, acc, W, src, dst, MID, WR) { float t0_, t1_; asm volatile( \
                SM_(3, 9) SM_(4, 10) SM_(5, 11) "v_add_f32 %1, %16, %18\n\t" SM_(6, 12) "v_add_f32 %2, %17, %19\n\t" SM_(7, 13) \
                "v_cvt_pk_bf16_f32 %1, %1, %2\n\t" MID SM_(8, 14) "ds_write2_b32 %20, %1, %1 " WR \
                : "+v"(acc), "=&v"(t0_), "=&v"(t1_) : "v"(A[2]), "v"(A[3]), "v"(A[4]), "v"(A[5]), "v"(A[6]), "v"(A[7]), C(W[2]), C(W[3]), C(W[4]), C(W[5]), C(W[6]), C(W[7]), "v"(sidx), \
                  "v"(src[0]), "v"(src[1]), "v"(src[2]), "v"(src[3]), "v"(dst) : "memory"); }
#define POST3(C, acc, K, W, src, dst, WR) { float t0_, t1_; asm volatile( \
                "v_add_f32 %1, %10, %12\n\tv_smfmac_f32_16x16x64_bf16 %0, %3, %6, %9\n\tv_add_f32 %2, %11, %13\n\tv_smfmac_f32_16x16x64_bf16 %0, %4, %7, %9\n\t" \
                "v_cvt_pk_bf16_f32 %1, %1, %2\n\tv_smfmac_f32_16x16x64_bf16 %0, %5, %8, %9\n\tds_write2_b32 %14, %1, %1 " WR \
                : "+v"(acc), "=&v"(t0_), "=&v"(t1_) : "v"(A[K]), "v"(A[K + 1]), "v"(A[K + 2]), C(W[K]), C(W[K + 1]), C(W[K + 2]), "v"(sidx), \
                  "v"(src[0]), "v"(src[1]), "v"(src[2]), "v"(src[3]), "v"(dst) : "memory"); }
        if constexpr (!LAST) {
            SMFA(aq0, A[1], wq[0][1]);
            SMFA(aq1, A[1], wq[1][1]);
            tm = *(LAS u32x2*)(lds + L_TS + 768); SB();
            SMFA(ak0, A[1], wk[0][1]);
            SMFA(ak1, A[1], wk[1][1]); SMFV(av, A[1], wv[1]);
            TS(2);
            SMFA(aq0, A[2], wq[0][2]); SMFA(aq0, A[3], wq[0][3]); SMFA(aq0, A[4], wq[0][4]); SMFA(aq0, A[5], wq[0][5]);
            look = ld4(rw, tvo); SB();
            SMFA(aq0, A[6], wq[0][6]);
            if (__builtin_expect(b == 0, 0)) {
                unsigned sv = s;
                asm volatile("buffer_store_dword %0, %1, %2, %3 offen sc1" :: "v"(sv), "v"(t0o), "s"(rw), "s"(gflag) : "memory");
            }
            SMFA(aq0, A[7], wq[0][7]);
            POST6("a", aq1, wq[1], aq0, qtw, "", "offset0:0 offset1:1");
            TS(3);
            POST6("a", ak0, wk[0], aq1, qtw, "", "offset0:128 offset1:129");
            POST6("a", ak1, wk[1], ak0, qtw + 4096, "s_waitcnt lgkmcnt(0)\n\ts_barrier\n\t", "offset0:0 offset1:1");
                TS(4);
            qf[0] = trf(0); SB();
            SMFV(av, A[2], wv[2]);
            qf[1] = trf(1024); SB();
            SMFV(av, A[3], wv[3]);
            qf[2] = trf(2048); SB();
            SMFV(av, A[4], wv[4]);
            qf[3] = trf(3072); SB();
            POST3("v", av, 5, wv, ak1, qtw + 4096, "offset0:128 offset1:129");
            TS(5);
            f32x4 s0, s1, s2, c[8];
            MF0(s1, kx[0][0], qf[0]);
            ones = ldsr128(lds + L_ONES + lane * 16);
            if (NVX < 3) vx[2] = ldsr128(qb + 8192 + 2048);
            SB();
            MF0(s2, kx[1][0], qf[0]);
            if (NVX < 4) vx[3] = ldsr128(qb + 8192 + 3072);
            SB();
            MF(s1, kx[0][1], qf[1]);
            {
                float t0_, t1_;
                asm volatile("v_add_f32 %2, %8, %10\n\tv_add_f32 %3, %9, %11\n\tv_mfma_f32_16x16x32_bf16 %1, %4, %6, %1\n\tv_cvt_pk_bf16_f32 %2, %2, %3\n\t"
                             "v_mfma_f32_16x16x32_bf16 %0, %5, %7, %0\n\ts_waitcnt lgkmcnt(0)\n\ts_barrier\n\tds_write_b32 %12, %2"
                             : "+v"(s1), "+v"(s2), "=&v"(t0_), "=&v"(t1_) : "v"(kx[1][1]), "v"(kx[0][2]), "v"(qf[1]), "v"(qf[2]), "v"(av[0]), "v"(av[1]), "v"(av[2]), "v"(av[3]), "v"(vtw) : "memory");
            }
                TS(6);
            kf[0] = trf(4096); SB();
            MF(s2, kx[1][2], qf[2]);
            kf[1] = trf(4096 + 1024); SB();
            MF(s1, kx[0][3], qf[3]);
            kf[2] = trf(4096 + 2048); SB();
            MF(s2, kx[1][3], qf[3]);
            kf[3] = trf(4096 + 3072);
            SB();
            ST(2);
            TS(7);
            u32x4 pm;
            float mx;
            {
                float t_;
                MF0(s0, kf[0], qf[0]);
                pm = __builtin_bit_cast(u32x4, zero4());
                asm volatile("v_mfma_f32_16x16x32_bf16 %1, %2, %3, %1\n\tv_max3_f32 %0, %4, %5, %6" : "=&v"(mx), "+v"(s0) : "v"(kf[1]), "v"(qf[1]), "v"(s1[0]), "v"(s1[1]), "v"(s1[2]));
                asm volatile("v_mfma_f32_16x16x32_bf16 %2, %3, %4, %2\n\tv_max3_f32 %1, %5, %6, %7\n\tv_max3_f32 %0, %0, %8, %9"
                             : "+v"(mx), "=&v"(t_), "+v"(s0) : "v"(kf[2]), "v"(qf[2]), "v"(s1[3]), "v"(s2[0]), "v"(s2[1]), "v"(s2[2]), "v"(s2[3]));
                asm volatile("v_mfma_f32_16x16x32_bf16 %2, %3, %4, %2\n\tv_max_f32 %0, %0, %1\n\tv_mov_b32 %1, %0" : "+v"(mx), "+v"(t_), "+v"(s0) : "v"(kf[3]), "v"(qf[3]));
                asm volatile("s_barrier" ::: "memory");
                asm volatile("s_nop 0\n\tv_permlane16_swap_b32 %0, %1\n\tv_max_f32 %0, %0, %1\n\t"
                             "v_mov_b32 %1, %0\n\ts_nop 1\n\tv_permlane32_swap_b32 %0, %1\n\tv_max_f32 %0, %0, %1\n\tv_mul_f32 %0, 0xbe0293ee, %0" FW
                             : "+v"(mx), "+v"(t_) :: "memory");
            }
                ST2(12);
            TS(8);
            u32x4 px;
            float e0, e1, em;
            {
                float a_, b_, c_;
#define FE(d, src) "v_fmamk_f32 " d ", " src ", 0x3e0293ee, %20\n\t"
                asm volatile(FE("%7", "%10") FE("%8", "%11") "v_exp_f32 %7, %7\n\t" FE("%9", "%12") "v_exp_f32 %8, %8\n\t" FE("%1", "%13") "v_exp_f32 %9, %9\n\tv_cvt_pk_bf16_f32 %0, %7, %8\n\t"
                             "v_exp_f32 %1, %1\n\t" FE("%7", "%14") FE("%8", "%15") "v_exp_f32 %7, %7\n\tv_cvt_pk_bf16_f32 %1, %9, %1\n\tv_exp_f32 %8, %8\n\t" FE("%9", "%16") FE("%3", "%17")
                             "v_exp_f32 %9, %9\n\tv_cvt_pk_bf16_f32 %2, %7, %8\n\tv_exp_f32 %3, %3\n\t" FE("%4", "%18") FE("%5", "%19") "v_cvt_pk_bf16_f32 %3, %9, %3\n\tv_max_f32 %6, %4, %5"
                             : "=&v"(px.x), "=&v"(px.y), "=&v"(px.z), "=&v"(px.w), "=&v"(e0), "=&v"(e1), "=&v"(em), "=&v"(a_), "=&v"(b_), "=&v"(c_)
                             : "v"(s1[0]), "v"(s1[1]), "v"(s1[2]), "v"(s1[3]), "v"(s2[0]), "v"(s2[1]), "v"(s2[2]), "v"(s2[3]), "v"(s0[0]), "v"(s0[1]), "v"(mx), "v"(s0));
            }
            const bool slow = __any(em > 0.f);
            SB();
            TS(9);
            f32x4 o[4], ls;
            MF0(o[0], vx[0], px);
            kf[0].x = *(LAS unsigned*)vtr; SB(); kf[2].x = *(LAS unsigned*)(vtr + 512); SB();
            MF0(o[2], vx[2], px);
            kf[1].x = *(LAS unsigned*)(vtr + 256); SB(); kf[3].x = *(LAS unsigned*)(vtr + 768); SB();
            float up = 0.f;
            if (__builtin_expect(slow, 0)) {
                up = xmax(vmax(em, 0.f)); e0 -= up; e1 -= up;
            }
            SB();
            MF0(o[1], vx[1], px);
            asm volatile("v_exp_f32 %1, %1\n\tv_mfma_f32_16x16x32_bf16 %0, %2, %3, 0" : "=&v"(o[3]), "+v"(e0) : "v"(vx[3]), "v"(px));
            asm volatile("v_exp_f32 %2, %2\n\tv_mfma_f32_16x16x32_bf16 %0, %4, %5, 0\n\tv_cvt_pk_bf16_f32 %1, %3, %2" : "=&v"(ls), "=&v"(pm.x), "+v"(e1) : "v"(e0), "v"(ones), "v"(px));
            SB();
            if (__builtin_expect(slow, 0)) {
                asm volatile("s_nop 7" : "+v"(o[0]), "+v"(o[1]), "+v"(o[2]), "+v"(o[3]), "+v"(ls));
                const float al = __builtin_amdgcn_exp2f(-up);
                o[0] *= al; o[1] *= al; o[2] *= al; o[3] *= al; ls *= al;
            }
            c[0] = zero4();
            asm volatile("" : "+v"(pm), "+v"(o[0]), "+v"(o[1]), "+v"(o[2]), "+v"(o[3]), "+v"(ls));
            TS(10);
            MF(ls, ones, pm);
            c[1] = zero4();
            MF(o[0], kf[0], pm);
            c[2] = zero4();
            MF(o[2], kf[2], pm);
            c[3] = zero4();
            MF(o[1], kf[1], pm);
            c[4] = zero4();
            u32x4 o8;
            {
                float inv; f32x2 il, ih, t0_, t1_;
                asm volatile("v_mfma_f32_16x16x32_bf16 %0, %3, %4, %0\n\tv_rcp_f32 %1, %5\n\ts_nop 0\n\tv_cndmask_b32_e64 %2, 0, %1, %6\n\tv_cndmask_b32_e64 %1, %1, 0, %6"
                             : "+v"(o[3]), "=&v"(il.x), "=&v"(ih.x) : "v"(kf[3]), "v"(pm), "v"(ls[0]), "s"(himask), "v"(ls));
                (void)inv; (void)t0_; (void)t1_;
                TS(11);
                c[5] = zero4();
                f32x4 oa = o[0] * il.x + o[2] * ih.x;
                o8.x = pk(oa[0], oa[1]); o8.y = pk(oa[2], oa[3]);
                asm volatile("" : "+v"(o8.x), "+v"(o8.y), "+v"(o[1]), "+v"(o[3]));
                const f32x4 ob = o[1] * il.x + o[3] * ih.x;
                o8.z = pk(ob[0], ob[1]); o8.w = pk(ob[2], ob[3]);
            }
            ST2(13);
            asm volatile("s_nop 1" : "+v"(o8));
            TS(12);
            SB();
            SMFV(c[0], o8, wo[0]); c[6] = zero4(); SMFV(c[1], o8, wo[1]); c[7] = zero4(); SMFV(c[2], o8, wo[2]); SMFV(c[3], o8, wo[3]);
            const unsigned slot = pblk(ring, 16 * w) + b * 128;
            u32x4 pa;
            {
                float t_;
                asm volatile("v_add_f32 %4, %15, %17\n\tv_smfmac_f32_16x16x64_bf16 %0, %9, %10, %14\n\tv_add_f32 %8, %16, %18\n\tv_smfmac_f32_16x16x64_bf16 %1, %9, %11, %14\n\t"
                             "v_cvt_pk_bf16_f32 %4, %4, %8\n\tv_smfmac_f32_16x16x64_bf16 %2, %9, %12, %14\n\tv_add_f32 %5, %19, %21\n\tv_smfmac_f32_16x16x64_bf16 %3, %9, %13, %14\n\t"
                             "v_add_f32 %8, %20, %22\n\tv_cvt_pk_bf16_f32 %5, %5, %8\n\tv_add_f32 %6, %23, %25\n\tv_add_f32 %8, %24, %26\n\tv_cvt_pk_bf16_f32 %6, %6, %8"
                             : "+v"(c[4]), "+v"(c[5]), "+v"(c[6]), "+v"(c[7]), "=&v"(pa.x), "=&v"(pa.y), "=&v"(pa.z), "=&v"(pa.w), "=&v"(t_)
                             : "v"(o8), "v"(wo[4]), "v"(wo[5]), "v"(wo[6]), "v"(wo[7]), "v"(sidx),
                               "v"(c[0][0]), "v"(c[0][1]), "v"(c[0][2]), "v"(c[0][3]), "v"(c[1][0]), "v"(c[1][1]), "v"(c[1][2]), "v"(c[1][3]), "v"(c[2][0]), "v"(c[2][1]), "v"(c[2][2]), "v"(c[2][3]));
            }
            auto fold = [](f32x4 v) { const f32x2 t = (f32x2){v[0], v[1]} + (f32x2){v[2], v[3]}; return pk(t.x, t.y); };
            pa.w = fold(c[3]);
            st16(rw, pvo, pa, slot);
            TS(13);
            SB();
            st16(rw, pvo, (u32x4){fold(c[4]), fold(c[5]), fold(c[6]), fold(c[7])}, slot + 131072);
            TS(14);
            SB();
            const unsigned now = rt();
            ST(3);
            T1 = __builtin_amdgcn_readfirstlane(tm.x); T2 = __builtin_amdgcn_readfirstlane(tm.y);
            { const int dl = s == 0 ? 99 : (int)(now + TV1 - T1); if (dl > 0) T1 = now + TV1 + (dl > TS_ ? TM : 0);
            }
        } else {
            st4(rw, b == 0 ? t0o : OOB, s, gflag);
#pragma unroll
            for (int ks = 1; ks < 8; ++ks) { SMFA(ak0, A[ks], wk[0][ks]); SMFA(ak1, A[ks], wk[1][ks]); SMFV(av, A[ks], wv[ks]); }
            asm volatile("s_nop 7" : "+v"(av), "+v"(ak0), "+v"(ak1));
            unsigned d0 = pk(ak0[0] + ak0[2], ak0[1] + ak0[3]); dsw2<0, 1>(qtw + 4096, d0, d0);
            d0 = pk(ak1[0] + ak1[2], ak1[1] + ak1[3]); dsw2<128, 129>(qtw + 4096, d0, d0);
            ldsw32(vtw, pk(av[0] + av[2], av[1] + av[3]));
            bar();
        }
        {
            const u32x4 kv = trf(4096 + w * 1024);
            const u32x4 vv = {*(LAS unsigned*)vtr, *(LAS unsigned*)(vtr + 256), *(LAS unsigned*)(vtr + 512), *(LAS unsigned*)(vtr + 768)};
            const unsigned kb = kvm_off(ho, cfg, s * 32 + h);
            st16(rw, kso, kv, kb + w * 512); st16(rw, tso, vv, kb + 2048 + hf * 1024);
        }
        if constexpr (LAST) return; else {
        ST(4);
        u32x4 p0, p1, nv[3];
        unsigned pb = pblk(ring, b) + w * 2048, pb1 = pb + 1024, mnx = ho0 + (HO_MEM + rnext * MRS) * 16384 + b * 128;
        asm volatile("" : "+s"(pb), "+s"(pb1), "+s"(mnx));
        if (__builtin_expect((unsigned)s + 2 > (unsigned)__builtin_amdgcn_readfirstlane(*(LAS unsigned*)(lds + L_TS + 1024)), 0)) qkv_wait(s + 2);
        qkv_issue(s + 1, nv);
        tvo = look_at(min(s + 3, NSEG - 1));
        const unsigned qmin = min(min(__builtin_amdgcn_readlane(look, 32), __builtin_amdgcn_readlane(look, 33)), min(__builtin_amdgcn_readlane(look, 34), __builtin_amdgcn_readlane(look, 35)));
        if (b == 0 && (s & 3) == 3) {
            const unsigned c = (unsigned)__builtin_amdgcn_s_memtime(), t = rt();
            const float tpc = (float)(t - pt) * __builtin_amdgcn_rcpf((float)(c - pc));
            { int xi = (int)(CX * tpc + 0.5f), yi = (int)(CY * tpc + 0.5f); asm volatile("" : "+v"(xi), "+v"(yi)); X = TX + __builtin_amdgcn_readfirstlane(xi); Y = TY + __builtin_amdgcn_readfirstlane(yi); }
            pc = c; pt = t; mg -= mg > 0;
        }
        wait_rt(T1, quant);
        ST(5);
        p0 = ld16(rw, l16, pb); p1 = ld16(rw, l16, pb1);
        SB();
        qkv_store(s + 1, nv);
        SB();
        u32x4 sel = ldsr128(lds + L_SEL + lane * 16);
        asm volatile("" : "+v"(sel));
        SB();
        f32x4 rr;
        MF0(rr, sel, p0); MF(rr, sel, p1);
        SB();
        if (__builtin_expect(!__all(mx4(p0, mx4(p1, 0u)) != SENT), 0)) {
            for (unsigned spins = 0;; ++spins) {
                asm volatile("" ::: "memory");
                p0 = ld16(rw, l16, pb); p1 = ld16(rw, l16, pb1);
                if (__all(mx4(p0, mx4(p1, 0u)) != SENT)) break;
                if (spins > cap) { cap = 0; break; }
            }
            MF0(rr, sel, p0); MF(rr, sel, p1);
            asm volatile("s_nop 7" : "+v"(rr));
        }
        ST(6);
        {
            asm volatile("s_nop 0" : "+v"(rr));
            unsigned q0, q1, q2, q3;
            asm volatile("v_add_f32_dpp %0, %4, %4 row_ror:8 row_mask:0xf bank_mask:0xf\n\tv_add_f32_dpp %1, %5, %5 row_ror:8 row_mask:0xf bank_mask:0xf\n\t"
                         "v_add_f32_dpp %2, %6, %6 row_ror:8 row_mask:0xf bank_mask:0xf\n\tv_add_f32_dpp %3, %7, %7 row_ror:8 row_mask:0xf bank_mask:0xf"
                         : "=&v"(q0), "=&v"(q1), "=&v"(q2), "=&v"(q3) : "v"(rr[0]), "v"(rr[1]), "v"(rr[2]), "v"(rr[3]));
            dsw2<0, 8>(rdw, q0, q1); dsw2<2, 10>(rdw, q2, q3);
        }
        bar();
        {
            unsigned mv;
            if (__builtin_expect(b == 0 && w >= 2, 0)) {
                const unsigned now = rt() + 2; const int dl = s == 0 ? 99 : (int)(now + TV2 - T2);
                if (dl > TS_ && s > 1) mg = min(mg + MUP, 24u);
                const unsigned n1 = min((dl > 0 ? now + TV2 + (dl > TS_ ? TPEN : 0) : T2) + X + mg, SENT - 1024);
                mv = (lane & 1) ? n1 + Y + (mg >> 1) : n1;
            } else {
                f32x2 a0, a1, a2, a3;
                asm volatile("ds_read_b64 %0, %4\n\tds_read_b64 %1, %4 offset:256\n\tds_read_b64 %2, %4 offset:512\n\tds_read_b64 %3, %4 offset:768\n\ts_waitcnt lgkmcnt(0)"
                             : "=&v"(a0), "=&v"(a1), "=&v"(a2), "=&v"(a3) : "v"(rdr) : "memory");
                const f32x2 t = (a0 + a1) + (a2 + a3);
                mv = pk(t.x, t.y);
            }
            st4(rw, mso, mv, mnx);
            SB();
            const unsigned now = rt(); const int dl = s == 0 ? 99 : (int)(now + TV2 - T2);
            SB();
            {
                const u32x4 z = sent4();
                st16(rw, l16, z, pb);
                st16(rw, l16, z, pb1);
                pold = pb1;
                st4(rw, mso, z.x, ho0 + (HO_MEM + ring * MRS) * 16384 + b * 128);
            }
            if (dl > 0) T2 = now + TV2 + (dl > TS_ ? TM : 0);
        }
        if (__builtin_expect(s == NSEG - 2, 0)) { unsigned sv = s + 1; asm volatile("buffer_store_dword %0, %1, %2, %3 offen sc1" :: "v"(sv), "v"(tso - 16 * 63), "s"(rw), "s"(kflag) : "memory"); }
        if (w == 1) *(LAS unsigned*)(lds + L_TS + 1024) = qmin;
        ST(7);
        ring = rnext;
        ST(8);
        }
    };
    for (int s = 0; s < NSEG - 1; ++s) step(s, bool_c<false>{});
    step(NSEG - 1, bool_c<true>{});
    drain(); bar();
    st4(rw, t0o, NSEG, kflag);
}

template <int ROLE>
DI void stream_role(const Params& p, lptr lds, const int j, const int tid) {
    const int w = __builtin_amdgcn_readfirstlane(tid >> 6), lane = tid & 63, n = lane & 15, g = lane >> 4, pp = lane & 3;
    const rsrc_t rw = mkrs(p.ws, WS_END);
    const float* mod = (const float*)(p.ws + WS_MOD);
    const unsigned cfg = get_cfg(p.ws);
    unsigned cap = 1u << 17;
    const unsigned l16 = lane * 16, t0o = tid == 64 ? 0u : OOB;
    const int cgp = (j & 7) + 8 * (j >> 4), qtt = (j >> 3) & 1, qm = cgp >> 5, qh = cgp & 31;
    u32x4 xv[4], pv[32]; f32x4 mv[4];
#pragma unroll
    for (int r = 0; r < 4; ++r) xv[r] = *(const u32x4*)(p.x + (size_t)((j & 127) * 16 + w * 4 + r) * D + lane * 8);
    mv[0] = *(const f32x4*)(mod + lane * 8); mv[1] = *(const f32x4*)(mod + lane * 8 + 4); mv[2] = *(const f32x4*)(mod + 512 + lane * 8); mv[3] = *(const f32x4*)(mod + 516 + lane * 8);
    const u16* Wsrc = (qm == 0 ? p.Wq : qm == 1 ? p.Wk : p.Wv) + qh * 128;
    if (j >= 128) panel_issue(Wsrc, Wsrc + 64, HD, tid, pv);
    if (j < 128) {
        float t[8], sh[8];
#pragma unroll
        for (int i = 0; i < 8; ++i) { t[i] = rb(1.f + mv[2 + (i >> 2)][i & 3]); sh[i] = mv[i >> 2][i & 3]; }
#pragma unroll
        for (int r = 0; r < 4; ++r) {
            float v[8] = {bflo(xv[r].x), bfhi(xv[r].x), bflo(xv[r].y), bfhi(xv[r].y), bflo(xv[r].z), bfhi(xv[r].z), bflo(xv[r].w), bfhi(xv[r].w)};
            float sum = ((v[0] + v[1]) + (v[2] + v[3])) + ((v[4] + v[5]) + (v[6] + v[7]));
            sum += dpp<0xB1>(sum); sum += dpp<0x4E>(sum); sum += dpp<0x124>(sum); sum += dpp<0x128>(sum); sum = xsum(sum);
            const float mean = sum * (1.f / D); float var = 0.f;
#pragma unroll
            for (int i = 0; i < 8; ++i) { const float d = v[i] - mean; var += d * d; }
            var += dpp<0xB1>(var); var += dpp<0x4E>(var); var += dpp<0x124>(var); var += dpp<0x128>(var); var = xsum(var);
            const float mb = rb(mean), sd = 1.f / rb(sqrtf(rb(rb(var * (1.f / D)) + 1e-5f)));
#pragma unroll
            for (int i = 0; i < 8; ++i) v[i] = lnmod(v[i], mb, sd, t[i], sh[i]);
            st16(rw, WS_H1 + (unsigned)(j * 16 + (lane >> 2)) * 1024u + ((w * 4 + r) * 4 + (lane & 3)) * 16, (u32x4){pk(v[0], v[1]), pk(v[2], v[3]), pk(v[4], v[5]), pk(v[6], v[7])});
        }
    }
    SU(64 + j, 0);
    u32x4 qa[16], qb[16];
    if (j < 128) {
        SB();
        panel_issue(Wsrc, Wsrc + 64, HD, tid, pv);
        asm volatile("s_waitcnt vmcnt(32)" ::: "memory"); bar();
        st4(rw, t0o, 1, flg(FL_LN + j, cfg));
    }
    panel_finish(lds, tid, 32 * w + 8 * pp, 32 * w + 8 * pp + 4, pv, qa, qb);
    SU(64 + j, 1);
    if (w == 0) wait_flags(rw, cfg, FL_LN + qtt, 64, 2, 1, lane, cap);
    bar();
    SU(64 + j, 2);
    u32x4 hv[4];
    const unsigned h1off = WS_H1 + (unsigned)(qtt * 16 + w * 4) * 1024u;
    auto h1_issue = [&](int s) {
#pragma unroll
        for (int i = 0; i < 4; ++i) hv[i] = ld16(rw, (n * 4 + g) * 16, h1off + (unsigned)s * 32768u + i * 1024); };
    auto qkv_in = [&](int s) {
#pragma unroll
        for (int i = 0; i < 4; ++i) ldsw128(lds + L_Q + (w * 4 + i) * 1024 + lane * 16, hv[i]);
    };
    auto qkv_mm = [&](int s) {
        f32x4 ca, cb;
        if (qm < 2) tile_mma<false>(lds + L_Q, lane, qa, qb, ca, cb); else tile_mma<true>(lds + L_Q, lane, qa, qb, ca, cb);
        const unsigned blk = qm < 2 ? qtt * 4 + w : w * 2 + qtt;
        st16(rw, l16, (u32x4){pk(ca[0], ca[1]), pk(ca[2], ca[3]), pk(cb[0], cb[1]), pk(cb[2], cb[3])}, qkv_off(s * 32 + qh, qm, cfg) + blk * 1024);
    };
    auto qkv_seg = [&](int s) { qkv_in(s); h1_issue(min(s + 1, NSEG - 1)); bar(); qkv_mm(s); };
    auto publish = [&](unsigned line, unsigned v) { drain(); bar(); st4(rw, t0o, v, flg(line, cfg)); };
    h1_issue(0);
    const int r = ROLE == 0 ? j : ROLE == 1 ? j - 64 : ROLE == 2 ? j - 120 : ROLE == 3 ? j - 152 : j - 168;
    const int tt = ROLE == 3 ? r >> 3 : ROLE == 1 ? r & 1 : (r >> 3) & 1;
    const int cgx = ROLE == 1 ? r >> 1 : (r & 7) + 8 * (r >> 4);
    qkv_seg(0); publish(FL_QKV + j, 1);
    SU(64 + j, 3);
    for (int s = 1; s < 7; s += 2) { qkv_seg(s); bar(); qkv_seg(s + 1); publish(FL_QKV + j, s + 2); }
    SU(64 + j, 4);
    if (ROLE == 0) { const u16* W = p.Wo + (size_t)(r & 7) * 512 * D + (r >> 4) * 128; panel_issue(W, W + 64, D, tid, pv); }
    if (ROLE == 1) panel_issue(p.wg + cgx * 64, p.wu + cgx * 64, F, tid, pv);
    SB();
    qkv_seg(7); publish(FL_QKV + j, 8);
    u32x4 sa[16], sb[16];
    if (ROLE == 0) panel_finish(lds, tid, 32 * w + 4 * pp, 32 * w + 16 + 4 * pp, pv, sa, sb);
    if (ROLE == 1) panel_finish(lds, tid, 16 * w + 4 * pp, 64 + 16 * w + 4 * pp, pv, sa, sb);
    if (ROLE == 2) {
        const rsrc_t rd = mkrs(p.wd + cgx * 32, (1791 * D + 32) * 2);
#pragma unroll
        for (int half = 0; half < 2; ++half) {
            u32x4 v[14];
#pragma unroll
            for (int i = 0; i < 14; ++i) v[i] = ldw(rd, ((tid >> 2) * D + (tid & 3) * 8) * 2, (half * 896 + i * 64) * D * 2);
            bar();
#pragma unroll
            for (int i = 0; i < 14; ++i) ldsw128(lds + ((tid >> 2) + 64 * i) * 80 + (tid & 3) * 16, v[i]);
            bar();
            if ((w >> 1) == half) {
                lptr pa = lds + ((w & 1) * 448 + 8 * g + (n >> 2)) * 80 + pp * 8;
#pragma unroll
                for (int ks = 0; ks < 14; ++ks) { sa[ks] = tr2(pa + ks * 32 * 80, 4 * 80); sb[ks] = tr2(pa + ks * 32 * 80 + 32, 4 * 80); }
            }
        }
        bar();
    }
    for (int s = 8; s < LEAD; s += 2) { qkv_seg(s); bar(); qkv_seg(s + 1); publish(FL_QKV + j, s + 2); }
    SU(64 + j, 5);
    unsigned poff; unsigned ahead = 0;
    if (ROLE == 0) poff = FL_A + (r & 7) * 2 + (lane & 1);
    if (ROLE == 1) poff = FL_L + tt * 8 + (lane & 7);
    if (ROLE == 2) poff = FL_C + tt + 2 * (lane < 28 ? lane : 0);
    if (ROLE == 3) poff = FL_B + (lane & 7) + tt * 8 + ((lane >> 3) & 3) * 16;
    if (ROLE == 4) poff = FL_G;
    if (ROLE == 5) {
        const int hh = 2 * r + ((lane >> 1) & 1), cg3 = ((lane >> 2) & 3) * 32 - 32 + hh;
        poff = (lane & 12) == 0 ? FL_KM + (hh & 3) + 8 * (hh >> 2) + 4 * (lane & 1) : FL_QKV + (cg3 & 7) + 16 * (cg3 >> 3) + 8 * (lane & 1);
        ahead = (lane & 12) != 0;
        if (lane == 63) { poff = FL_G; ahead = 0; }
    }
    poff = flg(poff, cfg);
    const unsigned oflag = flg((ROLE == 0 ? FL_B : ROLE == 1 ? FL_C : ROLE == 3 ? FL_L : FL_A) + r, cfg), qflag = flg(FL_QKV + j, cfg);
    unsigned seen = 0;
    auto ready = [&](unsigned nd) {
        if (ROLE != 5) return __all(seen >= nd);
        const unsigned gl = __builtin_amdgcn_readlane(seen, 63);
        return __all(((lane & 12) == 0 ? max(seen, gl) : lane == 63 ? nd : seen) >= nd);
    };
    auto wait_in = [&](unsigned need) {
        if (w == 0) {
            const unsigned nd = min(need + ahead, (unsigned)NSEG);
            for (unsigned spins = 0; !ready(nd); ++spins) {
                if (spins > cap) { cap = 0; break; }
                if (spins) __builtin_amdgcn_s_sleep(PSL);
                asm volatile("" ::: "memory");
                seen = ld4(rw, poff);
            }
        }
        bar();
    };
    const unsigned pof0 = w == 0 ? poff : OOB;
    const int hh = 2 * r + (w >> 1), att = w & 1;
    u32x4 xq[4], ka[4], kb[4], vb[8];
    const unsigned l16b = att ? l16 : OOB;
    auto a_issue_qk = [&](int s) {
        const unsigned q_ = qkv_off(s * 32 + hh, 0, cfg) + att * 4096, k_ = qkv_off(s * 32 + hh, 1, cfg);
#pragma unroll
        for (int ks = 0; ks < 4; ++ks) { xq[ks] = ld16(rw, l16, q_ + ks * 1024); ka[ks] = ld16(rw, l16, k_ + ks * 1024); kb[ks] = ld16(rw, l16b, k_ + 4096 + ks * 1024); }
    };
    auto a_issue_v = [&](int s) {
        const unsigned v_ = qkv_off(s * 32 + hh, 2, cfg);
#pragma unroll
        for (int i = 0; i < 8; ++i) vb[i] = ld16(rw, (i & 1) ? l16b : l16, v_ + i * 1024);
    };
    if (ROLE == 5) { wait_in(0); a_issue_qk(0); a_issue_v(0); }
    const int d0 = 128 * w + 16 * (lane >> 3) + 4 * (lane & 3);
    f32x4 cga, cgb, cgc;
    if (ROLE == 3) { cga = *(const f32x4*)(mod + 1024 + d0); cgb = *(const f32x4*)(mod + 1536 + d0); cgc = *(const f32x4*)(mod + 2048 + d0);
#pragma unroll
        for (int i = 0; i < 4; ++i) cgc[i] = rb(1.f + cgc[i]); }
    if (ROLE == 2) cga = *(const f32x4*)(mod + 2560 + cgx * 32 + (w & 1) * 16 + 4 * g);
    u32x2 xnext = {0, 0};
    if (ROLE == 3) xnext = *(const u32x2*)(p.x + (size_t)(tt * 16 + 2 * (r & 7) + ((lane >> 2) & 1)) * D + d0);
    qkv_in(LEAD);
    auto iter = [&](const int s, auto doq_) {
        constexpr bool doq = decltype(doq_)::value;
        u32x2 xv = {0, 0};
        wait_in(s + 1);
        u32x4 a[14];
        if (ROLE == 0) {
#pragma unroll
            for (int i = 0; i < 4; ++i) a[i] = ld16(rw, l16, loc(WS_O, (unsigned)((s * 2 + tt) * 128 + (r & 7) * 16 + w * 4) * 1024u, cfg, 1) + i * 1024);
        }
        if (ROLE == 1) {
#pragma unroll
            for (int i = 0; i < 4; ++i) a[i] = ld16(rw, (n * 4 + g) * 16, loc(WS_H2, (unsigned)((s * 2 + tt) * 16 + w * 4) * 1024u, cfg, 0) + i * 1024);
        }
        if (ROLE == 2) {
#pragma unroll
            for (int ks = 0; ks < 14; ++ks) a[ks] = ld16(rw, (g >> 1) * 512 + n * 32 + (g & 1) * 16, loc(WS_G, (unsigned)((s * 2 + tt) * 56 + w * 14 + ks) * 1024u, cfg, 1));
            xv = ld8(rw, (n >> 3) * 16384 + (n & 7) * 1024 + ((w & 1) * 16 + 4 * g) * 2, loc(WS_X1, (s * 2 + tt) * 16384, cfg, 1) + cgx * 64);
        }
        if (ROLE == 3) {
#pragma unroll
            for (int kg = 0; kg < 8; ++kg) a[kg] = ld16(rw, (lane >> 3) * 1024 + (lane & 7) * 16, loc(WS_PB, (unsigned)(((s * 8 + kg) * 2 + tt) * 32 + 8 * w) * 1024u, cfg, 1) + (r & 7) * 128);
            xv = xnext;
            xnext = *(const u32x2*)(p.x + (size_t)(min(s + 1, NSEG - 1) * 32 + tt * 16 + 2 * (r & 7) + ((lane >> 2) & 1)) * D + d0);
        }
        if (ROLE == 5) {
            const unsigned kb_ = kvm_off(WS_HO, cfg, s * 32 + hh);
#pragma unroll
            for (int ks = 0; ks < 4; ++ks) a[ks] = ld16(rw, ((2 * (n >> 2) + (n & 1)) * 4 + g) * 16, kb_ + ks * 512);
            a[4] = ld16(rw, l16, kb_ + 2048); a[5] = ld16(rw, l16, kb_ + 3072);
        }
        seen = ld4(rw, pof0);
        if (doq) h1_issue(min(s + LEAD + 1, NSEG - 1));
        SB();
        if (doq) qkv_mm(s + LEAD);
        SB();
        if (ROLE == 0 || ROLE == 1) {
#pragma unroll
            for (int i = 0; i < 4; ++i) ldsw128(lds + L_A + (w * 4 + i) * 1024 + lane * 16, a[i]);
            bar();
            f32x4 ca, cb; tile_mma<false>(lds + L_A, lane, sa, sb, ca, cb);
            if (ROLE == 0) {
                const unsigned o = loc(WS_PB, (unsigned)(((s * 8 + (r & 7)) * 2 + tt) * 32 + (r >> 4) * 8 + w * 2) * 1024u, cfg, 1);
                st16(rw, (n * 4 + g) * 16, __builtin_bit_cast(u32x4, ca), o); st16(rw, (n * 4 + g) * 16, __builtin_bit_cast(u32x4, cb), o + 1024);
            } else {
                float q[4];
#pragma unroll
                for (int i = 0; i < 4; ++i) { const float gv = rb(ca[i]); q[i] = rb(gv * __builtin_amdgcn_rcpf(1.f + __expf(-gv))) * rb(cb[i]); }
                st8(rw, n * 32 + g * 8, (u32x2){pk(q[0], q[1]), pk(q[2], q[3])}, loc(WS_G, (unsigned)((s * 2 + tt) * 56 + cgx * 2 + (w >> 1)) * 1024u, cfg, 1) + (w & 1) * 512);
            }
        }
        if (ROLE == 2) {
            f32x4 c0 = {0, 0, 0, 0}, c1 = c0;
            asm volatile("s_nop 1" : "+v"(c0), "+v"(c1));
#pragma unroll
            for (int ks = 0; ks < 14; ++ks) { MFWA(c0, sa[ks], a[ks]); MFWA(c1, sb[ks], a[ks]); }
            asm volatile("s_nop 7" : "+v"(c0), "+v"(c1));
            *(LAS f32x4*)(lds + L_A + (w * 2) * 1024 + lane * 16) = c0; *(LAS f32x4*)(lds + L_A + (w * 2 + 1) * 1024 + lane * 16) = c1;
            bar();
            if (w < 2) {
                lptr q = lds + L_A + w * 1024 + lane * 16;
                const f32x4 y = (*(LAS f32x4*)q + *(LAS f32x4*)(q + 2048)) + (*(LAS f32x4*)(q + 4096) + *(LAS f32x4*)(q + 6144));
                const float o0 = bflo(xv.x) + rb(cga[0] * rb(y[0])), o1 = bfhi(xv.x) + rb(cga[1] * rb(y[1])), o2 = bflo(xv.y) + rb(cga[2] * rb(y[2])), o3 = bfhi(xv.y) + rb(cga[3] * rb(y[3]));
                *(u32x2*)(p.out + (size_t)(s * 32 + tt * 16 + n) * D + cgx * 32 + (w & 1) * 16 + 4 * g) = (u32x2){pk(o0, o1), pk(o2, o3)};
            }
        }
        if (ROLE == 5) {
            f32x4 x0, x1, x2;
            MF0(x1, ka[0], xq[0]); MF0(x2, kb[0], xq[0]);
#pragma unroll
            for (int ks = 1; ks < 4; ++ks) { MF(x1, ka[ks], xq[ks]); MF(x2, kb[ks], xq[ks]); }
            MF0(x0, a[0], xq[0]); MF(x0, a[1], xq[1]); MF(x0, a[2], xq[2]); MF(x0, a[3], xq[3]);
            a_issue_qk(min(s + 1, NSEG - 1));
            asm volatile("s_nop 7" : "+v"(x0), "+v"(x1), "+v"(x2));
            SB();
            const float raw[10] = {x1[0], x1[1], x1[2], x1[3], x2[0], x2[1], x2[2], x2[3], x0[0], x0[1]};
            float sc[10];
            const int qi = 16 * att + n - 4 * g;
#pragma unroll
            for (int i = 0; i < 5; ++i) {
                const unsigned u = vcvt(raw[2 * i], raw[2 * i + 1]);
                const unsigned v = pk(bflo(u) * 0.08838834764831845f, bfhi(u) * 0.08838834764831845f);
                sc[2 * i] = bflo(v); sc[2 * i + 1] = bfhi(v);
            }
#pragma unroll
            for (int i = 0; i < 8; ++i) sc[i] = ((i & 3) + 16 * (i >> 2) <= qi) ? sc[i] : -1e9f;
            float mx = vmax3(vmax3(vmax3(sc[0], sc[1], sc[2]), vmax3(sc[3], sc[4], sc[5]), vmax(sc[6], sc[7])), sc[8], sc[9]);
            mx = xmax(mx) * -1.4426950408889634f;
#pragma unroll
            for (int i = 0; i < 10; ++i) sc[i] = __builtin_amdgcn_exp2f(__builtin_fmaf(sc[i], 1.4426950408889634f, mx));
            const float inv = __builtin_amdgcn_rcpf(xsum((((sc[0] + sc[1]) + (sc[2] + sc[3])) + ((sc[4] + sc[5]) + (sc[6] + sc[7]))) + (sc[8] + sc[9])));
            u32x4 xpx = {pk(sc[0] * inv, sc[1] * inv), pk(sc[2] * inv, sc[3] * inv), pk(sc[4] * inv, sc[5] * inv), pk(sc[6] * inv, sc[7] * inv)};
            u32x4 xpm = {pk(sc[8] * inv, sc[9] * inv), 0u, 0u, 0u};
            f32x4 o[8];
#pragma unroll
            for (int e = 0; e < 4; ++e) {
                u32x4 fa = {vb[2 * e].x, vb[2 * e].y, vb[2 * e + 1].x, vb[2 * e + 1].y}, fb = {vb[2 * e].z, vb[2 * e].w, vb[2 * e + 1].z, vb[2 * e + 1].w};
                u32x4 ma = vb[2 * e], mb = vb[2 * e + 1];
                ma.x = a[4 + (e >> 1)][2 * (e & 1)]; mb.x = a[4 + (e >> 1)][2 * (e & 1) + 1];
                asm volatile("s_nop 1" : "+v"(fa), "+v"(fb), "+v"(ma), "+v"(mb), "+v"(xpx), "+v"(xpm));
                MF0(o[2 * e], fa, xpx); MF0(o[2 * e + 1], fb, xpx); MF(o[2 * e], ma, xpm); MF(o[2 * e + 1], mb, xpm);
            }
            asm volatile("s_nop 7" : "+v"(o[0]), "+v"(o[1]), "+v"(o[2]), "+v"(o[3]), "+v"(o[4]), "+v"(o[5]), "+v"(o[6]), "+v"(o[7]));
#pragma unroll
            for (int e = 0; e < 4; ++e)
                st16(rw, l16, (u32x4){pk(o[2 * e][0], o[2 * e][1]), pk(o[2 * e][2], o[2 * e][3]), pk(o[2 * e + 1][0], o[2 * e + 1][1]), pk(o[2 * e + 1][2], o[2 * e + 1][3])}, loc(WS_O, (unsigned)((s * 2 + att) * 128 + hh * 4) * 1024u, cfg, 1) + e * 1024);
            SB();
            a_issue_v(min(s + 1, NSEG - 1));
        }
        if (ROLE == 3) {
            LAS float* sm = (LAS float*)(lds + L_A);
            const int tk = (lane >> 2) & 1, tok = 2 * (r & 7) + tk;
            f32x4 acc = __builtin_bit_cast(f32x4, a[0]);
#pragma unroll
            for (int kg = 1; kg < 8; ++kg) acc += __builtin_bit_cast(f32x4, a[kg]);
            float x1[4] = {bflo(xv.x), bfhi(xv.x), bflo(xv.y), bfhi(xv.y)};
            float sum = 0.f;
#pragma unroll
            for (int i = 0; i < 4; ++i) { x1[i] = rb(x1[i] + rb(cga[i] * rb(acc[i]))); sum += x1[i]; }
            st8(rw, tk * 1024 + d0 * 2, (u32x2){pk(x1[0], x1[1]), pk(x1[2], x1[3])}, loc(WS_X1, (s * 32 + tt * 16 + 2 * (r & 7)) * 1024, cfg, 1));
            sum += dpp<0xB1>(sum); sum += dpp<0x4E>(sum); sum += dpp<0x128>(sum); sum = xsum(sum);
            if (lane == 0 || lane == 4) sm[w * 2 + tk] = sum;
            bar();
            const float mean = ((sm[tk] + sm[2 + tk]) + (sm[4 + tk] + sm[6 + tk])) * (1.f / D);
            float var = 0.f;
#pragma unroll
            for (int i = 0; i < 4; ++i) { const float d = x1[i] - mean; var += d * d; }
            var += dpp<0xB1>(var); var += dpp<0x4E>(var); var += dpp<0x128>(var); var = xsum(var);
            if (lane == 0 || lane == 4) sm[8 + w * 2 + tk] = var;
            bar();
            var = ((sm[8 + tk] + sm[10 + tk]) + (sm[12 + tk] + sm[14 + tk])) * (1.f / D);
            const float mb = rb(mean), sd = 1.f / rb(sqrtf(rb(rb(var) + 1e-5f)));
            float hh2[4];
#pragma unroll
            for (int i = 0; i < 4; ++i) hh2[i] = lnmod(x1[i], mb, sd, cgc[i], cgb[i]);
            st8(rw, ((d0 >> 5) & 3) * 1024 + tok * 64 + (d0 & 31) * 2, (u32x2){pk(hh2[0], hh2[1]), pk(hh2[2], hh2[3])}, loc(WS_H2, (unsigned)((s * 2 + tt) * 16 + 4 * w) * 1024u, cfg, 0));
        }
        if (ROLE == 5) asm volatile("s_waitcnt vmcnt(8)" ::: "memory"); else drain();
        bar();
        if (ROLE != 2 && ROLE != 4) st4(rw, t0o, s + 1, oflag);
        if (doq) st4(rw, t0o, s + LEAD + 1, qflag);
        if (doq) qkv_in(s + 1 + LEAD);
    };
    int s = 0;
    for (; s < NSEG - LEAD; ++s) iter(s, bool_c<true>{});
    if (ROLE != 4) for (; s < NSEG; ++s) iter(s, bool_c<false>{});
}

__global__ void __launch_bounds__(256, 1) k_main(Params p) {
    extern __shared__ __attribute__((aligned(16))) char smem[];
    const lptr lds = (lptr)smem;
    const int b = blockIdx.x, tid = threadIdx.x;
    if (b < 128 && (b & 7) < 4) chain_role(p, lds, (b >> 3) * 4 + (b & 3), tid);
    else {
        const int u = (b >> 3) * 4 + (b & 3);
        const int j = (b & 4) ? (u < 64 ? u : u + 56) : (u < 120 ? u : u + 64);
        if (j < 64) stream_role<0>(p, lds, j, tid);
        else if (j < 120) stream_role<1>(p, lds, j, tid);
        else if (j < 152) stream_role<2>(p, lds, j, tid);
        else if (j < 168) stream_role<3>(p, lds, j, tid);
        else if (j < 184) stream_role<5>(p, lds, j, tid);
        else stream_role<4>(p, lds, j, tid);
    }
}

__global__ void __launch_bounds__(256) k_init(Params p) {
    __shared__ float red[4][64];
    const int tid = threadIdx.x, w = tid >> 6, lane = tid & 63;
    if (blockIdx.x < 96) {
        const int r16 = lane >> 2, cc = lane & 3, n0 = blockIdx.x * 32 + cc * 8;
        u32x4 wv[8]; float s[8];
#pragma unroll
        for (int it = 0; it < 8; ++it) wv[it] = *(const u32x4*)(p.wmod + (size_t)(w * 128 + it * 16 + r16) * 3072 + n0);
#pragma unroll
        for (int it = 0; it < 8; ++it) { const float cv = bflo(p.c[w * 128 + it * 16 + r16]); s[it] = rb(cv / (1.f + __expf(-cv))); }
        float acc[8] = {};
#pragma unroll
        for (int it = 0; it < 8; ++it) {
            acc[0] += s[it] * bflo(wv[it].x); acc[1] += s[it] * bfhi(wv[it].x); acc[2] += s[it] * bflo(wv[it].y); acc[3] += s[it] * bfhi(wv[it].y);
            acc[4] += s[it] * bflo(wv[it].z); acc[5] += s[it] * bfhi(wv[it].z); acc[6] += s[it] * bflo(wv[it].w); acc[7] += s[it] * bfhi(wv[it].w);
        }
#pragma unroll
        for (int i = 0; i < 8; ++i) { acc[i] += dpp<0x124>(acc[i]); acc[i] += dpp<0x128>(acc[i]); acc[i] = xsum(acc[i]); }
        if (lane < 4) for (int i = 0; i < 8; ++i) red[w][cc * 8 + i] = acc[i];
        __syncthreads();
        if (tid < 32) ((float*)(p.ws + WS_MOD))[blockIdx.x * 32 + tid] = rb((red[0][tid] + red[1][tid]) + (red[2][tid] + red[3][tid]));
    } else if (blockIdx.x < 128 && (blockIdx.x & 7) == 0) {
        const unsigned a = WS_HO + 240 * 16384 + ((blockIdx.x >> 3) & 1) * 8192 + w * 1024 + (blockIdx.x & 16) * 16;
        unsigned best = ~0u;
        unsigned* ap = (unsigned*)(p.ws + a);
        for (int rep = 0; rep < PREP; ++rep) {
            unsigned long long t0, t1; unsigned r = 0;
            asm volatile("s_memtime %0\n\ts_waitcnt lgkmcnt(0)" : "=s"(t0) :: "memory");
            if (lane == 0) r = __hip_atomic_fetch_add(ap, 1u, __ATOMIC_RELAXED, __HIP_MEMORY_SCOPE_AGENT);
            asm volatile("s_waitcnt vmcnt(0)\n\ts_memtime %0\n\ts_waitcnt lgkmcnt(0)" : "=s"(t1), "+v"(r) :: "memory");
            best = min(best, (unsigned)(t1 - t0));
        }
        if (lane == 0) red[w][0] = (float)best;
        __syncthreads();
        if (tid < 2) ((float*)(p.ws + WS_CFG))[((blockIdx.x >> 3) & 1) * 4 + ((blockIdx.x >> 4) & 1) * 2 + tid] = fminf(red[tid][0], red[2 + tid][0]);
    } else if (blockIdx.x >= 256) {
        const unsigned bx = blockIdx.x - 256;
        if ((bx & 7) < 4 && (bx >> 6) < PRE) {
            const unsigned hd = (bx & 3) + 4 * ((bx >> 3) & 7), part = bx >> 6;
            const u16* W = (part < 2 ? p.Wq : p.Wk) + (size_t)((part & 1) * 256 + (tid >> 4)) * HD + hd * 128 + (tid & 15) * 8;
            unsigned acc = 0;
#pragma unroll
            for (int i = 0; i < 16; ++i) { const u32x4 v = *(const u32x4*)(W + (size_t)i * 16 * HD); acc ^= v.x ^ v.y ^ v.z ^ v.w; }
            asm volatile("" :: "v"(acc));
        }
    } else {
        const unsigned t = (blockIdx.x < 128 ? blockIdx.x - 97 - ((blockIdx.x - 96) >> 3) : blockIdx.x - 100) * 256 + tid, nt = (256 - 100) * 256;
        for (int i = 0; i < FDLY; ++i) __builtin_amdgcn_s_sleep(16);
        const u32x4 z = {SENT, SENT, SENT, SENT}, zero = {0, 0, 0, 0};
        for (unsigned i = t; i < FL_N * 16; i += nt) ((u32x4*)(p.ws + WS_FLAG))[i] = zero;
        u32x4* ho = (u32x4*)(p.ws + WS_HO);
        for (unsigned i = t; i < HO_N * 1024; i += nt) {
            u32x4 v = z;
            const unsigned blk = i >> 10, m = i & 511;
            if (blk >= 192 && blk < HO_MEM) continue;
            if (blk >= HO_MEM && blk < HO_MEM + NC) {
                const unsigned* mp = (const unsigned*)(p.mem0 + (2 * ((m >> 2) & 3) + (m & 1)) * D + 16 * (m >> 4) + (m & 2));
                v = (u32x4){min(mp[0], SENT - 1), min(mp[2], SENT - 1), min(mp[4], SENT - 1), min(mp[6], SENT - 1)};
            }
            if (blk == HO_MEM + NC) v = zero;
            ho[i] = v;
        }
    }
}

extern "C" void kernel_launch(void* const* d_in, const int* in_sizes, int n_in, void* d_out, int out_size, void* d_ws, size_t ws_size, hipStream_t stream) {
    Params p;
    p.x = (const u16*)d_in[0]; p.mem0 = (const u16*)d_in[1]; p.Wq = (const u16*)d_in[2]; p.Wk = (const u16*)d_in[3]; p.Wv = (const u16*)d_in[4]; p.Wo = (const u16*)d_in[5];
    p.wg = (const u16*)d_in[8]; p.wu = (const u16*)d_in[9]; p.wd = (const u16*)d_in[10]; p.c = (const u16*)d_in[11]; p.wmod = (const u16*)d_in[12];
    p.out = (u16*)d_out; p.ws = (char*)d_ws + ((0x200000 - ((uintptr_t)d_ws & 0x1FFFFF)) & 0x1FFFFF);
    constexpr int LDSB = 140 * 1024;
    (void)hipFuncSetAttribute((const void*)k_main, hipFuncAttributeMaxDynamicSharedMemorySize, LDSB);
    k_init<<<512, 256, 0, stream>>>(p);
    k_main<<<256, 256, LDSB, stream>>>(p);
}
```

```cpp
#include <hip/hip_runtime.h>
#include <hip/hip_bf16.h>
#include <stdint.h>

typedef unsigned short u16;
typedef __bf16 bf16x2 __attribute__((ext_vector_type(2)));
typedef __bf16 bf16x8 __attribute__((ext_vector_type(8)));
typedef short s16x4 __attribute__((ext_vector_type(4)));
typedef float f32x4 __attribute__((ext_vector_type(4)));
typedef float f32x2 __attribute__((ext_vector_type(2)));
typedef unsigned u32x2 __attribute__((ext_vector_type(2)));
typedef unsigned u32x4 __attribute__((ext_vector_type(4)));
typedef unsigned u32x8 __attribute__((ext_vector_type(8)));
#define LAS __attribute__((address_space(3)))
typedef LAS char* lptr;
#define DI __device__ __forceinline__

#define TX 56
#define TY 44
#define TV1 8
#define TV2 8
#define TPEN 20
#define TM 10
#define SIDE 0
#define SLP 5
#define PREP 5
#define FDLY 0
#define CX 2150
#define CY 430
#define MUP 2
#define PSL 1
#define TS_ 6
#ifndef NVX
#define NVX 2
#endif
#define FW ""
#define PRE 2
#define NPRE 4
#define LEAD 16

constexpr int S = 2048, D = 512, NSEG = 64, F = 1792, HD = 4096;
constexpr unsigned SENT = 0xFFFFFFFFu;
constexpr int NRING = 3;
constexpr unsigned WS_MOD = 0;
constexpr unsigned WS_FLAG = 131072;
constexpr unsigned FL_LN = 0, FL_ARM = 64, FL_QKV = 128, FL_L = 320, FL_KM = 384, FL_B = 448, FL_C = 512, FL_A = 576, FL_G = 600, FL_N = 640;
constexpr unsigned WS_CFG = 12544;
#define NC 4
constexpr unsigned WS_HO = 220u << 20, HO_MEM = 256, MRS = NC + 1, HO_N = HO_MEM + 3 * MRS;
constexpr unsigned WS_N = 1u << 20, WS_DBG = 3u << 20;
constexpr unsigned WS_H1 = 4u << 20, WS_X1 = 6u << 20, WS_H2 = 10u << 20, WS_G = 14u << 20, WS_O = 28u << 20, WS_QKV = 60u << 20, WS_PB = 156u << 20, WS_END = 246u << 20;

DI float bflo(unsigned x) { return __uint_as_float(x << 16); }
DI float bfhi(unsigned x) { return __uint_as_float(x & 0xffff0000u); }
DI unsigned pk(float lo, float hi) { bf16x2 v = {(__bf16)lo, (__bf16)hi}; return __builtin_bit_cast(unsigned, v); }
DI float rb(float x) { return (float)(__bf16)x; }
DI void bar() { asm volatile("s_waitcnt lgkmcnt(0)\n\ts_barrier" ::: "memory"); }
DI void drain() { asm volatile("s_waitcnt vmcnt(0)" ::: "memory"); }
DI u32x2 trread(lptr p) { return __builtin_bit_cast(u32x2, __builtin_amdgcn_ds_read_tr16_b64_v4i16((LAS s16x4*)p)); }
DI u32x4 tr2(lptr p, int off) { const u32x2 a = trread(p), b = trread(p + off); return (u32x4){a.x, a.y, b.x, b.y}; }
DI u32x4 ldsr128(lptr p) { return *(LAS u32x4*)p; }
DI void ldsw128(lptr p, u32x4 v) { *(LAS u32x4*)p = v; }
DI void ldsw64(lptr p, u32x2 v) { *(LAS u32x2*)p = v; }
DI void ldsw32(lptr p, unsigned v) { *(LAS unsigned*)p = v; }
DI f32x4 mfma32(u32x4 a, u32x4 b, f32x4 c) { return __builtin_amdgcn_mfma_f32_16x16x32_bf16(__builtin_bit_cast(bf16x8, a), __builtin_bit_cast(bf16x8, b), c, 0, 0, 0); }
DI float swadd32(float a, float b) {
    auto r = __builtin_amdgcn_permlane32_swap(__float_as_uint(a), __float_as_uint(b), false, false);
    return __uint_as_float(r[0]) + __uint_as_float(r[1]);
}
DI float vmax(float a, float b) { float r; asm("v_max_f32 %0, %1, %2" : "=v"(r) : "v"(a), "v"(b)); return r; }
DI float vmax3(float a, float b, float c) { float r; asm("v_max3_f32 %0, %1, %2, %3" : "=v"(r) : "v"(a), "v"(b), "v"(c)); return r; }
DI float xmax(float x) {
    auto s = __builtin_amdgcn_permlane16_swap(__float_as_uint(x), __float_as_uint(x), false, false);
    x = vmax(__uint_as_float(s[0]), __uint_as_float(s[1]));
    auto t = __builtin_amdgcn_permlane32_swap(__float_as_uint(x), __float_as_uint(x), false, false);
    return vmax(__uint_as_float(t[0]), __uint_as_float(t[1]));
}
DI float xsum(float x) {
    auto s = __builtin_amdgcn_permlane16_swap(__float_as_uint(x), __float_as_uint(x), false, false);
    x = __uint_as_float(s[0]) + __uint_as_float(s[1]);
    auto t = __builtin_amdgcn_permlane32_swap(__float_as_uint(x), __float_as_uint(x), false, false);
    return __uint_as_float(t[0]) + __uint_as_float(t[1]);
}
template <int CTRL> DI float dpp(float x) { return __builtin_bit_cast(float, __builtin_amdgcn_mov_dpp(__builtin_bit_cast(int, x), CTRL, 0xf, 0xf, true)); }
template <int CTRL> DI int dppi(int x) { return __builtin_amdgcn_mov_dpp(x, CTRL, 0xf, 0xf, true); }
DI unsigned mx4(u32x4 v, unsigned m) { return max(max(max(v.x, v.y), max(v.z, v.w)), m); }
typedef __amdgpu_buffer_rsrc_t rsrc_t;
DI rsrc_t mkrs(const void* p, unsigned bytes) { return __builtin_amdgcn_make_buffer_rsrc((void*)p, 0, bytes, 0x00020000); }
DI u32x4 ld16(rsrc_t r, unsigned off, unsigned soff = 0) { return __builtin_amdgcn_raw_buffer_load_b128(r, off, soff, 16); }
DI u32x2 ld8(rsrc_t r, unsigned off, unsigned soff) { return __builtin_amdgcn_raw_buffer_load_b64(r, off, soff, 16); }
DI unsigned ld4(rsrc_t r, unsigned off, unsigned soff = 0) { return __builtin_amdgcn_raw_buffer_load_b32(r, off, soff, 16); }
DI void st16(rsrc_t r, unsigned off, u32x4 v, unsigned soff = 0) { __builtin_amdgcn_raw_buffer_store_b128(v, r, off, soff, 16); }
DI void st8(rsrc_t r, unsigned off, u32x2 v, unsigned soff) { __builtin_amdgcn_raw_buffer_store_b64(v, r, off, soff, 16); }
DI void st4(rsrc_t r, unsigned off, unsigned v, unsigned soff = 0) { __builtin_amdgcn_raw_buffer_store_b32(v, r, off, soff, 16); }
DI u32x4 ldw(rsrc_t r, unsigned voff, unsigned soff) { return __builtin_amdgcn_raw_buffer_load_b128(r, voff, soff, 0); }
DI unsigned rt() { return (unsigned)__builtin_amdgcn_s_memrealtime(); }
DI void wait_rt(unsigned t, unsigned q = 1) {
    int d;
    do d = (int)(t - rt()); while (d >= (int)q && d < 4096);
    d = min(d, 8);
    for (; d > 0; --d) asm volatile("");
    asm volatile("" ::: "memory");
}

template <bool V> struct bool_c { static constexpr bool value = V; };
DI unsigned loc(unsigned base, unsigned off, unsigned cfg, unsigned far) { const unsigned x = base + ((off >> 13) << 14) + (off & 8191); return x + ((((cfg >> ((x >> 21) & 1)) ^ far) & 1) << 13); }
DI unsigned qkv_off(unsigned i, unsigned sec, unsigned cfg) { return loc(WS_QKV, (i * 3 + sec) * 8192u, cfg, 1); }
DI unsigned flg(unsigned line, unsigned cfg) { return loc(WS_FLAG, line * 128, cfg, line >= FL_KM); }
DI unsigned kvm_off(unsigned ho, unsigned cfg, unsigned i) { return ho + (8u << 20) + (i >> 1) * 16384 + ((cfg >> ((i >> 8) & 1)) & 1) * 8192 + (i & 1) * 4096; }
DI unsigned get_cfg(const char* ws) {
    const f32x4 a = *(const f32x4*)(ws + WS_CFG), b = *(const f32x4*)(ws + WS_CFG + 16);
    return __builtin_amdgcn_readfirstlane(fminf(fminf(b[0], b[1]), fminf(b[2], b[3])) < fminf(fminf(a[0], a[1]), fminf(a[2], a[3])) ? 2u : 1u);
}
struct Params {
    const u16 *x, *mem0, *Wq, *Wk, *Wv, *Wo, *wg, *wu, *wd, *c, *wmod;
    u16* out; char* ws;
};

DI float lnmod(float v, float mb, float isd, float t, float sh) { return rb(rb(rb(v - mb) * isd) * t) + sh; }

DI void wait_flags(rsrc_t rw, unsigned cfg, unsigned first, int cnt, int stride, unsigned need, int lane, unsigned& cap) {
    const unsigned off = flg(first + (lane < cnt ? lane : 0) * stride, cfg);
    for (unsigned spins = 0;; ++spins) {
        asm volatile("" ::: "memory");
        if (__all(ld4(rw, off) >= need)) break;
        if (spins > cap) { cap = 0; break; }
        __builtin_amdgcn_s_sleep(2);
    }
    asm volatile("" ::: "memory");
}

constexpr int PSTR = 272;
DI void panel_issue(const u16* srcA, const u16* srcB, int ld, int tid, u32x4 (&v)[32]) {
    const rsrc_t ra = mkrs(srcA, (511 * ld + 64) * 2), rb_ = mkrs(srcB, (511 * ld + 64) * 2);
    const unsigned voff = ((tid >> 3) * ld + (tid & 7) * 8) * 2;
#pragma unroll
    for (int i = 0; i < 16; ++i) { v[2 * i] = ldw(ra, voff, i * 32 * ld * 2); v[2 * i + 1] = ldw(rb_, voff, i * 32 * ld * 2); }
}
DI void panel_finish(lptr lds, int tid, int colA, int colB, u32x4 (&v)[32], u32x4 (&wa)[16], u32x4 (&wb)[16]) {
    const int lane = tid & 63, g = lane >> 4, q = (lane & 15) >> 2;
    const lptr wp = lds + (tid >> 3) * PSTR + (tid & 7) * 16;
#pragma unroll
    for (int half = 0; half < 2; ++half) {
        bar();
#pragma unroll
        for (int i = 0; i < 8; ++i) { ldsw128(wp + i * 32 * PSTR, v[16 * half + 2 * i]); ldsw128(wp + i * 32 * PSTR + 128, v[16 * half + 2 * i + 1]); }
        bar();
        lptr pa = lds + (8 * g + q) * PSTR + colA * 2, pb = lds + (8 * g + q) * PSTR + colB * 2;
#pragma unroll
        for (int ks = 0; ks < 8; ++ks) { wa[half * 8 + ks] = tr2(pa + ks * 32 * PSTR, 4 * PSTR); wb[half * 8 + ks] = tr2(pb + ks * 32 * PSTR, 4 * PSTR); asm volatile("" : "+a"(wa[half * 8 + ks]), "+a"(wb[half * 8 + ks])); }
    }
    bar();
}
constexpr int L_A = 0, L_Q = 73728;
#define MFWA(acc, wgt, x) asm volatile("v_mfma_f32_16x16x32_bf16 %0, %1, %2, %0" : "+v"(acc) : "a"(wgt), "v"(x))
#define MFWB(acc, wgt, x) asm volatile("v_mfma_f32_16x16x32_bf16 %0, %2, %1, %0" : "+v"(acc) : "a"(wgt), "v"(x))
template <bool SWAP> DI void tile_mma(lptr lds, int lane, const u32x4 (&wa)[16], const u32x4 (&wb)[16], f32x4& ca, f32x4& cb) {
    ca = (f32x4){0, 0, 0, 0}; cb = ca;
    u32x4 a[16];
#pragma unroll
    for (int ks = 0; ks < 4; ++ks) a[ks] = ldsr128(lds + ks * 1024 + lane * 16);
    asm volatile("s_nop 1" : "+v"(ca), "+v"(cb));
    __builtin_amdgcn_sched_barrier(0);
#pragma unroll
    for (int ks = 0; ks < 16; ++ks) {
        if (SWAP) MFWB(ca, wa[ks], a[ks]); else MFWA(ca, wa[ks], a[ks]);
        if (ks + 4 < 16) a[ks + 4] = ldsr128(lds + (ks + 4) * 1024 + lane * 16);
        __builtin_amdgcn_sched_barrier(0);
        if (SWAP) MFWB(cb, wb[ks], a[ks]); else MFWA(cb, wb[ks], a[ks]);
    }
    asm volatile("s_nop 7" : "+v"(ca), "+v"(cb));
}

constexpr int CSTR = 656, OSTR = 1040;
constexpr int L_MEMF = 0, L_QT = 8192, L_KT = 12288, L_VT = 16384, L_RED = 20480, L_TS = 21504, L_SEL = 23552, L_ONES = 24576, L_QKV = 25600;
constexpr float SC_L2E = 0.08838834764831845f * 1.4426950408889634f;
constexpr unsigned OOB = 0x80000000u;
#define SMFA(acc, a, w) asm volatile("v_smfmac_f32_16x16x64_bf16 %0, %1, %2, %3" : "+v"(acc) : "v"(a), "a"(w), "v"(sidx))
#define SMFV(acc, a, w) asm volatile("v_smfmac_f32_16x16x64_bf16 %0, %1, %2, %3" : "+v"(acc) : "v"(a), "v"(w), "v"(sidx))
#define MF0(acc, a, b) asm volatile("v_mfma_f32_16x16x32_bf16 %0, %1, %2, 0" : "=&v"(acc) : "v"(a), "v"(b))
#define MF(acc, a, b) asm volatile("v_mfma_f32_16x16x32_bf16 %0, %1, %2, %0" : "+v"(acc) : "v"(a), "v"(b))
#define NOP2() asm volatile("s_nop 1")

#define SB() __builtin_amdgcn_sched_barrier(0)
DI f32x4 zero4() { f32x2 a, b; asm volatile("v_mov_b64 %0, 0\n\tv_mov_b64 %1, 0" : "=v"(a), "=v"(b)); return (f32x4){a.x, a.y, b.x, b.y}; }
DI u32x4 sent4() { u32x2 a, b; asm volatile("v_mov_b64 %0, -1\n\tv_mov_b64 %1, -1" : "=v"(a), "=v"(b)); return (u32x4){a.x, a.y, b.x, b.y}; }
DI float vadd(float a, float b) { float r; asm volatile("v_add_f32 %0, %1, %2" : "=v"(r) : "v"(a), "v"(b)); return r; }
DI unsigned vcvt(float a, float b) { unsigned r; asm volatile("v_cvt_pk_bf16_f32 %0, %1, %2" : "=v"(r) : "v"(a), "v"(b)); return r; }
template <int O0, int O1> DI void dsw2(lptr p, unsigned a, unsigned b) { asm volatile("ds_write2_b32 %0, %1, %2 offset0:%3 offset1:%4" :: "v"(p), "v"(a), "v"(b), "i"(O0), "i"(O1) : "memory"); }
#define ST(k)
#define ST2(k)
#define TS(k)
#define SU(id, k)
DI int wave_max_rel(unsigned v, unsigned base) {
    int d = (int)(v - base);
    d = max(d, dppi<0xB1>(d)); d = max(d, dppi<0x4E>(d)); d = max(d, dppi<0x124>(d)); d = max(d, dppi<0x128>(d));
    auto s = __builtin_amdgcn_permlane16_swap((unsigned)d, (unsigned)d, false, false); d = max((int)s[0], (int)s[1]);
    auto t = __builtin_amdgcn_permlane32_swap((unsigned)d, (unsigned)d, false, false); d = max((int)t[0], (int)t[1]);
    return __builtin_amdgcn_readfirstlane(d);
}

DI int lane_id() { int l; asm volatile("v_mbcnt_lo_u32_b32 %0, -1, 0\n\tv_mbcnt_hi_u32_b32 %0, -1, %0" : "=v"(l)); return l; }
DI void chain_role(const Params& p, lptr lds, const int b, const int tid0) {
    const int w = __builtin_amdgcn_readfirstlane(tid0 >> 6);
    const int h = (b & 3) + 4 * (b >> 3), hf = (b >> 2) & 1;
    u32x8 wq[2][8], wk[2][8], wv[8], wo[8];
    const unsigned cfg = get_cfg(p.ws), ho = WS_HO;
    const unsigned ho0 = ho + (cfg & 1) * 8192, ho1 = ho + ((cfg >> 1) & 1) * 8192;
    auto pblk = [&](unsigned ring_, unsigned r) { return (ring_ == 2 ? ho1 : ho0) + (ring_ * 64 + r) * 16384; };
    {
        const rsrc_t rw = mkrs(p.ws, WS_END);
        const unsigned lane = tid0 & 63; const u32x4 z = {SENT, SENT, SENT, SENT};
#pragma unroll
        for (int r = 0; r < 3; ++r) { st16(rw, lane * 16, z, pblk(r, b) + w * 2048); st16(rw, lane * 16, z, pblk(r, b) + w * 2048 + 1024); }
        const unsigned mso = w < 2 ? (2 * w + (lane >> 5)) * 16384 + (lane & 31) * 4 : b == 0 ? NC * 16384 + (w - 2) * 256 + lane * 4 : OOB;
        st4(rw, mso, SENT, ho0 + (HO_MEM + MRS) * 16384 + b * 128); st4(rw, mso, SENT, ho0 + (HO_MEM + 2 * MRS) * 16384 + b * 128);
        SB();
    }
    {
        const int tid = tid0;
        constexpr int WSTR = 272;
        u32x4 v[3][16];
        const unsigned vo = ((tid >> 3) * HD + (tid & 7) * 8) * 2;
        auto issue = [&](int r, u32x4 (&d)[16]) {
            if (r < 5) {
                const rsrc_t rs = mkrs((r < 2 ? p.Wq : r < 4 ? p.Wk : p.Wv) + h * 128 + (r < 4 ? (r & 1) : hf) * 64, (511 * HD + 64) * 2);
#pragma unroll
                for (int i = 0; i < 16; ++i) d[i] = ldw(rs, vo, i * 32 * HD * 2);
            } else {
                const rsrc_t rs = mkrs(p.Wo + (size_t)(h * 128 + hf * 64) * D, 65536);
#pragma unroll
                for (int i = 0; i < 16; ++i) d[i] = ldw(rs, lane_id() * 16, w * 1024 + i * 4096);
            } };
        issue(0, v[0]); issue(1, v[1]); issue(2, v[2]);
        asm volatile("s_waitcnt vmcnt(48)" ::: "memory"); bar();
        st4(mkrs(p.ws, WS_END), lane_id() + w == 0 ? 0u : OOB, 1, flg(FL_ARM + b, cfg));
        const int lane = lane_id();
        const lptr wp = lds + (w * 8 + (lane >> 3)) * WSTR + (lane & 7) * 16;
        const int g = lane >> 4, q = (lane & 15) >> 2, pp = lane & 3;
        auto stage = [&](u32x4 (&d)[16], int half) {
#pragma unroll
            for (int i = 0; i < 16; ++i) ldsw128(wp + i * 32 * WSTR + half * 128, d[i]);
        };
        auto frag = [&](int j, int colbyte) { u32x8 f;
            const lptr base = lds + (((2 * w + j) & 7) * 64 + 8 * g + q) * WSTR + colbyte;
#pragma unroll
            for (int m = 0; m < 4; ++m) { const u32x2 t = trread(base + (32 * (m >> 1) + 4 * (m & 1)) * WSTR); f[2 * m] = t.x; f[2 * m + 1] = t.y; }
            return f; };
        auto take = [&](u32x8 (&dst)[2][8]) {
#pragma unroll
            for (int j = 0; j < 8; ++j) { dst[0][j] = frag(j, w * 64 + 8 * pp); dst[1][j] = frag(j, w * 64 + 32 + 8 * pp); asm volatile("" : "+a"(dst[0][j]), "+a"(dst[1][j])); } };
        stage(v[0], 0); issue(3, v[0]); stage(v[1], 1); issue(4, v[1]); bar(); take(wq); bar();
        stage(v[2], 0); issue(5, v[2]); stage(v[0], 1); bar(); take(wk); bar();
        stage(v[1], 0); bar();
#pragma unroll
        for (int j = 0; j < 8; ++j) wv[j] = frag(j, (32 * (w >> 1) + 8 * pp + 4 * (w & 1)) * 2);
        bar();
#pragma unroll
        for (int i = 0; i < 16; ++i) ldsw128(lds + (w + 4 * i) * OSTR + lane * 16, v[2][i]);
        bar();
        lptr base = lds + (32 * (q >> 1) + 4 * g + (q & 1)) * OSTR;
#pragma unroll
        for (int t = 0; t < 8; ++t) {
            const int col = 128 * w + 64 * (t >> 2) + 16 * pp + 4 * (t & 3);
#pragma unroll
            for (int m = 0; m < 4; ++m) { const u32x2 tv = trread(base + (16 * (m >> 1) + 2 * (m & 1)) * OSTR + col * 2); wo[t][2 * m] = tv.x; wo[t][2 * m + 1] = tv.y; }
        }
        bar();
    }
    const int lane = lane_id(), tid = w * 64 + lane, n = lane & 15, g = lane >> 4;
    const rsrc_t rw = mkrs(p.ws, WS_END);
    const int hi = (n >> 1) & 1, rowi = 2 * (n >> 2) + (n & 1);
    const int sidx = hi ? 0xEEEE : 0x4444;
    unsigned cap = 1u << 17;
    const unsigned l16 = lane * 16;
    const unsigned xc = (b & 3) + 4 * ((b >> 2) % (NC / 4));
    const unsigned t0o = tid == 0 ? 0u : OOB;
    auto qkv_issue = [&](int s, u32x4 (&v)[3]) {
        const unsigned kb_ = qkv_off(s * 32 + h, 1, cfg) + w * 2048;
        v[0] = ld16(rw, l16, kb_); v[1] = ld16(rw, l16, kb_ + 1024);
        v[2] = ld16(rw, l16, qkv_off(s * 32 + h, 2, cfg) + hf * 4096 + w * 1024);
    };
    auto qkv_store = [&](int s, u32x4 (&v)[3]) {
        lptr d = lds + L_QKV + (s & 1) * 12288 + lane * 16;
        ldsw128(d + w * 2048, v[0]); ldsw128(d + w * 2048 + 1024, v[1]);
        ldsw64(d + 8192 + (w >> 1) * 2048 + (w & 1) * 8, (u32x2){v[2].x, v[2].y}); ldsw64(d + 8192 + (w >> 1) * 2048 + 1024 + (w & 1) * 8, (u32x2){v[2].z, v[2].w});
    };
    const unsigned qflag = flg(FL_QKV + ((lane & 2) ? 64 + (h & 3) + 8 * (h >> 2) + 4 * (lane & 1) : 32 + h), cfg), kflag = __builtin_amdgcn_readfirstlane(flg(FL_KM + b, cfg)), gflag = __builtin_amdgcn_readfirstlane(flg(FL_G, cfg));
    auto qkv_wait = [&](unsigned need) { for (unsigned spins = 0;; ++spins) { asm volatile("" ::: "memory"); if (__all(ld4(rw, qflag) >= need)) break; if (spins > cap) { cap = 0; break; } __builtin_amdgcn_s_sleep(1); } asm volatile("" ::: "memory"); };
    SU(b, 0);
    wait_flags(rw, cfg, FL_ARM, 64, 1, 1, lane, cap);
    { qkv_wait(1); SU(b, 1); u32x4 v[3]; qkv_issue(0, v); qkv_store(0, v); }
    const lptr qtw = lds + L_QT + w * 1024 + (n >> 3) * 256 + (((n & 7) * 32) ^ ((n >> 3) * 128)) + g * 8;
    const lptr tr0 = lds + L_QT + g * 256 + (n >> 2) * 32 + (lane & 3) * 8 + (g & 1) * 128, tr1 = tr0 + ((g & 1) ? -128 : 128);
    auto trf = [&](int off) { const u32x2 a = trread(tr0 + off), c = trread(tr1 + off); return (u32x4){a.x, a.y, c.x, c.y}; };
    const lptr mw = lds + L_MEMF + tid * 16 + w * 1024, tsw = lds + L_TS + tid * 4, vtw = lds + L_VT + tid * 4, vtr = lds + L_VT + lane * 4;
    const lptr rdw = lds + L_RED + (w * 64 + 32 * ((n >> 2) & 1) + 16 * ((n >> 1) & 1) + (n & 1) + 4 * (g & 1)) * 4, rdr = lds + L_RED + (lane & 31) * 8;
    const unsigned tso = w == 3 ? l16 : OOB, sco = w == 3 ? (lane & 1) * 4 : OOB, mso = w < 2 ? (2 * w + (lane >> 5)) * 16384 + (lane & 31) * 4 : b == 0 ? NC * 16384 + (w - 2) * 256 + lane * 4 : OOB,
         kso = (hf | hi) ? OOB : (rowi * 4 + g) * 16, pvo = (2 * (n >> 2) + (g >> 1)) * 16384 + ((n & 3) + 4 * (g & 1)) * 16;
    if (w == 1) ldsw128(lds + L_ONES + lane * 16, (u32x4){0x3f803f80u, 0x3f803f80u, 0x3f803f80u, 0x3f803f80u});
    if (w == 0) { const unsigned one = 0x3f80u << (16 * (n & 1)), k2 = (n >> 1) & 3; ldsw128(lds + L_SEL + lane * 16, (u32x4){k2 == 0 ? one : 0u, k2 == 1 ? one : 0u, k2 == 2 ? one : 0u, k2 == 3 ? one : 0u}); }
    if (tid == 0) *(LAS unsigned*)(lds + L_TS + 1024) = 0;
    const unsigned long long himask = 0xCCCCCCCCCCCCCCCCull;
    bar();
    auto look_at = [&](int sg) { const unsigned o = qkv_off(sg * 32 + h, w == 0 ? 1 : 2, cfg) + (w == 0 ? 0 : hf * 4096); return w >= 2 ? OOB : (w == 1 && lane >= 32) ? qflag : o + l16 * 8; };
    unsigned tvo = look_at(2);
    unsigned pold = pblk(2, b) + w * 2048 + 1024;
    unsigned quant;
    { const unsigned a = rt(); unsigned c, e; do c = rt(); while (c == a); do e = rt(); while (e == c); quant = min(e - c, 8u); }
    unsigned T2 = rt(), T1 = 0, ring = 0;
    unsigned pc = (unsigned)__builtin_amdgcn_s_memtime(), pt = T2, X = TX + CX / 24, Y = TY + CY / 24, mg = 0;
    auto step = [&](const int s, auto last_) {
        constexpr bool LAST = decltype(last_)::value;
        f32x4 aq0, aq1, ak0, ak1, av;
        const unsigned rnext = ring == NRING - 1 ? 0 : ring + 1;
        unsigned mo = ho0 + (HO_MEM + ring * MRS + xc) * 16384 + w * 2048, mo1 = mo + 1024, to = ho0 + (HO_MEM + ring * MRS + NC) * 16384 + xc * 128;
        asm volatile("" : "+s"(mo), "+s"(mo1), "+s"(to));
        u32x4 A[8], kx[2][4], qf[4], kf[4], vx[4], ones; unsigned tsv = 0;
        wait_rt(T2, quant);
        ST(0);
        A[0] = ld16(rw, l16, mo); A[1] = ld16(rw, l16, mo1); tsv = ld4(rw, sco, to);
        SB();
        SB();
        lptr qb = lds + L_QKV + (s & 1) * 12288 + lane * 16;
        lptr ar0 = lds + L_MEMF + lane * 16 + ((2 * w + 2) & 7) * 1024, ar1 = lds + L_MEMF + lane * 16 + ((2 * w + 4) & 7) * 1024, ar2 = lds + L_MEMF + lane * 16 + ((2 * w + 6) & 7) * 1024;
        asm volatile("" : "+v"(qb), "+v"(ar0), "+v"(ar1), "+v"(ar2));
        if constexpr (!LAST) {
#pragma unroll
            for (int ks = 0; ks < NPRE; ++ks) { kx[0][ks] = ldsr128(qb + ks * 1024); kx[1][ks] = ldsr128(qb + 4096 + ks * 1024); }
#pragma unroll
            for (int T = 0; T < NVX; ++T) vx[T] = ldsr128(qb + 8192 + T * 1024);
        }
        aq0 = zero4(); aq1 = zero4(); ak0 = zero4(); ak1 = zero4(); av = zero4();
        if constexpr (!LAST) {
#pragma unroll
            for (int ks = 0; ks < NPRE; ++ks) asm volatile("" : "+v"(kx[0][ks]), "+v"(kx[1][ks]));
#pragma unroll
            for (int T = 0; T < NVX; ++T) asm volatile("" : "+v"(vx[T]));
        }
        SB();
        ldsw128(mw, A[0]); ldsw128(mw + 1024, A[1]); ldsw32(tsw, tsv);
        SB();
        if (__builtin_expect(!__all(mx4(A[0], mx4(A[1], tsv)) != SENT), 0)) {
            for (unsigned spins = 0;; ++spins) {
                asm volatile("" ::: "memory");
                A[0] = ld16(rw, l16, mo); A[1] = ld16(rw, l16, mo1); tsv = ld4(rw, sco, to);
                if (__all(mx4(A[0], mx4(A[1], tsv)) != SENT)) break;
                if (spins > cap) { cap = 0; break; }
            }
            ldsw128(mw, A[0]); ldsw128(mw + 1024, A[1]); ldsw32(tsw, tsv);
        }
        ST(1);
        TS(0);
        NOP2();
        if constexpr (!LAST) { SMFA(aq0, A[0], wq[0][0]); SMFA(aq1, A[0], wq[1][0]); }
        SMFA(ak0, A[0], wk[0][0]);
        bar();
        ST2(9);
        TS(1);
        A[2] = ldsr128(ar0); A[3] = ldsr128(ar0 + 1024); SB();
        SMFA(ak1, A[0], wk[1][0]);
        A[4] = ldsr128(ar1); A[5] = ldsr128(ar1 + 1024); SB();
        SMFV(av, A[0], wv[0]);
        A[6] = ldsr128(ar2); A[7] = ldsr128(ar2 + 1024); SB();
        u32x2 tm = {0, 0}; unsigned look = 0;
#define SM_(a, w) "v_smfmac_f32_16x16x64_bf16 %0, %" #a ", %" #w ", %15\n\t"
#define POST6(C, acc, W, src, dst, MID, WR) { float t0_, t1_; asm volatile( \
                SM_(3, 9) SM_(4, 10) SM_(5, 11) "v_add_f32 %1, %16, %18\n\t" SM_(6, 12) "v_add_f32 %2, %17, %19\n\t" SM_(7, 13) \
                "v_cvt_pk_bf16_f32 %1, %1, %2\n\t" MID SM_(8, 14) "ds_write2_b32 %20, %1, %1 " WR \
                : "+v"(acc), "=&v"(t0_), "=&v"(t1_) : "v"(A[2]), "v"(A[3]), "v"(A[4]), "v"(A[5]), "v"(A[6]), "v"(A[7]), C(W[2]), C(W[3]), C(W[4]), C(W[5]), C(W[6]), C(W[7]), "v"(sidx), \
                  "v"(src[0]), "v"(src[1]), "v"(src[2]), "v"(src[3]), "v"(dst) : "memory"); }
#define POST3(C, acc, K, W, src, dst, WR) { float t0_, t1_; asm volatile( \
                "v_add_f32 %1, %10, %12\n\tv_smfmac_f32_16x16x64_bf16 %0, %3, %6, %9\n\tv_add_f32 %2, %11, %13\n\tv_smfmac_f32_16x16x64_bf16 %0, %4, %7, %9\n\t" \
                "v_cvt_pk_bf16_f32 %1, %1, %2\n\tv_smfmac_f32_16x16x64_bf16 %0, %5, %8, %9\n\tds_write2_b32 %14, %1, %1 " WR \
                : "+v"(acc), "=&v"(t0_), "=&v"(t1_) : "v"(A[K]), "v"(A[K + 1]), "v"(A[K + 2]), C(W[K]), C(W[K + 1]), C(W[K + 2]), "v"(sidx), \
                  "v"(src[0]), "v"(src[1]), "v"(src[2]), "v"(src[3]), "v"(dst) : "memory"); }
        if constexpr (!LAST) {
            SMFA(aq0, A[1], wq[0][1]);
            SMFA(aq1, A[1], wq[1][1]);
            tm = *(LAS u32x2*)(lds + L_TS + 768); SB();
            SMFA(ak0, A[1], wk[0][1]);
            SMFA(ak1, A[1], wk[1][1]); SMFV(av, A[1], wv[1]);
            TS(2);
            SMFA(aq0, A[2], wq[0][2]); SMFA(aq0, A[3], wq[0][3]); SMFA(aq0, A[4], wq[0][4]); SMFA(aq0, A[5], wq[0][5]);
            look = ld4(rw, tvo); SB();
            SMFA(aq0, A[6], wq[0][6]);
            if (__builtin_expect(b == 0, 0)) {
                unsigned sv = s;
                asm volatile("buffer_store_dword %0, %1, %2, %3 offen sc1" :: "v"(sv), "v"(t0o), "s"(rw), "s"(gflag) : "memory");
            }
            SMFA(aq0, A[7], wq[0][7]);
            POST6("a", aq1, wq[1], aq0, qtw, "", "offset0:0 offset1:1");
            TS(3);
            POST6("a", ak0, wk[0], aq1, qtw, "", "offset0:128 offset1:129");
            POST6("a", ak1, wk[1], ak0, qtw + 4096, "s_waitcnt lgkmcnt(0)\n\ts_barrier\n\t", "offset0:0 offset1:1");
                TS(4);
            qf[0] = trf(0); SB();
            SMFV(av, A[2], wv[2]);
            qf[1] = trf(1024); SB();
            SMFV(av, A[3], wv[3]);
            qf[2] = trf(2048); SB();
            SMFV(av, A[4], wv[4]);
            qf[3] = trf(3072); SB();
            POST3("v", av, 5, wv, ak1, qtw + 4096, "offset0:128 offset1:129");
            TS(5);
            f32x4 s0, s1, s2, c[8];
            MF0(s1, kx[0][0], qf[0]);
            ones = ldsr128(lds + L_ONES + lane * 16);
            if (NVX < 3) vx[2] = ldsr128(qb + 8192 + 2048);
            SB();
            MF0(s2, kx[1][0], qf[0]);
            if (NVX < 4) vx[3] = ldsr128(qb + 8192 + 3072);
            SB();
            MF(s1, kx[0][1], qf[1]);
            {
                float t0_, t1_;
                asm volatile("v_add_f32 %2, %8, %10\n\tv_add_f32 %3, %9, %11\n\tv_mfma_f32_16x16x32_bf16 %1, %4, %6, %1\n\tv_cvt_pk_bf16_f32 %2, %2, %3\n\t"
                             "v_mfma_f32_16x16x32_bf16 %0, %5, %7, %0\n\ts_waitcnt lgkmcnt(0)\n\ts_barrier\n\tds_write_b32 %12, %2"
                             : "+v"(s1), "+v"(s2), "=&v"(t0_), "=&v"(t1_) : "v"(kx[1][1]), "v"(kx[0][2]), "v"(qf[1]), "v"(qf[2]), "v"(av[0]), "v"(av[1]), "v"(av[2]), "v"(av[3]), "v"(vtw) : "memory");
            }
                TS(6);
            kf[0] = trf(4096); SB();
            MF(s2, kx[1][2], qf[2]);
            kf[1] = trf(4096 + 1024); SB();
            MF(s1, kx[0][3], qf[3]);
            kf[2] = trf(4096 + 2048); SB();
            MF(s2, kx[1][3], qf[3]);
            kf[3] = trf(4096 + 3072);
            SB();
            ST(2);
            TS(7);
            u32x4 pm;
            float mx;
            {
                float t_;
                MF0(s0, kf[0], qf[0]);
                pm = __builtin_bit_cast(u32x4, zero4());
                asm volatile("v_mfma_f32_16x16x32_bf16 %1, %2, %3, %1\n\tv_max3_f32 %0, %4, %5, %6" : "=&v"(mx), "+v"(s0) : "v"(kf[1]), "v"(qf[1]), "v"(s1[0]), "v"(s1[1]), "v"(s1[2]));
                asm volatile("v_mfma_f32_16x16x32_bf16 %2, %3, %4, %2\n\tv_max3_f32 %1, %5, %6, %7\n\tv_max3_f32 %0, %0, %8, %9"
                             : "+v"(mx), "=&v"(t_), "+v"(s0) : "v"(kf[2]), "v"(qf[2]), "v"(s1[3]), "v"(s2[0]), "v"(s2[1]), "v"(s2[2]), "v"(s2[3]));
                asm volatile("v_mfma_f32_16x16x32_bf16 %2, %3, %4, %2\n\tv_max_f32 %0, %0, %1\n\tv_mov_b32 %1, %0" : "+v"(mx), "+v"(t_), "+v"(s0) : "v"(kf[3]), "v"(qf[3]));
                asm volatile("s_barrier" ::: "memory");
                asm volatile("s_nop 0\n\tv_permlane16_swap_b32 %0, %1\n\tv_max_f32 %0, %0, %1\n\t"
                             "v_mov_b32 %1, %0\n\ts_nop 1\n\tv_permlane32_swap_b32 %0, %1\n\tv_max_f32 %0, %0, %1\n\tv_mul_f32 %0, 0xbe0293ee, %0" FW
                             : "+v"(mx), "+v"(t_) :: "memory");
            }
                ST2(12);
            TS(8);
            u32x4 px;
            float e0, e1, em;
            {
                float a_, b_, c_;
#define FE(d, src) "v_fmamk_f32 " d ", " src ", 0x3e0293ee, %20\n\t"
                asm volatile(FE("%7", "%10") FE("%8", "%11") "v_exp_f32 %7, %7\n\t" FE("%9", "%12") "v_exp_f32 %8, %8\n\t" FE("%1", "%13") "v_exp_f32 %9, %9\n\tv_cvt_pk_bf16_f32 %0, %7, %8\n\t"
                             "v_exp_f32 %1, %1\n\t" FE("%7", "%14") FE("%8", "%15") "v_exp_f32 %7, %7\n\tv_cvt_pk_bf16_f32 %1, %9, %1\n\tv_exp_f32 %8, %8\n\t" FE("%9", "%16") FE("%3", "%17")
                             "v_exp_f32 %9, %9\n\tv_cvt_pk_bf16_f32 %2, %7, %8\n\tv_exp_f32 %3, %3\n\t" FE("%4", "%18") FE("%5", "%19") "v_cvt_pk_bf16_f32 %3, %9, %3\n\tv_max_f32 %6, %4, %5"
                             : "=&v"(px.x), "=&v"(px.y), "=&v"(px.z), "=&v"(px.w), "=&v"(e0), "=&v"(e1), "=&v"(em), "=&v"(a_), "=&v"(b_), "=&v"(c_)
                             : "v"(s1[0]), "v"(s1[1]), "v"(s1[2]), "v"(s1[3]), "v"(s2[0]), "v"(s2[1]), "v"(s2[2]), "v"(s2[3]), "v"(s0[0]), "v"(s0[1]), "v"(mx), "v"(s0));
            }
            const bool slow = __any(em > 0.f);
            SB();
            TS(9);
            f32x4 o[4], ls;
            MF0(o[0], vx[0], px);
            kf[0].x = *(LAS unsigned*)vtr; SB(); kf[2].x = *(LAS unsigned*)(vtr + 512); SB();
            MF0(o[2], vx[2], px);
            kf[1].x = *(LAS unsigned*)(vtr + 256); SB(); kf[3].x = *(LAS unsigned*)(vtr + 768); SB();
            float up = 0.f;
            if (__builtin_expect(slow, 0)) {
                up = xmax(vmax(em, 0.f)); e0 -= up; e1 -= up;
            }
            SB();
            MF0(o[1], vx[1], px);
            asm volatile("v_exp_f32 %1, %1\n\tv_mfma_f32_16x16x32_bf16 %0, %2, %3, 0" : "=&v"(o[3]), "+v"(e0) : "v"(vx[3]), "v"(px));
            asm volatile("v_exp_f32 %2, %2\n\tv_mfma_f32_16x16x32_bf16 %0, %4, %5, 0\n\tv_cvt_pk_bf16_f32 %1, %3, %2" : "=&v"(ls), "=&v"(pm.x), "+v"(e1) : "v"(e0), "v"(ones), "v"(px));
            SB();
            if (__builtin_expect(slow, 0)) {
                asm volatile("s_nop 7" : "+v"(o[0]), "+v"(o[1]), "+v"(o[2]), "+v"(o[3]), "+v"(ls));
                const float al = __builtin_amdgcn_exp2f(-up);
                o[0] *= al; o[1] *= al; o[2] *= al; o[3] *= al; ls *= al;
            }
            c[0] = zero4();
            asm volatile("" : "+v"(pm), "+v"(o[0]), "+v"(o[1]), "+v"(o[2]), "+v"(o[3]), "+v"(ls));
            TS(10);
            MF(ls, ones, pm);
            c[1] = zero4();
            MF(o[0], kf[0], pm);
            c[2] = zero4();
            MF(o[2], kf[2], pm);
            c[3] = zero4();
            MF(o[1], kf[1], pm);
            c[4] = zero4();
            u32x4 o8;
            {
                float inv; f32x2 il, ih, t0_, t1_;
                asm volatile("v_mfma_f32_16x16x32_bf16 %0, %3, %4, %0\n\tv_rcp_f32 %1, %5\n\ts_nop 0\n\tv_cndmask_b32_e64 %2, 0, %1, %6\n\tv_cndmask_b32_e64 %1, %1, 0, %6"
                             : "+v"(o[3]), "=&v"(il.x), "=&v"(ih.x) : "v"(kf[3]), "v"(pm), "v"(ls[0]), "s"(himask), "v"(ls));
                (void)inv; (void)t0_; (void)t1_;
                TS(11);
                c[5] = zero4();
                f32x4 oa = o[0] * il.x + o[2] * ih.x;
                o8.x = pk(oa[0], oa[1]); o8.y = pk(oa[2], oa[3]);
                asm volatile("" : "+v"(o8.x), "+v"(o8.y), "+v"(o[1]), "+v"(o[3]));
                const f32x4 ob = o[1] * il.x + o[3] * ih.x;
                o8.z = pk(ob[0], ob[1]); o8.w = pk(ob[2], ob[3]);
            }
            ST2(13);
            asm volatile("s_nop 1" : "+v"(o8));
            TS(12);
            SB();
            SMFV(c[0], o8, wo[0]); c[6] = zero4(); SMFV(c[1], o8, wo[1]); c[7] = zero4(); SMFV(c[2], o8, wo[2]); SMFV(c[3], o8, wo[3]);
            const unsigned slot = pblk(ring, 16 * w) + b * 128;
            u32x4 pa;
            {
                float t_;
                asm volatile("v_add_f32 %4, %15, %17\n\tv_smfmac_f32_16x16x64_bf16 %0, %9, %10, %14\n\tv_add_f32 %8, %16, %18\n\tv_smfmac_f32_16x16x64_bf16 %1, %9, %11, %14\n\t"
                             "v_cvt_pk_bf16_f32 %4, %4, %8\n\tv_smfmac_f32_16x16x64_bf16 %2, %9, %12, %14\n\tv_add_f32 %5, %19, %21\n\tv_smfmac_f32_16x16x64_bf16 %3, %9, %13, %14\n\t"
                             "v_add_f32 %8, %20, %22\n\tv_cvt_pk_bf16_f32 %5, %5, %8\n\tv_add_f32 %6, %23, %25\n\tv_add_f32 %8, %24, %26\n\tv_cvt_pk_bf16_f32 %6, %6, %8"
                             : "+v"(c[4]), "+v"(c[5]), "+v"(c[6]), "+v"(c[7]), "=&v"(pa.x), "=&v"(pa.y), "=&v"(pa.z), "=&v"(pa.w), "=&v"(t_)
                             : "v"(o8), "v"(wo[4]), "v"(wo[5]), "v"(wo[6]), "v"(wo[7]), "v"(sidx),
                               "v"(c[0][0]), "v"(c[0][1]), "v"(c[0][2]), "v"(c[0][3]), "v"(c[1][0]), "v"(c[1][1]), "v"(c[1][2]), "v"(c[1][3]), "v"(c[2][0]), "v"(c[2][1]), "v"(c[2][2]), "v"(c[2][3]));
            }
            auto fold = [](f32x4 v) { const f32x2 t = (f32x2){v[0], v[1]} + (f32x2){v[2], v[3]}; return pk(t.x, t.y); };
            pa.w = fold(c[3]);
            st16(rw, pvo, pa, slot);
            TS(13);
            SB();
            st16(rw, pvo, (u32x4){fold(c[4]), fold(c[5]), fold(c[6]), fold(c[7])}, slot + 131072);
            TS(14);
            SB();
            const unsigned now = rt();
            ST(3);
            T1 = __builtin_amdgcn_readfirstlane(tm.x); T2 = __builtin_amdgcn_readfirstlane(tm.y);
            { const int dl = s == 0 ? 99 : (int)(now + TV1 - T1); if (dl > 0) T1 = now + TV1 + (dl > TS_ ? TM : 0);
            }
        } else {
            st4(rw, b == 0 ? t0o : OOB, s, gflag);
#pragma unroll
            for (int ks = 1; ks < 8; ++ks) { SMFA(ak0, A[ks], wk[0][ks]); SMFA(ak1, A[ks], wk[1][ks]); SMFV(av, A[ks], wv[ks]); }
            asm volatile("s_nop 7" : "+v"(av), "+v"(ak0), "+v"(ak1));
            unsigned d0 = pk(ak0[0] + ak0[2], ak0[1] + ak0[3]); dsw2<0, 1>(qtw + 4096, d0, d0);
            d0 = pk(ak1[0] + ak1[2], ak1[1] + ak1[3]); dsw2<128, 129>(qtw + 4096, d0, d0);
            ldsw32(vtw, pk(av[0] + av[2], av[1] + av[3]));
            bar();
        }
        {
            const u32x4 kv = trf(4096 + w * 1024);
            const u32x4 vv = {*(LAS unsigned*)vtr, *(LAS unsigned*)(vtr + 256), *(LAS unsigned*)(vtr + 512), *(LAS unsigned*)(vtr + 768)};
            const unsigned kb = kvm_off(ho, cfg, s * 32 + h);
            st16(rw, kso, kv, kb + w * 512); st16(rw, tso, vv, kb + 2048 + hf * 1024);
        }
        if constexpr (LAST) return; else {
        ST(4);
        u32x4 p0, p1, nv[3];
        unsigned pb = pblk(ring, b) + w * 2048, pb1 = pb + 1024, mnx = ho0 + (HO_MEM + rnext * MRS) * 16384 + b * 128;
        asm volatile("" : "+s"(pb), "+s"(pb1), "+s"(mnx));
        if (__builtin_expect((unsigned)s + 2 > (unsigned)__builtin_amdgcn_readfirstlane(*(LAS unsigned*)(lds + L_TS + 1024)), 0)) qkv_wait(s + 2);
        qkv_issue(s + 1, nv);
        tvo = look_at(min(s + 3, NSEG - 1));
        const unsigned qmin = min(min(__builtin_amdgcn_readlane(look, 32), __builtin_amdgcn_readlane(look, 33)), min(__builtin_amdgcn_readlane(look, 34), __builtin_amdgcn_readlane(look, 35)));
        if (b == 0 && (s & 3) == 3) {
            const unsigned c = (unsigned)__builtin_amdgcn_s_memtime(), t = rt();
            const float tpc = (float)(t - pt) * __builtin_amdgcn_rcpf((float)(c - pc));
            { int xi = (int)(CX * tpc + 0.5f), yi = (int)(CY * tpc + 0.5f); asm volatile("" : "+v"(xi), "+v"(yi)); X = TX + __builtin_amdgcn_readfirstlane(xi); Y = TY + __builtin_amdgcn_readfirstlane(yi); }
            pc = c; pt = t; mg -= mg > 0;
        }
        wait_rt(T1, quant);
        ST(5);
        p0 = ld16(rw, l16, pb); p1 = ld16(rw, l16, pb1);
        SB();
        qkv_store(s + 1, nv);
        SB();
        u32x4 sel = ldsr128(lds + L_SEL + lane * 16);
        asm volatile("" : "+v"(sel));
        SB();
        f32x4 rr;
        MF0(rr, sel, p0); MF(rr, sel, p1);
        SB();
        if (__builtin_expect(!__all(mx4(p0, mx4(p1, 0u)) != SENT), 0)) {
            for (unsigned spins = 0;; ++spins) {
                asm volatile("" ::: "memory");
                p0 = ld16(rw, l16, pb); p1 = ld16(rw, l16, pb1);
                if (__all(mx4(p0, mx4(p1, 0u)) != SENT)) break;
                if (spins > cap) { cap = 0; break; }
            }
            MF0(rr, sel, p0); MF(rr, sel, p1);
            asm volatile("s_nop 7" : "+v"(rr));
        }
        ST(6);
        {
            asm volatile("s_nop 0" : "+v"(rr));
            unsigned q0, q1, q2, q3;
            asm volatile("v_add_f32_dpp %0, %4, %4 row_ror:8 row_mask:0xf bank_mask:0xf\n\tv_add_f32_dpp %1, %5, %5 row_ror:8 row_mask:0xf bank_mask:0xf\n\t"
                         "v_add_f32_dpp %2, %6, %6 row_ror:8 row_mask:0xf bank_mask:0xf\n\tv_add_f32_dpp %3, %7, %7 row_ror:8 row_mask:0xf bank_mask:0xf"
                         : "=&v"(q0), "=&v"(q1), "=&v"(q2), "=&v"(q3) : "v"(rr[0]), "v"(rr[1]), "v"(rr[2]), "v"(rr[3]));
            dsw2<0, 8>(rdw, q0, q1); dsw2<2, 10>(rdw, q2, q3);
        }
        bar();
        {
            unsigned mv;
            if (__builtin_expect(b == 0 && w >= 2, 0)) {
                const unsigned now = rt() + 2; const int dl = s == 0 ? 99 : (int)(now + TV2 - T2);
                if (dl > TS_ && s > 1) mg = min(mg + MUP, 24u);
                const unsigned n1 = min((dl > 0 ? now + TV2 + (dl > TS_ ? TPEN : 0) : T2) + X + mg, SENT - 1024);
                mv = (lane & 1) ? n1 + Y + (mg >> 1) : n1;
            } else {
                f32x2 a0, a1, a2, a3;
                asm volatile("ds_read_b64 %0, %4\n\tds_read_b64 %1, %4 offset:256\n\tds_read_b64 %2, %4 offset:512\n\tds_read_b64 %3, %4 offset:768\n\ts_waitcnt lgkmcnt(0)"
                             : "=&v"(a0), "=&v"(a1), "=&v"(a2), "=&v"(a3) : "v"(rdr) : "memory");
                const f32x2 t = (a0 + a1) + (a2 + a3);
                mv = pk(t.x, t.y);
            }
            st4(rw, mso, mv, mnx);
            SB();
            const unsigned now = rt(); const int dl = s == 0 ? 99 : (int)(now + TV2 - T2);
            SB();
            {
                const u32x4 z = sent4();
                st16(rw, l16, z, pb);
                st16(rw, l16, z, pb1);
                pold = pb1;
                st4(rw, mso, z.x, ho0 + (HO_MEM + ring * MRS) * 16384 + b * 128);
            }
            if (dl > 0) T2 = now + TV2 + (dl > TS_ ? TM : 0);
        }
        if (__builtin_expect(s == NSEG - 2, 0)) { unsigned sv = s + 1; asm volatile("buffer_store_dword %0, %1, %2, %3 offen sc1" :: "v"(sv), "v"(tso - 16 * 63), "s"(rw), "s"(kflag) : "memory"); }
        if (w == 1) *(LAS unsigned*)(lds + L_TS + 1024) = qmin;
        ST(7);
        ring = rnext;
        ST(8);
        }
    };
    for (int s = 0; s < NSEG - 1; ++s) step(s, bool_c<false>{});
    step(NSEG - 1, bool_c<true>{});
    drain(); bar();
    st4(rw, t0o, NSEG, kflag);
}

template <int ROLE>
DI void stream_role(const Params& p, lptr lds, const int j, const int tid) {
    const int w = __builtin_amdgcn_readfirstlane(tid >> 6), lane = tid & 63, n = lane & 15, g = lane >> 4, pp = lane & 3;
    const rsrc_t rw = mkrs(p.ws, WS_END);
    const float* mod = (const float*)(p.ws + WS_MOD);
    const unsigned cfg = get_cfg(p.ws);
    unsigned cap = 1u << 17;
    const unsigned l16 = lane * 16, t0o = tid == 64 ? 0u : OOB;
    const int r = ROLE == 0 ? j : ROLE == 1 ? j - 64 : ROLE == 2 ? j - 120 : ROLE == 3 ? j - 152 : ROLE == 5 ? j - 168 : j - 128;
    const int tt = ROLE == 3 ? r >> 3 : ROLE == 1 ? r & 1 : (r >> 3) & 1;
    const int cgx = ROLE == 1 ? r >> 1 : (r & 7) + 8 * (r >> 4);
    constexpr int QD = ROLE == 0 ? 2 : (ROLE == 1 || ROLE == 4) ? 1 : 0;
    const int qm = QD == 2 ? r >> 5 : 2, qh = QD == 2 ? r & 31 : (r & 3) + 4 * (r >> 3), qtt = QD == 2 ? 0 : (r >> 2) & 1;
    const unsigned qline = FL_QKV + (QD == 2 ? r : 64 + r);
    u32x4 pv[32];
    if (QD) { const u16* W = (qm == 0 ? p.Wq : qm == 1 ? p.Wk : p.Wv) + qh * 128; panel_issue(W, W + 64, HD, tid, pv); }
    else {
        const int sg = ROLE == 2 ? r : ROLE == 3 ? 32 + r : 48 + r;
        u32x4 xv[8];
#pragma unroll
        for (int q = 0; q < 8; ++q) xv[q] = *(const u32x4*)(p.ws + WS_N + (size_t)(sg * 32 + w * 8 + q) * 1024 + lane * 16);
        float t[8], sh[8];
        { const f32x4 m0 = *(const f32x4*)(mod + lane * 8), m1 = *(const f32x4*)(mod + lane * 8 + 4), m2 = *(const f32x4*)(mod + 512 + lane * 8), m3 = *(const f32x4*)(mod + 516 + lane * 8);
#pragma unroll
          for (int i = 0; i < 4; ++i) { sh[i] = m0[i]; sh[4 + i] = m1[i]; t[i] = rb(1.f + m2[i]); t[4 + i] = rb(1.f + m3[i]); } }
#pragma unroll
        for (int q = 0; q < 8; ++q) {
            u32x4 o;
#pragma unroll
            for (int i = 0; i < 4; ++i) { const unsigned u = vcvt(bflo(xv[q][i]) * t[2 * i], bfhi(xv[q][i]) * t[2 * i + 1]); o[i] = pk(bflo(u) + sh[2 * i], bfhi(u) + sh[2 * i + 1]); }
            st16(rw, (lane >> 2) * 1024 + (lane & 3) * 16, o, WS_H1 + (unsigned)(sg * 2 + (w >> 1)) * 16384u + ((w & 1) * 8 + q) * 64);
        }
        drain(); bar();
        st4(rw, t0o, 1, flg(FL_LN + sg, cfg));
    }
    SU(64 + j, 0);
    u32x4 qa[16], qb[16], hv[QD ? 4 * QD : 1];
    const unsigned h1off = WS_H1 + (unsigned)(qtt * 16 + w * 4) * 1024u;
    auto h1_issue = [&](int s) {
#pragma unroll
        for (int i = 0; i < 4 * QD; ++i) hv[i] = ld16(rw, (n * 4 + g) * 16, h1off + (unsigned)s * 32768u + (i >> 2) * 16384 + (i & 3) * 1024); };
    auto qkv_in = [&]() {
#pragma unroll
        for (int i = 0; i < 4 * QD; ++i) ldsw128(lds + L_Q + (i >> 2) * 16384 + (w * 4 + (i & 3)) * 1024 + lane * 16, hv[i]);
    };
    auto qkv_mm = [&](int s) {
#pragma unroll
        for (int t = 0; t < QD; ++t) {
            f32x4 ca, cb;
            tile_mma<QD == 1>(lds + L_Q + t * 16384, lane, qa, qb, ca, cb);
            st16(rw, l16, (u32x4){pk(ca[0], ca[1]), pk(ca[2], ca[3]), pk(cb[0], cb[1]), pk(cb[2], cb[3])}, qkv_off(s * 32 + qh, qm, cfg) + (QD == 2 ? t * 4 + w : w * 2 + qtt) * 1024);
        }
    };
    auto qkv_seg = [&](int s) { qkv_in(); h1_issue(min(s + 1, NSEG - 1)); bar(); qkv_mm(s); };
    auto publish = [&](unsigned v) { drain(); bar(); st4(rw, t0o, v, flg(qline, cfg)); };
    u32x4 sa[16], sb[16];
    if (QD) {
        panel_finish(lds, tid, 32 * w + 8 * pp, 32 * w + 8 * pp + 4, pv, qa, qb);
        SU(64 + j, 1);
        if (w == 0) wait_flags(rw, cfg, FL_LN, 64, 1, 1, lane, cap);
        bar();
        SU(64 + j, 2);
        h1_issue(0);
        for (int s = 0; s < 7; ++s) { qkv_seg(s); publish(s + 1); }
        SU(64 + j, 4);
        if (ROLE == 0) { const u16* W = p.Wo + (size_t)(r & 7) * 512 * D + (r >> 4) * 128; panel_issue(W, W + 64, D, tid, pv); }
        if (ROLE == 1) panel_issue(p.wg + cgx * 64, p.wu + cgx * 64, F, tid, pv);
        SB();
        qkv_seg(7); publish(8);
        if (ROLE == 0) panel_finish(lds, tid, 32 * w + 4 * pp, 32 * w + 16 + 4 * pp, pv, sa, sb);
        if (ROLE == 1) panel_finish(lds, tid, 16 * w + 4 * pp, 64 + 16 * w + 4 * pp, pv, sa, sb);
    }
    if (ROLE == 2) {
        const rsrc_t rd = mkrs(p.wd + cgx * 32, (1791 * D + 32) * 2);
#pragma unroll
        for (int half = 0; half < 2; ++half) {
            u32x4 v[14];
#pragma unroll
            for (int i = 0; i < 14; ++i) v[i] = ldw(rd, ((tid >> 2) * D + (tid & 3) * 8) * 2, (half * 896 + i * 64) * D * 2);
            bar();
#pragma unroll
            for (int i = 0; i < 14; ++i) ldsw128(lds + ((tid >> 2) + 64 * i) * 80 + (tid & 3) * 16, v[i]);
            bar();
            if ((w >> 1) == half) {
                lptr pa = lds + ((w & 1) * 448 + 8 * g + (n >> 2)) * 80 + pp * 8;
#pragma unroll
                for (int ks = 0; ks < 14; ++ks) { sa[ks] = tr2(pa + ks * 32 * 80, 4 * 80); sb[ks] = tr2(pa + ks * 32 * 80 + 32, 4 * 80); }
            }
        }
        bar();
    }
    if (QD) for (int s = 8; s < LEAD; ++s) { qkv_seg(s); publish(s + 1); }
    SU(64 + j, 5);
    unsigned poff; unsigned ahead = 0;
    if (ROLE == 0) poff = FL_A + (r & 7) * 2 + (lane & 1);
    if (ROLE == 1) poff = FL_L + tt * 8 + (lane & 7);
    if (ROLE == 2) poff = FL_C + tt + 2 * (lane < 28 ? lane : 0);
    if (ROLE == 3) poff = FL_B + (lane & 7) + tt * 8 + ((lane >> 3) & 3) * 16;
    if (ROLE == 4) poff = FL_G;
    if (ROLE == 5) {
        const int hh = 2 * r + ((lane >> 1) & 1), k = (lane >> 2) & 3, pr = (hh & 3) + 8 * (hh >> 2) + 4 * (lane & 1);
        poff = k == 0 ? FL_KM + pr : k == 3 ? FL_QKV + 64 + pr : FL_QKV + (k - 1) * 32 + hh;
        ahead = (lane & 12) != 0;
        if (lane == 63) { poff = FL_G; ahead = 0; }
    }
    poff = flg(poff, cfg);
    const unsigned oflag = flg((ROLE == 0 ? FL_B : ROLE == 1 ? FL_C : ROLE == 3 ? FL_L : FL_A) + r, cfg), qflag = flg(qline, cfg);
    unsigned seen = 0;
    auto ready = [&](unsigned nd) {
        if (ROLE != 5) return __all(seen >= nd);
        const unsigned gl = __builtin_amdgcn_readlane(seen, 63);
        return __all(((lane & 12) == 0 ? max(seen, gl) : lane == 63 ? nd : seen) >= nd);
    };
    auto wait_in = [&](unsigned need) {
        if (w == 0) {
            const unsigned nd = min(need + ahead, (unsigned)NSEG);
            for (unsigned spins = 0; !ready(nd); ++spins) {
                if (spins > cap) { cap = 0; break; }
                if (spins) __builtin_amdgcn_s_sleep(PSL);
                asm volatile("" ::: "memory");
                seen = ld4(rw, poff);
            }
        }
        bar();
    };
    const unsigned pof0 = w == 0 ? poff : OOB;
    const int hh = 2 * r + (w >> 1), att = w & 1;
    u32x4 xq[4], ka[4], kb[4], vb[8];
    const unsigned l16b = att ? l16 : OOB;
    auto a_issue_qk = [&](int s) {
        const unsigned q_ = qkv_off(s * 32 + hh, 0, cfg) + att * 4096, k_ = qkv_off(s * 32 + hh, 1, cfg);
#pragma unroll
        for (int ks = 0; ks < 4; ++ks) { xq[ks] = ld16(rw, l16, q_ + ks * 1024); ka[ks] = ld16(rw, l16, k_ + ks * 1024); kb[ks] = ld16(rw, l16b, k_ + 4096 + ks * 1024); }
    };
    auto a_issue_v = [&](int s) {
        const unsigned v_ = qkv_off(s * 32 + hh, 2, cfg);
#pragma unroll
        for (int i = 0; i < 8; ++i) vb[i] = ld16(rw, (i & 1) ? l16b : l16, v_ + i * 1024);
    };
    if (ROLE == 5) { wait_in(0); a_issue_qk(0); a_issue_v(0); }
    const int d0 = 128 * w + 16 * (lane >> 3) + 4 * (lane & 3);
    f32x4 cga, cgb, cgc;
    if (ROLE == 3) { cga = *(const f32x4*)(mod + 1024 + d0); cgb = *(const f32x4*)(mod + 1536 + d0); cgc = *(const f32x4*)(mod + 2048 + d0);
#pragma unroll
        for (int i = 0; i < 4; ++i) cgc[i] = rb(1.f + cgc[i]); }
    if (ROLE == 2) cga = *(const f32x4*)(mod + 2560 + cgx * 32 + (w & 1) * 16 + 4 * g);
    u32x2 xnext = {0, 0};
    if (ROLE == 3) xnext = *(const u32x2*)(p.x + (size_t)(tt * 16 + 2 * (r & 7) + ((lane >> 2) & 1)) * D + d0);
    if (QD) qkv_in();
    auto iter = [&](const int s, auto doq_) {
        constexpr bool doq = decltype(doq_)::value;
        u32x2 xv = {0, 0};
        wait_in(s + 1);
        u32x4 a[14];
        if (ROLE == 0) {
#pragma unroll
            for (int i = 0; i < 4; ++i) a[i] = ld16(rw, l16, loc(WS_O, (unsigned)((s * 2 + tt) * 128 + (r & 7) * 16 + w * 4) * 1024u, cfg, 1) + i * 1024);
        }
        if (ROLE == 1) {
#pragma unroll
            for (int i = 0; i < 4; ++i) a[i] = ld16(rw, (n * 4 + g) * 16, loc(WS_H2, (unsigned)((s * 2 + tt) * 16 + w * 4) * 1024u, cfg, 0) + i * 1024);
        }
        if (ROLE == 2) {
#pragma unroll
            for (int ks = 0; ks < 14; ++ks) a[ks] = ld16(rw, (g >> 1) * 512 + n * 32 + (g & 1) * 16, loc(WS_G, (unsigned)((s * 2 + tt) * 56 + w * 14 + ks) * 1024u, cfg, 1));
            xv = ld8(rw, (n >> 3) * 16384 + (n & 7) * 1024 + ((w & 1) * 16 + 4 * g) * 2, loc(WS_X1, (s * 2 + tt) * 16384, cfg, 1) + cgx * 64);
        }
        if (ROLE == 3) {
#pragma unroll
            for (int kg = 0; kg < 8; ++kg) a[kg] = ld16(rw, (lane >> 3) * 1024 + (lane & 7) * 16, loc(WS_PB, (unsigned)(((s * 8 + kg) * 2 + tt) * 32 + 8 * w) * 1024u, cfg, 1) + (r & 7) * 128);
            xv = xnext;
            xnext = *(const u32x2*)(p.x + (size_t)(min(s + 1, NSEG - 1) * 32 + tt * 16 + 2 * (r & 7) + ((lane >> 2) & 1)) * D + d0);
        }
        if (ROLE == 5) {
            const unsigned kb_ = kvm_off(WS_HO, cfg, s * 32 + hh);
#pragma unroll
            for (int ks = 0; ks < 4; ++ks) a[ks] = ld16(rw, ((2 * (n >> 2) + (n & 1)) * 4 + g) * 16, kb_ + ks * 512);
            a[4] = ld16(rw, l16, kb_ + 2048); a[5] = ld16(rw, l16, kb_ + 3072);
        }
        seen = ld4(rw, pof0);
        if (doq) h1_issue(min(s + LEAD + 1, NSEG - 1));
        SB();
        if (doq) qkv_mm(s + LEAD);
        SB();
        if (ROLE == 0 || ROLE == 1) {
#pragma unroll
            for (int i = 0; i < 4; ++i) ldsw128(lds + L_A + (w * 4 + i) * 1024 + lane * 16, a[i]);
            bar();
            f32x4 ca, cb; tile_mma<false>(lds + L_A, lane, sa, sb, ca, cb);
            if (ROLE == 0) {
                const unsigned o = loc(WS_PB, (unsigned)(((s * 8 + (r & 7)) * 2 + tt) * 32 + (r >> 4) * 8 + w * 2) * 1024u, cfg, 1);
                st16(rw, (n * 4 + g) * 16, __builtin_bit_cast(u32x4, ca), o); st16(rw, (n * 4 + g) * 16, __builtin_bit_cast(u32x4, cb), o + 1024);
            } else {
                float q[4];
#pragma unroll
                for (int i = 0; i < 4; ++i) { const float gv = rb(ca[i]); q[i] = rb(gv * __builtin_amdgcn_rcpf(1.f + __expf(-gv))) * rb(cb[i]); }
                st8(rw, n * 32 + g * 8, (u32x2){pk(q[0], q[1]), pk(q[2], q[3])}, loc(WS_G, (unsigned)((s * 2 + tt) * 56 + cgx * 2 + (w >> 1)) * 1024u, cfg, 1) + (w & 1) * 512);
            }
        }
        if (ROLE == 2) {
            f32x4 c0 = {0, 0, 0, 0}, c1 = c0;
            asm volatile("s_nop 1" : "+v"(c0), "+v"(c1));
#pragma unroll
            for (int ks = 0; ks < 14; ++ks) { MFWA(c0, sa[ks], a[ks]); MFWA(c1, sb[ks], a[ks]); }
            asm volatile("s_nop 7" : "+v"(c0), "+v"(c1));
            *(LAS f32x4*)(lds + L_A + (w * 2) * 1024 + lane * 16) = c0; *(LAS f32x4*)(lds + L_A + (w * 2 + 1) * 1024 + lane * 16) = c1;
            bar();
            if (w < 2) {
                lptr q = lds + L_A + w * 1024 + lane * 16;
                const f32x4 y = (*(LAS f32x4*)q + *(LAS f32x4*)(q + 2048)) + (*(LAS f32x4*)(q + 4096) + *(LAS f32x4*)(q + 6144));
                const float o0 = bflo(xv.x) + rb(cga[0] * rb(y[0])), o1 = bfhi(xv.x) + rb(cga[1] * rb(y[1])), o2 = bflo(xv.y) + rb(cga[2] * rb(y[2])), o3 = bfhi(xv.y) + rb(cga[3] * rb(y[3]));
                *(u32x2*)(p.out + (size_t)(s * 32 + tt * 16 + n) * D + cgx * 32 + (w & 1) * 16 + 4 * g) = (u32x2){pk(o0, o1), pk(o2, o3)};
            }
        }
        if (ROLE == 5) {
            f32x4 x0, x1, x2;
            MF0(x1, ka[0], xq[0]); MF0(x2, kb[0], xq[0]);
#pragma unroll
            for (int ks = 1; ks < 4; ++ks) { MF(x1, ka[ks], xq[ks]); MF(x2, kb[ks], xq[ks]); }
            MF0(x0, a[0], xq[0]); MF(x0, a[1], xq[1]); MF(x0, a[2], xq[2]); MF(x0, a[3], xq[3]);
            a_issue_qk(min(s + 1, NSEG - 1));
            asm volatile("s_nop 7" : "+v"(x0), "+v"(x1), "+v"(x2));
            SB();
            const float raw[10] = {x1[0], x1[1], x1[2], x1[3], x2[0], x2[1], x2[2], x2[3], x0[0], x0[1]};
            float sc[10];
            const int qi = 16 * att + n - 4 * g;
#pragma unroll
            for (int i = 0; i < 5; ++i) {
                const unsigned u = vcvt(raw[2 * i], raw[2 * i + 1]);
                const unsigned v = pk(bflo(u) * 0.08838834764831845f, bfhi(u) * 0.08838834764831845f);
                sc[2 * i] = bflo(v); sc[2 * i + 1] = bfhi(v);
            }
#pragma unroll
            for (int i = 0; i < 8; ++i) sc[i] = ((i & 3) + 16 * (i >> 2) <= qi) ? sc[i] : -1e9f;
            float mx = vmax3(vmax3(vmax3(sc[0], sc[1], sc[2]), vmax3(sc[3], sc[4], sc[5]), vmax(sc[6], sc[7])), sc[8], sc[9]);
            mx = xmax(mx) * -1.4426950408889634f;
#pragma unroll
            for (int i = 0; i < 10; ++i) sc[i] = __builtin_amdgcn_exp2f(__builtin_fmaf(sc[i], 1.4426950408889634f, mx));
            const float inv = __builtin_amdgcn_rcpf(xsum((((sc[0] + sc[1]) + (sc[2] + sc[3])) + ((sc[4] + sc[5]) + (sc[6] + sc[7]))) + (sc[8] + sc[9])));
            u32x4 xpx = {pk(sc[0] * inv, sc[1] * inv), pk(sc[2] * inv, sc[3] * inv), pk(sc[4] * inv, sc[5] * inv), pk(sc[6] * inv, sc[7] * inv)};
            u32x4 xpm = {pk(sc[8] * inv, sc[9] * inv), 0u, 0u, 0u};
            f32x4 o[8];
#pragma unroll
            for (int e = 0; e < 4; ++e) {
                u32x4 fa = {vb[2 * e].x, vb[2 * e].y, vb[2 * e + 1].x, vb[2 * e + 1].y}, fb = {vb[2 * e].z, vb[2 * e].w, vb[2 * e + 1].z, vb[2 * e + 1].w};
                u32x4 ma = vb[2 * e], mb = vb[2 * e + 1];
                ma.x = a[4 + (e >> 1)][2 * (e & 1)]; mb.x = a[4 + (e >> 1)][2 * (e & 1) + 1];
                asm volatile("s_nop 1" : "+v"(fa), "+v"(fb), "+v"(ma), "+v"(mb), "+v"(xpx), "+v"(xpm));
                MF0(o[2 * e], fa, xpx); MF0(o[2 * e + 1], fb, xpx); MF(o[2 * e], ma, xpm); MF(o[2 * e + 1], mb, xpm);
            }
            asm volatile("s_nop 7" : "+v"(o[0]), "+v"(o[1]), "+v"(o[2]), "+v"(o[3]), "+v"(o[4]), "+v"(o[5]), "+v"(o[6]), "+v"(o[7]));
#pragma unroll
            for (int e = 0; e < 4; ++e)
                st16(rw, l16, (u32x4){pk(o[2 * e][0], o[2 * e][1]), pk(o[2 * e][2], o[2 * e][3]), pk(o[2 * e + 1][0], o[2 * e + 1][1]), pk(o[2 * e + 1][2], o[2 * e + 1][3])}, loc(WS_O, (unsigned)((s * 2 + att) * 128 + hh * 4) * 1024u, cfg, 1) + e * 1024);
            SB();
            a_issue_v(min(s + 1, NSEG - 1));
        }
        if (ROLE == 3) {
            LAS float* sm = (LAS float*)(lds + L_A);
            const int tk = (lane >> 2) & 1, tok = 2 * (r & 7) + tk;
            f32x4 acc = __builtin_bit_cast(f32x4, a[0]);
#pragma unroll
            for (int kg = 1; kg < 8; ++kg) acc += __builtin_bit_cast(f32x4, a[kg]);
            float x1[4] = {bflo(xv.x), bfhi(xv.x), bflo(xv.y), bfhi(xv.y)};
            float sum = 0.f;
#pragma unroll
            for (int i = 0; i < 4; ++i) { x1[i] = rb(x1[i] + rb(cga[i] * rb(acc[i]))); sum += x1[i]; }
            st8(rw, tk * 1024 + d0 * 2, (u32x2){pk(x1[0], x1[1]), pk(x1[2], x1[3])}, loc(WS_X1, (s * 32 + tt * 16 + 2 * (r & 7)) * 1024, cfg, 1));
            sum += dpp<0xB1>(sum); sum += dpp<0x4E>(sum); sum += dpp<0x128>(sum); sum = xsum(sum);
            const float mw_ = sum * (1.f / 128);
            float var = 0.f;
#pragma unroll
            for (int i = 0; i < 4; ++i) { const float d = x1[i] - mw_; var += d * d; }
            var += dpp<0xB1>(var); var += dpp<0x4E>(var); var += dpp<0x128>(var); var = xsum(var);
            if (lane == 0 || lane == 4) { sm[tk * 8 + w] = mw_; sm[tk * 8 + 4 + w] = var; }
            bar();
            const f32x4 ms = *(LAS f32x4*)(sm + tk * 8), vs = *(LAS f32x4*)(sm + tk * 8 + 4);
            const float mean = ((ms[0] + ms[1]) + (ms[2] + ms[3])) * 0.25f;
            const f32x4 dm = ms - mean;
            var = (((vs[0] + vs[1]) + (vs[2] + vs[3])) + 128.f * ((dm[0] * dm[0] + dm[1] * dm[1]) + (dm[2] * dm[2] + dm[3] * dm[3]))) * (1.f / D);
            const float mb = rb(mean), sd = __builtin_amdgcn_rcpf(rb(__builtin_amdgcn_sqrtf(rb(rb(var) + 1e-5f))));
            float hh2[4];
#pragma unroll
            for (int i = 0; i < 4; ++i) hh2[i] = lnmod(x1[i], mb, sd, cgc[i], cgb[i]);
            st8(rw, ((d0 >> 5) & 3) * 1024 + tok * 64 + (d0 & 31) * 2, (u32x2){pk(hh2[0], hh2[1]), pk(hh2[2], hh2[3])}, loc(WS_H2, (unsigned)((s * 2 + tt) * 16 + 4 * w) * 1024u, cfg, 0));
        }
        if (ROLE == 5) asm volatile("s_waitcnt vmcnt(8)" ::: "memory"); else drain();
        bar();
        if (ROLE != 2 && ROLE != 4) st4(rw, t0o, s + 1, oflag);
        if (doq) st4(rw, t0o, s + LEAD + 1, qflag);
        if (doq) qkv_in();
    };
    int s = 0;
    if (QD) for (; s < NSEG - LEAD; ++s) iter(s, bool_c<QD != 0>{});
    if (ROLE != 4) for (; s < NSEG; ++s) iter(s, bool_c<false>{});
}

__global__ void __launch_bounds__(256, 1) k_main(Params p) {
    extern __shared__ __attribute__((aligned(16))) char smem[];
    const lptr lds = (lptr)smem;
    const int b = blockIdx.x, tid = threadIdx.x;
    if (b < 128 && (b & 7) < 4) chain_role(p, lds, (b >> 3) * 4 + (b & 3), tid);
    else {
        const int u = (b >> 3) * 4 + (b & 3);
        const int j = (b & 4) ? (u < 64 ? u : u + 56) : (u < 120 ? u : u + 64);
        if (j < 64) stream_role<0>(p, lds, j, tid);
        else if (j < 120) stream_role<1>(p, lds, j, tid);
        else if (j < 152) stream_role<2>(p, lds, j, tid);
        else if (j < 168) stream_role<3>(p, lds, j, tid);
        else if (j < 184) stream_role<5>(p, lds, j, tid);
        else stream_role<4>(p, lds, j, tid);
    }
}

__global__ void __launch_bounds__(256) k_init(Params p) {
    __shared__ float red[4][64];
    const int tid = threadIdx.x, w = tid >> 6, lane = tid & 63;
    const unsigned bid = blockIdx.x - 256;
    if (bid < 96) {
        const int r16 = lane >> 2, cc = lane & 3, n0 = bid * 32 + cc * 8;
        u32x4 wv[8]; float s[8];
#pragma unroll
        for (int it = 0; it < 8; ++it) wv[it] = *(const u32x4*)(p.wmod + (size_t)(w * 128 + it * 16 + r16) * 3072 + n0);
#pragma unroll
        for (int it = 0; it < 8; ++it) { const float cv = bflo(p.c[w * 128 + it * 16 + r16]); s[it] = rb(cv / (1.f + __expf(-cv))); }
        float acc[8] = {};
#pragma unroll
        for (int it = 0; it < 8; ++it) {
            acc[0] += s[it] * bflo(wv[it].x); acc[1] += s[it] * bfhi(wv[it].x); acc[2] += s[it] * bflo(wv[it].y); acc[3] += s[it] * bfhi(wv[it].y);
            acc[4] += s[it] * bflo(wv[it].z); acc[5] += s[it] * bfhi(wv[it].z); acc[6] += s[it] * bflo(wv[it].w); acc[7] += s[it] * bfhi(wv[it].w);
        }
#pragma unroll
        for (int i = 0; i < 8; ++i) { acc[i] += dpp<0x124>(acc[i]); acc[i] += dpp<0x128>(acc[i]); acc[i] = xsum(acc[i]); }
        if (lane < 4) for (int i = 0; i < 8; ++i) red[w][cc * 8 + i] = acc[i];
        __syncthreads();
        if (tid < 32) ((float*)(p.ws + WS_MOD))[bid * 32 + tid] = rb((red[0][tid] + red[1][tid]) + (red[2][tid] + red[3][tid]));
    } else if (bid < 128 && (bid & 7) == 0) {
        const unsigned a = WS_HO + 240 * 16384 + ((blockIdx.x >> 3) & 1) * 8192 + w * 1024 + (blockIdx.x & 16) * 16;
        unsigned best = ~0u;
        unsigned* ap = (unsigned*)(p.ws + a);
        for (int rep = 0; rep < PREP; ++rep) {
            unsigned long long t0, t1; unsigned r = 0;
            asm volatile("s_memtime %0\n\ts_waitcnt lgkmcnt(0)" : "=s"(t0) :: "memory");
            if (lane == 0) r = __hip_atomic_fetch_add(ap, 1u, __ATOMIC_RELAXED, __HIP_MEMORY_SCOPE_AGENT);
            asm volatile("s_waitcnt vmcnt(0)\n\ts_memtime %0\n\ts_waitcnt lgkmcnt(0)" : "=s"(t1), "+v"(r) :: "memory");
            best = min(best, (unsigned)(t1 - t0));
        }
        if (lane == 0) red[w][0] = (float)best;
        __syncthreads();
        if (tid < 2) ((float*)(p.ws + WS_CFG))[((blockIdx.x >> 3) & 1) * 4 + ((blockIdx.x >> 4) & 1) * 2 + tid] = fminf(red[tid][0], red[2 + tid][0]);
    } else if (blockIdx.x < 256) {
        const int row = blockIdx.x * 8 + w * 2;
        u32x4 xv[2] = {*(const u32x4*)(p.x + (size_t)row * D + lane * 8), *(const u32x4*)(p.x + (size_t)(row + 1) * D + lane * 8)};
#pragma unroll
        for (int q = 0; q < 2; ++q) {
            float v[8] = {bflo(xv[q].x), bfhi(xv[q].x), bflo(xv[q].y), bfhi(xv[q].y), bflo(xv[q].z), bfhi(xv[q].z), bflo(xv[q].w), bfhi(xv[q].w)};
            float sum = ((v[0] + v[1]) + (v[2] + v[3])) + ((v[4] + v[5]) + (v[6] + v[7]));
            sum += dpp<0xB1>(sum); sum += dpp<0x4E>(sum); sum += dpp<0x124>(sum); sum += dpp<0x128>(sum); sum = xsum(sum);
            const float mean = sum * (1.f / D); float var = 0.f;
#pragma unroll
            for (int i = 0; i < 8; ++i) { const float d = v[i] - mean; var += d * d; }
            var += dpp<0xB1>(var); var += dpp<0x4E>(var); var += dpp<0x124>(var); var += dpp<0x128>(var); var = xsum(var);
            const float mb = rb(mean), isd = 1.f / rb(sqrtf(rb(rb(var * (1.f / D)) + 1e-5f)));
#pragma unroll
            for (int i = 0; i < 8; ++i) v[i] = rb(v[i] - mb) * isd;
            *(u32x4*)(p.ws + WS_N + (size_t)(row + q) * 1024 + lane * 16) = (u32x4){pk(v[0], v[1]), pk(v[2], v[3]), pk(v[4], v[5]), pk(v[6], v[7])};
        }
    } else if (bid >= 128) {
        const unsigned bx = bid - 128;
        if ((bx & 7) < 4 && (bx >> 6) < PRE) {
            const unsigned hd = (bx & 3) + 4 * ((bx >> 3) & 7), part = bx >> 6;
            const u16* W = (part < 2 ? p.Wq : p.Wk) + (size_t)((part & 1) * 256 + (tid >> 4)) * HD + hd * 128 + (tid & 15) * 8;
            unsigned acc = 0;
#pragma unroll
            for (int i = 0; i < 16; ++i) { const u32x4 v = *(const u32x4*)(W + (size_t)i * 16 * HD); acc ^= v.x ^ v.y ^ v.z ^ v.w; }
            asm volatile("" :: "v"(acc));
        }
    } else {
        const unsigned t = (bid - 97 - ((bid - 96) >> 3)) * 256 + tid, nt = 28 * 256;
        const u32x4 zero = {0, 0, 0, 0};
        for (unsigned i = t; i < FL_N * 16; i += nt) ((u32x4*)(p.ws + WS_FLAG))[i] = zero;
        u32x4* ho = (u32x4*)(p.ws + WS_HO + HO_MEM * 16384);
        for (unsigned i = t; i < MRS * 1024; i += nt) {
            u32x4 v = zero;
            const unsigned m = i & 511;
            if (i < NC * 1024) {
                const unsigned* mp = (const unsigned*)(p.mem0 + (2 * ((m >> 2) & 3) + (m & 1)) * D + 16 * (m >> 4) + (m & 2));
                v = (u32x4){min(mp[0], SENT - 1), min(mp[2], SENT - 1), min(mp[4], SENT - 1), min(mp[6], SENT - 1)};
            }
            ho[i] = v;
        }
    }
}

extern "C" void kernel_launch(void* const* d_in, const int* in_sizes, int n_in, void* d_out, int out_size, void* d_ws, size_t ws_size, hipStream_t stream) {
    Params p;
    p.x = (const u16*)d_in[0]; p.mem0 = (const u16*)d_in[1]; p.Wq = (const u16*)d_in[2]; p.Wk = (const u16*)d_in[3]; p.Wv = (const u16*)d_in[4]; p.Wo = (const u16*)d_in[5];
    p.wg = (const u16*)d_in[8]; p.wu = (const u16*)d_in[9]; p.wd = (const u16*)d_in[10]; p.c = (const u16*)d_in[11]; p.wmod = (const u16*)d_in[12];
    p.out = (u16*)d_out; p.ws = (char*)d_ws + ((0x200000 - ((uintptr_t)d_ws & 0x1FFFFF)) & 0x1FFFFF);
    constexpr int LDSB = 140 * 1024;
    (void)hipFuncSetAttribute((const void*)k_main, hipFuncAttributeMaxDynamicSharedMemorySize, LDSB);
    k_init<<<384 + 64 * PRE, 256, 0, stream>>>(p);
    k_main<<<256, 256, LDSB, stream>>>(p);
}
```

```cpp
#include <hip/hip_runtime.h>
#include <hip/hip_bf16.h>
#include <stdint.h>

typedef unsigned short u16;
typedef __bf16 bf16x2 __attribute__((ext_vector_type(2)));
typedef __bf16 bf16x8 __attribute__((ext_vector_type(8)));
typedef short s16x4 __attribute__((ext_vector_type(4)));
typedef float f32x4 __attribute__((ext_vector_type(4)));
typedef float f32x2 __attribute__((ext_vector_type(2)));
typedef unsigned u32x2 __attribute__((ext_vector_type(2)));
typedef unsigned u32x4 __attribute__((ext_vector_type(4)));
typedef unsigned u32x8 __attribute__((ext_vector_type(8)));
#define LAS __attribute__((address_space(3)))
typedef LAS char* lptr;
#define DI __device__ __forceinline__

#define TX 56
#define TY 44
#define TV1 8
#define TV2 8
#define TPEN 20
#define TM 10
#define SIDE 0
#define SLP 5
#define PREP 5
#define FDLY 0
#define CX 2150
#define CY 430
#define MUP 2
#define PSL 1
#define TS_ 6
#ifndef NVX
#define NVX 2
#endif
#define FW ""
#define PRE 2
#define PDLY 3
#define NPRE 4
#define LEAD 16

constexpr int S = 2048, D = 512, NSEG = 64, F = 1792, HD = 4096;
constexpr unsigned SENT = 0xFFFFFFFFu;
constexpr int NRING = 3;
constexpr unsigned WS_MOD = 0;
constexpr unsigned WS_FLAG = 131072;
constexpr unsigned FL_ARM = 64, FL_QKV = 128, FL_L = 320, FL_KM = 384, FL_B = 448, FL_C = 512, FL_A = 576, FL_G = 600, FL_N = 640;
constexpr unsigned WS_EPOCH = 12544;
constexpr unsigned WS_EARLY = 16384, EF_PROBE = 0, EF_MOD = 16, EF_LN = 64, EF_MOD2 = 128;
#define NC 4
constexpr unsigned WS_HO = 220u << 20, HO_MEM = 256, MRS = NC + 1, HO_N = HO_MEM + 3 * MRS;
constexpr unsigned WS_N = 1u << 20, WS_DBG = 3u << 20;
constexpr unsigned WS_H1 = 4u << 20, WS_X1 = 6u << 20, WS_H2 = 10u << 20, WS_G = 14u << 20, WS_O = 28u << 20, WS_QKV = 60u << 20, WS_PB = 156u << 20, WS_END = 246u << 20;

DI float bflo(unsigned x) { return __uint_as_float(x << 16); }
DI float bfhi(unsigned x) { return __uint_as_float(x & 0xffff0000u); }
DI unsigned pk(float lo, float hi) { bf16x2 v = {(__bf16)lo, (__bf16)hi}; return __builtin_bit_cast(unsigned, v); }
DI float rb(float x) { return (float)(__bf16)x; }
DI void bar() { asm volatile("s_waitcnt lgkmcnt(0)\n\ts_barrier" ::: "memory"); }
DI void drain() { asm volatile("s_waitcnt vmcnt(0)" ::: "memory"); }
DI u32x2 trread(lptr p) { return __builtin_bit_cast(u32x2, __builtin_amdgcn_ds_read_tr16_b64_v4i16((LAS s16x4*)p)); }
DI u32x4 tr2(lptr p, int off) { const u32x2 a = trread(p), b = trread(p + off); return (u32x4){a.x, a.y, b.x, b.y}; }
DI u32x4 ldsr128(lptr p) { return *(LAS u32x4*)p; }
DI void ldsw128(lptr p, u32x4 v) { *(LAS u32x4*)p = v; }
DI void ldsw64(lptr p, u32x2 v) { *(LAS u32x2*)p = v; }
DI void ldsw32(lptr p, unsigned v) { *(LAS unsigned*)p = v; }
DI f32x4 mfma32(u32x4 a, u32x4 b, f32x4 c) { return __builtin_amdgcn_mfma_f32_16x16x32_bf16(__builtin_bit_cast(bf16x8, a), __builtin_bit_cast(bf16x8, b), c, 0, 0, 0); }
DI float swadd32(float a, float b) {
    auto r = __builtin_amdgcn_permlane32_swap(__float_as_uint(a), __float_as_uint(b), false, false);
    return __uint_as_float(r[0]) + __uint_as_float(r[1]);
}
DI float vmax(float a, float b) { float r; asm("v_max_f32 %0, %1, %2" : "=v"(r) : "v"(a), "v"(b)); return r; }
DI float vmax3(float a, float b, float c) { float r; asm("v_max3_f32 %0, %1, %2, %3" : "=v"(r) : "v"(a), "v"(b), "v"(c)); return r; }
DI float xmax(float x) {
    auto s = __builtin_amdgcn_permlane16_swap(__float_as_uint(x), __float_as_uint(x), false, false);
    x = vmax(__uint_as_float(s[0]), __uint_as_float(s[1]));
    auto t = __builtin_amdgcn_permlane32_swap(__float_as_uint(x), __float_as_uint(x), false, false);
    return vmax(__uint_as_float(t[0]), __uint_as_float(t[1]));
}
DI float xsum(float x) {
    auto s = __builtin_amdgcn_permlane16_swap(__float_as_uint(x), __float_as_uint(x), false, false);
    x = __uint_as_float(s[0]) + __uint_as_float(s[1]);
    auto t = __builtin_amdgcn_permlane32_swap(__float_as_uint(x), __float_as_uint(x), false, false);
    return __uint_as_float(t[0]) + __uint_as_float(t[1]);
}
template <int CTRL> DI float dpp(float x) { return __builtin_bit_cast(float, __builtin_amdgcn_mov_dpp(__builtin_bit_cast(int, x), CTRL, 0xf, 0xf, true)); }
template <int CTRL> DI int dppi(int x) { return __builtin_amdgcn_mov_dpp(x, CTRL, 0xf, 0xf, true); }
DI unsigned mx4(u32x4 v, unsigned m) { return max(max(max(v.x, v.y), max(v.z, v.w)), m); }
typedef __amdgpu_buffer_rsrc_t rsrc_t;
DI rsrc_t mkrs(const void* p, unsigned bytes) { return __builtin_amdgcn_make_buffer_rsrc((void*)p, 0, bytes, 0x00020000); }
DI u32x4 ld16(rsrc_t r, unsigned off, unsigned soff = 0) { return __builtin_amdgcn_raw_buffer_load_b128(r, off, soff, 16); }
DI u32x2 ld8(rsrc_t r, unsigned off, unsigned soff) { return __builtin_amdgcn_raw_buffer_load_b64(r, off, soff, 16); }
DI unsigned ld4(rsrc_t r, unsigned off, unsigned soff = 0) { return __builtin_amdgcn_raw_buffer_load_b32(r, off, soff, 16); }
DI void st16(rsrc_t r, unsigned off, u32x4 v, unsigned soff = 0) { __builtin_amdgcn_raw_buffer_store_b128(v, r, off, soff, 16); }
DI void st8(rsrc_t r, unsigned off, u32x2 v, unsigned soff) { __builtin_amdgcn_raw_buffer_store_b64(v, r, off, soff, 16); }
DI void st4(rsrc_t r, unsigned off, unsigned v, unsigned soff = 0) { __builtin_amdgcn_raw_buffer_store_b32(v, r, off, soff, 16); }
DI u32x4 ldw(rsrc_t r, unsigned voff, unsigned soff) { return __builtin_amdgcn_raw_buffer_load_b128(r, voff, soff, 0); }
DI unsigned rt() { return (unsigned)__builtin_amdgcn_s_memrealtime(); }
DI void wait_rt(unsigned t, unsigned q = 1) {
    int d;
    do d = (int)(t - rt()); while (d >= (int)q && d < 4096);
    d = min(d, 8);
    for (; d > 0; --d) asm volatile("");
    asm volatile("" ::: "memory");
}

template <int V> struct int_c { static constexpr int value = V; };
template <bool V> struct bool_c { static constexpr bool value = V; };
DI unsigned loc(unsigned base, unsigned off, unsigned cfg, unsigned far) { const unsigned x = base + ((off >> 13) << 14) + (off & 8191); return x + ((((cfg >> ((x >> 21) & 1)) ^ far) & 1) << 13); }
DI unsigned qkv_off(unsigned i, unsigned sec, unsigned cfg) { return loc(WS_QKV, (i * 3 + sec) * 8192u, cfg, 1); }
DI unsigned flg(unsigned line, unsigned cfg) { return loc(WS_FLAG, line * 128, cfg, line >= FL_KM); }
DI unsigned kvm_off(unsigned ho, unsigned cfg, unsigned i) { return ho + (8u << 20) + (i >> 1) * 16384 + ((cfg >> ((i >> 8) & 1)) & 1) * 8192 + (i & 1) * 4096; }
struct Tag { unsigned e, t; };
DI Tag get_tag(const char* ws) { const unsigned e = *(const __attribute__((address_space(4))) unsigned*)(uintptr_t)(ws + WS_EPOCH) + 1; return Tag{e, e * 0x9E3779B1u}; }
DI unsigned dec(u32x2 v, Tag tg) { const unsigned c = v.x - tg.t; return (v.y == tg.e && c < 0x100000u) ? c : 0u; }
DI unsigned ldf(rsrc_t r, unsigned off, Tag tg) { return dec(ld8(r, off, 0), tg); }
DI void stf(rsrc_t r, unsigned voff, unsigned v, unsigned soff, Tag tg) { st8(r, voff, (u32x2){v + tg.t, tg.e}, soff); }
struct Params {
    const u16 *x, *mem0, *Wq, *Wk, *Wv, *Wo, *wg, *wu, *wd, *c, *wmod;
    u16* out; char* ws;
};

DI float lnmod(float v, float mb, float isd, float t, float sh) { return rb(rb(rb(v - mb) * isd) * t) + sh; }

DI unsigned wait_words(rsrc_t rw, Tag tg, unsigned off, unsigned need, unsigned& cap) {
    unsigned v;
    for (unsigned spins = 0;; ++spins) {
        asm volatile("" ::: "memory");
        v = ldf(rw, off, tg);
        if (__all(v >= need)) break;
        if (spins > cap) { cap = 0; break; }
        __builtin_amdgcn_s_sleep(2);
    }
    asm volatile("" ::: "memory");
    return v;
}
DI unsigned get_cfg(rsrc_t rw, Tag tg, int lane, unsigned& cap) {
    int v = wait_words(rw, tg, WS_EARLY + (EF_PROBE + (lane & 15)) * 128, 1, cap);
    v = min(v, dppi<0x4E>(v)); v = min(v, dppi<0x124>(v)); v = min(v, dppi<0x128>(v));
    return __builtin_amdgcn_readlane(v, 1) < __builtin_amdgcn_readlane(v, 0) ? 1u : 2u;
}

constexpr int PSTR = 272;
DI void panel_issue(const u16* srcA, const u16* srcB, int ld, int tid, u32x4 (&v)[32]) {
    const rsrc_t ra = mkrs(srcA, (511 * ld + 64) * 2), rb_ = mkrs(srcB, (511 * ld + 64) * 2);
    const unsigned voff = ((tid >> 3) * ld + (tid & 7) * 8) * 2;
#pragma unroll
    for (int i = 0; i < 16; ++i) { v[2 * i] = ldw(ra, voff, i * 32 * ld * 2); v[2 * i + 1] = ldw(rb_, voff, i * 32 * ld * 2); }
}
DI void panel_finish(lptr lds, int tid, int colA, int colB, u32x4 (&v)[32], u32x4 (&wa)[16], u32x4 (&wb)[16]) {
    const int lane = tid & 63, g = lane >> 4, q = (lane & 15) >> 2;
    const lptr wp = lds + (tid >> 3) * PSTR + (tid & 7) * 16;
#pragma unroll
    for (int half = 0; half < 2; ++half) {
        bar();
#pragma unroll
        for (int i = 0; i < 8; ++i) { ldsw128(wp + i * 32 * PSTR, v[16 * half + 2 * i]); ldsw128(wp + i * 32 * PSTR + 128, v[16 * half + 2 * i + 1]); }
        bar();
        lptr pa = lds + (8 * g + q) * PSTR + colA * 2, pb = lds + (8 * g + q) * PSTR + colB * 2;
#pragma unroll
        for (int ks = 0; ks < 8; ++ks) { wa[half * 8 + ks] = tr2(pa + ks * 32 * PSTR, 4 * PSTR); wb[half * 8 + ks] = tr2(pb + ks * 32 * PSTR, 4 * PSTR); asm volatile("" : "+a"(wa[half * 8 + ks]), "+a"(wb[half * 8 + ks])); }
    }
    bar();
}
constexpr int L_A = 0, L_Q = 73728;
#define MFWA(acc, wgt, x) asm volatile("v_mfma_f32_16x16x32_bf16 %0, %1, %2, %0" : "+v"(acc) : "a"(wgt), "v"(x))
#define MFWB(acc, wgt, x) asm volatile("v_mfma_f32_16x16x32_bf16 %0, %2, %1, %0" : "+v"(acc) : "a"(wgt), "v"(x))
template <bool SWAP> DI void tile_mma(lptr lds, int lane, const u32x4 (&wa)[16], const u32x4 (&wb)[16], f32x4& ca, f32x4& cb) {
    ca = (f32x4){0, 0, 0, 0}; cb = ca;
    u32x4 a[16];
#pragma unroll
    for (int ks = 0; ks < 4; ++ks) a[ks] = ldsr128(lds + ks * 1024 + lane * 16);
    asm volatile("s_nop 1" : "+v"(ca), "+v"(cb));
    __builtin_amdgcn_sched_barrier(0);
#pragma unroll
    for (int ks = 0; ks < 16; ++ks) {
        if (SWAP) MFWB(ca, wa[ks], a[ks]); else MFWA(ca, wa[ks], a[ks]);
        if (ks + 4 < 16) a[ks + 4] = ldsr128(lds + (ks + 4) * 1024 + lane * 16);
        __builtin_amdgcn_sched_barrier(0);
        if (SWAP) MFWB(cb, wb[ks], a[ks]); else MFWA(cb, wb[ks], a[ks]);
    }
    asm volatile("s_nop 7" : "+v"(ca), "+v"(cb));
}

constexpr int CSTR = 656, OSTR = 1040;
constexpr int L_MEMF = 0, L_QT = 8192, L_KT = 12288, L_VT = 16384, L_RED = 20480, L_TS = 21504, L_SEL = 23552, L_ONES = 24576, L_QKV = 25600;
constexpr float SC_L2E = 0.08838834764831845f * 1.4426950408889634f;
constexpr unsigned OOB = 0x80000000u;
#define SMFA(acc, a, w) asm volatile("v_smfmac_f32_16x16x64_bf16 %0, %1, %2, %3" : "+v"(acc) : "v"(a), "a"(w), "v"(sidx))
#define SMFV(acc, a, w) asm volatile("v_smfmac_f32_16x16x64_bf16 %0, %1, %2, %3" : "+v"(acc) : "v"(a), "v"(w), "v"(sidx))
#define MF0(acc, a, b) asm volatile("v_mfma_f32_16x16x32_bf16 %0, %1, %2, 0" : "=&v"(acc) : "v"(a), "v"(b))
#define MF(acc, a, b) asm volatile("v_mfma_f32_16x16x32_bf16 %0, %1, %2, %0" : "+v"(acc) : "v"(a), "v"(b))
#define NOP2() asm volatile("s_nop 1")

#define SB() __builtin_amdgcn_sched_barrier(0)
DI f32x4 zero4() { f32x2 a, b; asm volatile("v_mov_b64 %0, 0\n\tv_mov_b64 %1, 0" : "=v"(a), "=v"(b)); return (f32x4){a.x, a.y, b.x, b.y}; }
DI u32x4 sent4() { u32x2 a, b; asm volatile("v_mov_b64 %0, -1\n\tv_mov_b64 %1, -1" : "=v"(a), "=v"(b)); return (u32x4){a.x, a.y, b.x, b.y}; }
DI float vadd(float a, float b) { float r; asm volatile("v_add_f32 %0, %1, %2" : "=v"(r) : "v"(a), "v"(b)); return r; }
DI unsigned vcvt(float a, float b) { unsigned r; asm volatile("v_cvt_pk_bf16_f32 %0, %1, %2" : "=v"(r) : "v"(a), "v"(b)); return r; }
template <int O0, int O1> DI void dsw2(lptr p, unsigned a, unsigned b) { asm volatile("ds_write2_b32 %0, %1, %2 offset0:%3 offset1:%4" :: "v"(p), "v"(a), "v"(b), "i"(O0), "i"(O1) : "memory"); }
#define ST(k)
#define ST2(k)
#define TS(k)
#define SU(id, k)
DI int wave_max_rel(unsigned v, unsigned base) {
    int d = (int)(v - base);
    d = max(d, dppi<0xB1>(d)); d = max(d, dppi<0x4E>(d)); d = max(d, dppi<0x124>(d)); d = max(d, dppi<0x128>(d));
    auto s = __builtin_amdgcn_permlane16_swap((unsigned)d, (unsigned)d, false, false); d = max((int)s[0], (int)s[1]);
    auto t = __builtin_amdgcn_permlane32_swap((unsigned)d, (unsigned)d, false, false); d = max((int)t[0], (int)t[1]);
    return __builtin_amdgcn_readfirstlane(d);
}

DI int lane_id() { int l; asm volatile("v_mbcnt_lo_u32_b32 %0, -1, 0\n\tv_mbcnt_hi_u32_b32 %0, -1, %0" : "=v"(l)); return l; }
DI void chain_role(const Params& p, lptr lds, const int b, const int tid0) {
    const int w = __builtin_amdgcn_readfirstlane(tid0 >> 6);
    const int h = (b & 3) + 4 * (b >> 3), hf = (b >> 2) & 1;
    u32x8 wq[2][8], wk[2][8], wv[8], wo[8];
    u32x4 kv0[3];
    unsigned quant;
    const Tag tg = get_tag(p.ws);
    unsigned cap = 1u << 17, cfg, ho0, ho1;
    constexpr unsigned ho = WS_HO;
    auto pblk = [&](unsigned ring_, unsigned r) { return (ring_ == 2 ? ho1 : ho0) + (ring_ * 64 + r) * 16384; };
    {
        const int tid = tid0;
        constexpr int WSTR = 272;
        u32x4 v[3][16];
        const unsigned vo = ((tid >> 3) * HD + (tid & 7) * 8) * 2;
        auto issue = [&](int r, u32x4 (&d)[16]) {
            if (r < 5) {
                const rsrc_t rs = mkrs((r < 2 ? p.Wq : r < 4 ? p.Wk : p.Wv) + h * 128 + (r < 4 ? (r & 1) : hf) * 64, (511 * HD + 64) * 2);
#pragma unroll
                for (int i = 0; i < 16; ++i) d[i] = ldw(rs, vo, i * 32 * HD * 2);
            } else {
                const rsrc_t rs = mkrs(p.Wo + (size_t)(h * 128 + hf * 64) * D, 65536);
#pragma unroll
                for (int i = 0; i < 16; ++i) d[i] = ldw(rs, lane_id() * 16, w * 1024 + i * 4096);
            } };
        unsigned m0w;
        { const int c = 8 * (b & 1) + ((tid >> 2) & 7); m0w = *(const unsigned*)(p.mem0 + (2 * (c >> 2) + (c & 1)) * D + 16 * (b >> 1) + 4 * (tid & 3) + (c & 2)); }
        issue(0, v[0]); issue(1, v[1]); issue(2, v[2]);
        SB();
        { const unsigned a = rt(); unsigned c, e; do c = rt(); while (c == a); do e = rt(); while (e == c); quant = min(e - c, 8u); }
        const int lane = lane_id();
        const lptr wp = lds + (w * 8 + (lane >> 3)) * WSTR + (lane & 7) * 16;
        const int g = lane >> 4, q = (lane & 15) >> 2, pp = lane & 3;
        auto stage = [&](u32x4 (&d)[16], int half) {
#pragma unroll
            for (int i = 0; i < 16; ++i) ldsw128(wp + i * 32 * WSTR + half * 128, d[i]);
        };
        auto frag = [&](int j, int colbyte) { u32x8 f;
            const lptr base = lds + (((2 * w + j) & 7) * 64 + 8 * g + q) * WSTR + colbyte;
#pragma unroll
            for (int m = 0; m < 4; ++m) { const u32x2 t = trread(base + (32 * (m >> 1) + 4 * (m & 1)) * WSTR); f[2 * m] = t.x; f[2 * m + 1] = t.y; }
            return f; };
        auto take = [&](u32x8 (&dst)[2][8]) {
#pragma unroll
            for (int j = 0; j < 8; ++j) { dst[0][j] = frag(j, w * 64 + 8 * pp); dst[1][j] = frag(j, w * 64 + 32 + 8 * pp); asm volatile("" : "+a"(dst[0][j]), "+a"(dst[1][j])); } };
        stage(v[0], 0); issue(3, v[0]); stage(v[1], 1); issue(4, v[1]); bar(); take(wq); bar();
        {
            const rsrc_t rw = mkrs(p.ws, WS_END);
            cfg = get_cfg(rw, tg, lane, cap);
            ho0 = ho + (cfg & 1) * 8192; ho1 = ho + ((cfg >> 1) & 1) * 8192;
            const u32x4 z = sent4();
#pragma unroll
            for (int r = 0; r < 3; ++r) { st16(rw, lane * 16, z, pblk(r, b) + w * 2048); st16(rw, lane * 16, z, pblk(r, b) + w * 2048 + 1024); }
            const unsigned mso = w < 2 ? (2 * w + (lane >> 5)) * 16384 + (lane & 31) * 4 : b == 0 ? NC * 16384 + (w - 2) * 256 + lane * 4 : OOB;
            st4(rw, mso, w < 2 ? min(m0w, SENT - 1) : 0u, ho0 + HO_MEM * 16384 + b * 128);
            st4(rw, mso, SENT, ho0 + (HO_MEM + MRS) * 16384 + b * 128); st4(rw, mso, SENT, ho0 + (HO_MEM + 2 * MRS) * 16384 + b * 128);
            SB();
        }
        stage(v[2], 0); issue(5, v[2]); stage(v[0], 1);
        asm volatile("s_waitcnt vmcnt(16)" ::: "memory");
        bar();
        stf(mkrs(p.ws, WS_END), lane + w == 0 ? 0u : OOB, 1, flg(FL_ARM + b, cfg), tg);
        take(wk);
        {
            const rsrc_t rw = mkrs(p.ws, WS_END);
            const unsigned fa = flg(FL_ARM + lane, cfg), fq = flg(FL_QKV + ((lane & 2) ? 64 + (h & 3) + 8 * (h >> 2) + 4 * (lane & 1) : 32 + h), cfg);
            for (unsigned spins = 0;; ++spins) {
                asm volatile("" ::: "memory");
                const u32x2 a = ld8(rw, fa, 0), c = ld8(rw, fq, 0);
                if (__all(min(dec(a, tg), dec(c, tg)) >= 1)) break;
                if (spins > cap) { cap = 0; break; }
                __builtin_amdgcn_s_sleep(1);
            }
            asm volatile("" ::: "memory");
            const unsigned kb_ = qkv_off(h, 1, cfg) + w * 2048;
            kv0[0] = ld16(rw, lane * 16, kb_); kv0[1] = ld16(rw, lane * 16, kb_ + 1024); kv0[2] = ld16(rw, lane * 16, qkv_off(h, 2, cfg) + hf * 4096 + w * 1024);
            SB();
        }
        bar();
        constexpr int VSTR = 144, L_WO = 512 * VSTR;
#pragma unroll
        for (int i = 0; i < 16; ++i) { ldsw128(lds + (w * 8 + (lane >> 3) + i * 32) * VSTR + (lane & 7) * 16, v[1][i]); ldsw128(lds + L_WO + (w + 4 * i) * OSTR + lane * 16, v[2][i]); }
        bar();
#pragma unroll
        for (int j = 0; j < 8; ++j) {
            const lptr vb_ = lds + (((2 * w + j) & 7) * 64 + 8 * g + q) * VSTR + (32 * (w >> 1) + 8 * pp + 4 * (w & 1)) * 2;
#pragma unroll
            for (int m = 0; m < 4; ++m) { const u32x2 t = trread(vb_ + (32 * (m >> 1) + 4 * (m & 1)) * VSTR); wv[j][2 * m] = t.x; wv[j][2 * m + 1] = t.y; }
        }
        lptr base = lds + L_WO + (32 * (q >> 1) + 4 * g + (q & 1)) * OSTR;
#pragma unroll
        for (int t = 0; t < 8; ++t) {
            const int col = 128 * w + 64 * (t >> 2) + 16 * pp + 4 * (t & 3);
#pragma unroll
            for (int m = 0; m < 4; ++m) { const u32x2 tv = trread(base + (16 * (m >> 1) + 2 * (m & 1)) * OSTR + col * 2); wo[t][2 * m] = tv.x; wo[t][2 * m + 1] = tv.y; }
        }
        bar();
    }
    const int lane = lane_id(), tid = w * 64 + lane, n = lane & 15, g = lane >> 4;
    const rsrc_t rw = mkrs(p.ws, WS_END);
    const int hi = (n >> 1) & 1, rowi = 2 * (n >> 2) + (n & 1);
    const int sidx = hi ? 0xEEEE : 0x4444;
    const unsigned l16 = lane * 16;
    const unsigned xc = (b & 3) + 4 * ((b >> 2) % (NC / 4));
    const unsigned t0o = tid == 0 ? 0u : OOB;
    auto qkv_issue = [&](int s, u32x4 (&v)[3]) {
        const unsigned kb_ = qkv_off(s * 32 + h, 1, cfg) + w * 2048;
        v[0] = ld16(rw, l16, kb_); v[1] = ld16(rw, l16, kb_ + 1024);
        v[2] = ld16(rw, l16, qkv_off(s * 32 + h, 2, cfg) + hf * 4096 + w * 1024);
    };
    auto qkv_store = [&](int s, u32x4 (&v)[3]) {
        lptr d = lds + L_QKV + (s & 1) * 12288 + lane * 16;
        ldsw128(d + w * 2048, v[0]); ldsw128(d + w * 2048 + 1024, v[1]);
        ldsw64(d + 8192 + (w >> 1) * 2048 + (w & 1) * 8, (u32x2){v[2].x, v[2].y}); ldsw64(d + 8192 + (w >> 1) * 2048 + 1024 + (w & 1) * 8, (u32x2){v[2].z, v[2].w});
    };
    const unsigned qflag = flg(FL_QKV + ((lane & 2) ? 64 + (h & 3) + 8 * (h >> 2) + 4 * (lane & 1) : 32 + h), cfg), kflag = __builtin_amdgcn_readfirstlane(flg(FL_KM + b, cfg)), gflag = __builtin_amdgcn_readfirstlane(flg(FL_G, cfg));
    auto qkv_wait = [&](unsigned need) { for (unsigned spins = 0;; ++spins) { asm volatile("" ::: "memory"); if (__all(ldf(rw, qflag, tg) >= need)) break; if (spins > cap) { cap = 0; break; } __builtin_amdgcn_s_sleep(1); } asm volatile("" ::: "memory"); };
    SU(b, 0);
    qkv_store(0, kv0);
    const lptr qtw = lds + L_QT + w * 1024 + (n >> 3) * 256 + (((n & 7) * 32) ^ ((n >> 3) * 128)) + g * 8;
    const lptr tr0 = lds + L_QT + g * 256 + (n >> 2) * 32 + (lane & 3) * 8 + (g & 1) * 128, tr1 = tr0 + ((g & 1) ? -128 : 128);
    auto trf = [&](int off) { const u32x2 a = trread(tr0 + off), c = trread(tr1 + off); return (u32x4){a.x, a.y, c.x, c.y}; };
    const lptr mw = lds + L_MEMF + tid * 16 + w * 1024, tsw = lds + L_TS + tid * 4, vtw = lds + L_VT + tid * 4, vtr = lds + L_VT + lane * 4;
    const lptr rdw = lds + L_RED + (w * 64 + 32 * ((n >> 2) & 1) + 16 * ((n >> 1) & 1) + (n & 1) + 4 * (g & 1)) * 4, rdr = lds + L_RED + (lane & 31) * 8;
    const unsigned tso = w == 3 ? l16 : OOB, sco = w == 3 ? (lane & 1) * 4 : OOB, mso = w < 2 ? (2 * w + (lane >> 5)) * 16384 + (lane & 31) * 4 : b == 0 ? NC * 16384 + (w - 2) * 256 + lane * 4 : OOB,
         kso = (hf | hi) ? OOB : (rowi * 4 + g) * 16, pvo = (2 * (n >> 2) + (g >> 1)) * 16384 + ((n & 3) + 4 * (g & 1)) * 16;
    if (w == 1) ldsw128(lds + L_ONES + lane * 16, (u32x4){0x3f803f80u, 0x3f803f80u, 0x3f803f80u, 0x3f803f80u});
    if (w == 0) { const unsigned one = 0x3f80u << (16 * (n & 1)), k2 = (n >> 1) & 3; ldsw128(lds + L_SEL + lane * 16, (u32x4){k2 == 0 ? one : 0u, k2 == 1 ? one : 0u, k2 == 2 ? one : 0u, k2 == 3 ? one : 0u}); }
    if (tid == 0) *(LAS unsigned*)(lds + L_TS + 1024) = 0;
    const unsigned long long himask = 0xCCCCCCCCCCCCCCCCull;
    bar();
    auto look_at = [&](int sg) { const unsigned o = qkv_off(sg * 32 + h, w == 0 ? 1 : 2, cfg) + (w == 0 ? 0 : hf * 4096); return w >= 2 ? OOB : (w == 1 && lane >= 32) ? qflag : o + l16 * 8; };
    unsigned tvo = look_at(2);
    unsigned pold = pblk(2, b) + w * 2048 + 1024;
    unsigned T2 = rt(), T1 = 0, ring = 0;
    unsigned pc = (unsigned)__builtin_amdgcn_s_memtime(), pt = T2, X = TX + CX / 24, Y = TY + CY / 24, mg = 0;
    auto step = [&](const int s, auto last_) {
        constexpr bool LAST = decltype(last_)::value;
        f32x4 aq0, aq1, ak0, ak1, av;
        const unsigned rnext = ring == NRING - 1 ? 0 : ring + 1;
        unsigned mo = ho0 + (HO_MEM + ring * MRS + xc) * 16384 + w * 2048, mo1 = mo + 1024, to = ho0 + (HO_MEM + ring * MRS + NC) * 16384 + xc * 128;
        asm volatile("" : "+s"(mo), "+s"(mo1), "+s"(to));
        u32x4 A[8], kx[2][4], qf[4], kf[4], vx[4], ones; unsigned tsv = 0;
        wait_rt(T2, quant);
        ST(0);
        A[0] = ld16(rw, l16, mo); A[1] = ld16(rw, l16, mo1); tsv = ld4(rw, sco, to);
        SB();
        SB();
        lptr qb = lds + L_QKV + (s & 1) * 12288 + lane * 16;
        lptr ar0 = lds + L_MEMF + lane * 16 + ((2 * w + 2) & 7) * 1024, ar1 = lds + L_MEMF + lane * 16 + ((2 * w + 4) & 7) * 1024, ar2 = lds + L_MEMF + lane * 16 + ((2 * w + 6) & 7) * 1024;
        asm volatile("" : "+v"(qb), "+v"(ar0), "+v"(ar1), "+v"(ar2));
        if constexpr (!LAST) {
#pragma unroll
            for (int ks = 0; ks < NPRE; ++ks) { kx[0][ks] = ldsr128(qb + ks * 1024); kx[1][ks] = ldsr128(qb + 4096 + ks * 1024); }
#pragma unroll
            for (int T = 0; T < NVX; ++T) vx[T] = ldsr128(qb + 8192 + T * 1024);
        }
        aq0 = zero4(); aq1 = zero4(); ak0 = zero4(); ak1 = zero4(); av = zero4();
        if constexpr (!LAST) {
#pragma unroll
            for (int ks = 0; ks < NPRE; ++ks) asm volatile("" : "+v"(kx[0][ks]), "+v"(kx[1][ks]));
#pragma unroll
            for (int T = 0; T < NVX; ++T) asm volatile("" : "+v"(vx[T]));
        }
        SB();
        ldsw128(mw, A[0]); ldsw128(mw + 1024, A[1]); ldsw32(tsw, tsv);
        SB();
        if (__builtin_expect(!__all(mx4(A[0], mx4(A[1], tsv)) != SENT), 0)) {
            for (unsigned spins = 0;; ++spins) {
                asm volatile("" ::: "memory");
                A[0] = ld16(rw, l16, mo); A[1] = ld16(rw, l16, mo1); tsv = ld4(rw, sco, to);
                if (__all(mx4(A[0], mx4(A[1], tsv)) != SENT)) break;
                if (spins > cap) { cap = 0; break; }
            }
            ldsw128(mw, A[0]); ldsw128(mw + 1024, A[1]); ldsw32(tsw, tsv);
        }
        ST(1);
        TS(0);
        NOP2();
        if constexpr (!LAST) { SMFA(aq0, A[0], wq[0][0]); SMFA(aq1, A[0], wq[1][0]); }
        SMFA(ak0, A[0], wk[0][0]);
        bar();
        ST2(9);
        TS(1);
        A[2] = ldsr128(ar0); A[3] = ldsr128(ar0 + 1024); SB();
        SMFA(ak1, A[0], wk[1][0]);
        A[4] = ldsr128(ar1); A[5] = ldsr128(ar1 + 1024); SB();
        SMFV(av, A[0], wv[0]);
        A[6] = ldsr128(ar2); A[7] = ldsr128(ar2 + 1024); SB();
        u32x2 tm = {0, 0}, look = {0, 0};
#define SM_(a, w) "v_smfmac_f32_16x16x64_bf16 %0, %" #a ", %" #w ", %15\n\t"
#define POST6(C, acc, W, src, dst, MID, WR) { float t0_, t1_; asm volatile( \
                SM_(3, 9) SM_(4, 10) SM_(5, 11) "v_add_f32 %1, %16, %18\n\t" SM_(6, 12) "v_add_f32 %2, %17, %19\n\t" SM_(7, 13) \
                "v_cvt_pk_bf16_f32 %1, %1, %2\n\t" MID SM_(8, 14) "ds_write2_b32 %20, %1, %1 " WR \
                : "+v"(acc), "=&v"(t0_), "=&v"(t1_) : "v"(A[2]), "v"(A[3]), "v"(A[4]), "v"(A[5]), "v"(A[6]), "v"(A[7]), C(W[2]), C(W[3]), C(W[4]), C(W[5]), C(W[6]), C(W[7]), "v"(sidx), \
                  "v"(src[0]), "v"(src[1]), "v"(src[2]), "v"(src[3]), "v"(dst) : "memory"); }
#define POST3(C, acc, K, W, src, dst, WR) { float t0_, t1_; asm volatile( \
                "v_add_f32 %1, %10, %12\n\tv_smfmac_f32_16x16x64_bf16 %0, %3, %6, %9\n\tv_add_f32 %2, %11, %13\n\tv_smfmac_f32_16x16x64_bf16 %0, %4, %7, %9\n\t" \
                "v_cvt_pk_bf16_f32 %1, %1, %2\n\tv_smfmac_f32_16x16x64_bf16 %0, %5, %8, %9\n\tds_write2_b32 %14, %1, %1 " WR \
                : "+v"(acc), "=&v"(t0_), "=&v"(t1_) : "v"(A[K]), "v"(A[K + 1]), "v"(A[K + 2]), C(W[K]), C(W[K + 1]), C(W[K + 2]), "v"(sidx), \
                  "v"(src[0]), "v"(src[1]), "v"(src[2]), "v"(src[3]), "v"(dst) : "memory"); }
        if constexpr (!LAST) {
            SMFA(aq0, A[1], wq[0][1]);
            SMFA(aq1, A[1], wq[1][1]);
            tm = *(LAS u32x2*)(lds + L_TS + 768); SB();
            SMFA(ak0, A[1], wk[0][1]);
            SMFA(ak1, A[1], wk[1][1]); SMFV(av, A[1], wv[1]);
            TS(2);
            SMFA(aq0, A[2], wq[0][2]); SMFA(aq0, A[3], wq[0][3]); SMFA(aq0, A[4], wq[0][4]); SMFA(aq0, A[5], wq[0][5]);
            look = ld8(rw, tvo, 0); SB();
            SMFA(aq0, A[6], wq[0][6]);
            if (__builtin_expect(b == 0, 0)) {
                const u32x2 sv = {s + tg.t, tg.e};
                asm volatile("buffer_store_dwordx2 %0, %1, %2, %3 offen sc1" :: "v"(sv), "v"(t0o), "s"(rw), "s"(gflag) : "memory");
            }
            SMFA(aq0, A[7], wq[0][7]);
            POST6("a", aq1, wq[1], aq0, qtw, "", "offset0:0 offset1:1");
            TS(3);
            POST6("a", ak0, wk[0], aq1, qtw, "", "offset0:128 offset1:129");
            POST6("a", ak1, wk[1], ak0, qtw + 4096, "s_waitcnt lgkmcnt(0)\n\ts_barrier\n\t", "offset0:0 offset1:1");
                TS(4);
            qf[0] = trf(0); SB();
            SMFV(av, A[2], wv[2]);
            qf[1] = trf(1024); SB();
            SMFV(av, A[3], wv[3]);
            qf[2] = trf(2048); SB();
            SMFV(av, A[4], wv[4]);
            qf[3] = trf(3072); SB();
            POST3("v", av, 5, wv, ak1, qtw + 4096, "offset0:128 offset1:129");
            TS(5);
            f32x4 s0, s1, s2, c[8];
            MF0(s1, kx[0][0], qf[0]);
            ones = ldsr128(lds + L_ONES + lane * 16);
            if (NVX < 3) vx[2] = ldsr128(qb + 8192 + 2048);
            SB();
            MF0(s2, kx[1][0], qf[0]);
            if (NVX < 4) vx[3] = ldsr128(qb + 8192 + 3072);
            SB();
            MF(s1, kx[0][1], qf[1]);
            {
                float t0_, t1_;
                asm volatile("v_add_f32 %2, %8, %10\n\tv_add_f32 %3, %9, %11\n\tv_mfma_f32_16x16x32_bf16 %1, %4, %6, %1\n\tv_cvt_pk_bf16_f32 %2, %2, %3\n\t"
                             "v_mfma_f32_16x16x32_bf16 %0, %5, %7, %0\n\ts_waitcnt lgkmcnt(0)\n\ts_barrier\n\tds_write_b32 %12, %2"
                             : "+v"(s1), "+v"(s2), "=&v"(t0_), "=&v"(t1_) : "v"(kx[1][1]), "v"(kx[0][2]), "v"(qf[1]), "v"(qf[2]), "v"(av[0]), "v"(av[1]), "v"(av[2]), "v"(av[3]), "v"(vtw) : "memory");
            }
                TS(6);
            kf[0] = trf(4096); SB();
            MF(s2, kx[1][2], qf[2]);
            kf[1] = trf(4096 + 1024); SB();
            MF(s1, kx[0][3], qf[3]);
            kf[2] = trf(4096 + 2048); SB();
            MF(s2, kx[1][3], qf[3]);
            kf[3] = trf(4096 + 3072);
            SB();
            ST(2);
            TS(7);
            u32x4 pm;
            float mx;
            {
                float t_;
                MF0(s0, kf[0], qf[0]);
                pm = __builtin_bit_cast(u32x4, zero4());
                asm volatile("v_mfma_f32_16x16x32_bf16 %1, %2, %3, %1\n\tv_max3_f32 %0, %4, %5, %6" : "=&v"(mx), "+v"(s0) : "v"(kf[1]), "v"(qf[1]), "v"(s1[0]), "v"(s1[1]), "v"(s1[2]));
                asm volatile("v_mfma_f32_16x16x32_bf16 %2, %3, %4, %2\n\tv_max3_f32 %1, %5, %6, %7\n\tv_max3_f32 %0, %0, %8, %9"
                             : "+v"(mx), "=&v"(t_), "+v"(s0) : "v"(kf[2]), "v"(qf[2]), "v"(s1[3]), "v"(s2[0]), "v"(s2[1]), "v"(s2[2]), "v"(s2[3]));
                asm volatile("v_mfma_f32_16x16x32_bf16 %2, %3, %4, %2\n\tv_max_f32 %0, %0, %1\n\tv_mov_b32 %1, %0" : "+v"(mx), "+v"(t_), "+v"(s0) : "v"(kf[3]), "v"(qf[3]));
                asm volatile("s_barrier" ::: "memory");
                asm volatile("s_nop 0\n\tv_permlane16_swap_b32 %0, %1\n\tv_max_f32 %0, %0, %1\n\t"
                             "v_mov_b32 %1, %0\n\ts_nop 1\n\tv_permlane32_swap_b32 %0, %1\n\tv_max_f32 %0, %0, %1\n\tv_mul_f32 %0, 0xbe0293ee, %0" FW
                             : "+v"(mx), "+v"(t_) :: "memory");
            }
                ST2(12);
            TS(8);
            u32x4 px;
            float e0, e1, em;
            {
                float a_, b_, c_;
#define FE(d, src) "v_fmamk_f32 " d ", " src ", 0x3e0293ee, %20\n\t"
                asm volatile(FE("%7", "%10") FE("%8", "%11") "v_exp_f32 %7, %7\n\t" FE("%9", "%12") "v_exp_f32 %8, %8\n\t" FE("%1", "%13") "v_exp_f32 %9, %9\n\tv_cvt_pk_bf16_f32 %0, %7, %8\n\t"
                             "v_exp_f32 %1, %1\n\t" FE("%7", "%14") FE("%8", "%15") "v_exp_f32 %7, %7\n\tv_cvt_pk_bf16_f32 %1, %9, %1\n\tv_exp_f32 %8, %8\n\t" FE("%9", "%16") FE("%3", "%17")
                             "v_exp_f32 %9, %9\n\tv_cvt_pk_bf16_f32 %2, %7, %8\n\tv_exp_f32 %3, %3\n\t" FE("%4", "%18") FE("%5", "%19") "v_cvt_pk_bf16_f32 %3, %9, %3\n\tv_max_f32 %6, %4, %5"
                             : "=&v"(px.x), "=&v"(px.y), "=&v"(px.z), "=&v"(px.w), "=&v"(e0), "=&v"(e1), "=&v"(em), "=&v"(a_), "=&v"(b_), "=&v"(c_)
                             : "v"(s1[0]), "v"(s1[1]), "v"(s1[2]), "v"(s1[3]), "v"(s2[0]), "v"(s2[1]), "v"(s2[2]), "v"(s2[3]), "v"(s0[0]), "v"(s0[1]), "v"(mx), "v"(s0));
            }
            const bool slow = __any(em > 0.f);
            SB();
            TS(9);
            f32x4 o[4], ls;
            MF0(o[0], vx[0], px);
            kf[0].x = *(LAS unsigned*)vtr; SB(); kf[2].x = *(LAS unsigned*)(vtr + 512); SB();
            MF0(o[2], vx[2], px);
            kf[1].x = *(LAS unsigned*)(vtr + 256); SB(); kf[3].x = *(LAS unsigned*)(vtr + 768); SB();
            float up = 0.f;
            if (__builtin_expect(slow, 0)) {
                up = xmax(vmax(em, 0.f)); e0 -= up; e1 -= up;
            }
            SB();
            MF0(o[1], vx[1], px);
            asm volatile("v_exp_f32 %1, %1\n\tv_mfma_f32_16x16x32_bf16 %0, %2, %3, 0" : "=&v"(o[3]), "+v"(e0) : "v"(vx[3]), "v"(px));
            asm volatile("v_exp_f32 %2, %2\n\tv_mfma_f32_16x16x32_bf16 %0, %4, %5, 0\n\tv_cvt_pk_bf16_f32 %1, %3, %2" : "=&v"(ls), "=&v"(pm.x), "+v"(e1) : "v"(e0), "v"(ones), "v"(px));
            SB();
            if (__builtin_expect(slow, 0)) {
                asm volatile("s_nop 7" : "+v"(o[0]), "+v"(o[1]), "+v"(o[2]), "+v"(o[3]), "+v"(ls));
                const float al = __builtin_amdgcn_exp2f(-up);
                o[0] *= al; o[1] *= al; o[2] *= al; o[3] *= al; ls *= al;
            }
            c[0] = zero4();
            asm volatile("" : "+v"(pm), "+v"(o[0]), "+v"(o[1]), "+v"(o[2]), "+v"(o[3]), "+v"(ls));
            TS(10);
            MF(ls, ones, pm);
            c[1] = zero4();
            MF(o[0], kf[0], pm);
            c[2] = zero4();
            MF(o[2], kf[2], pm);
            c[3] = zero4();
            MF(o[1], kf[1], pm);
            c[4] = zero4();
            u32x4 o8;
            {
                float inv; f32x2 il, ih, t0_, t1_;
                asm volatile("v_mfma_f32_16x16x32_bf16 %0, %3, %4, %0\n\tv_rcp_f32 %1, %5\n\ts_nop 0\n\tv_cndmask_b32_e64 %2, 0, %1, %6\n\tv_cndmask_b32_e64 %1, %1, 0, %6"
                             : "+v"(o[3]), "=&v"(il.x), "=&v"(ih.x) : "v"(kf[3]), "v"(pm), "v"(ls[0]), "s"(himask), "v"(ls));
                (void)inv; (void)t0_; (void)t1_;
                TS(11);
                c[5] = zero4();
                f32x4 oa = o[0] * il.x + o[2] * ih.x;
                o8.x = pk(oa[0], oa[1]); o8.y = pk(oa[2], oa[3]);
                asm volatile("" : "+v"(o8.x), "+v"(o8.y), "+v"(o[1]), "+v"(o[3]));
                const f32x4 ob = o[1] * il.x + o[3] * ih.x;
                o8.z = pk(ob[0], ob[1]); o8.w = pk(ob[2], ob[3]);
            }
            ST2(13);
            asm volatile("s_nop 1" : "+v"(o8));
            TS(12);
            SB();
            SMFV(c[0], o8, wo[0]); c[6] = zero4(); SMFV(c[1], o8, wo[1]); c[7] = zero4(); SMFV(c[2], o8, wo[2]); SMFV(c[3], o8, wo[3]);
            const unsigned slot = pblk(ring, 16 * w) + b * 128;
            u32x4 pa;
            {
                float t_;
                asm volatile("v_add_f32 %4, %15, %17\n\tv_smfmac_f32_16x16x64_bf16 %0, %9, %10, %14\n\tv_add_f32 %8, %16, %18\n\tv_smfmac_f32_16x16x64_bf16 %1, %9, %11, %14\n\t"
                             "v_cvt_pk_bf16_f32 %4, %4, %8\n\tv_smfmac_f32_16x16x64_bf16 %2, %9, %12, %14\n\tv_add_f32 %5, %19, %21\n\tv_smfmac_f32_16x16x64_bf16 %3, %9, %13, %14\n\t"
                             "v_add_f32 %8, %20, %22\n\tv_cvt_pk_bf16_f32 %5, %5, %8\n\tv_add_f32 %6, %23, %25\n\tv_add_f32 %8, %24, %26\n\tv_cvt_pk_bf16_f32 %6, %6, %8"
                             : "+v"(c[4]), "+v"(c[5]), "+v"(c[6]), "+v"(c[7]), "=&v"(pa.x), "=&v"(pa.y), "=&v"(pa.z), "=&v"(pa.w), "=&v"(t_)
                             : "v"(o8), "v"(wo[4]), "v"(wo[5]), "v"(wo[6]), "v"(wo[7]), "v"(sidx),
                               "v"(c[0][0]), "v"(c[0][1]), "v"(c[0][2]), "v"(c[0][3]), "v"(c[1][0]), "v"(c[1][1]), "v"(c[1][2]), "v"(c[1][3]), "v"(c[2][0]), "v"(c[2][1]), "v"(c[2][2]), "v"(c[2][3]));
            }
            auto fold = [](f32x4 v) { const f32x2 t = (f32x2){v[0], v[1]} + (f32x2){v[2], v[3]}; return pk(t.x, t.y); };
            pa.w = fold(c[3]);
            st16(rw, pvo, pa, slot);
            TS(13);
            SB();
            st16(rw, pvo, (u32x4){fold(c[4]), fold(c[5]), fold(c[6]), fold(c[7])}, slot + 131072);
            TS(14);
            SB();
            const unsigned now = rt();
            ST(3);
            T1 = __builtin_amdgcn_readfirstlane(tm.x); T2 = __builtin_amdgcn_readfirstlane(tm.y);
            { const int dl = s == 0 ? 99 : (int)(now + TV1 - T1); if (dl > 0) T1 = now + TV1 + (dl > TS_ ? TM : 0);
            }
        } else {
            stf(rw, b == 0 ? t0o : OOB, s, gflag, tg);
#pragma unroll
            for (int ks = 1; ks < 8; ++ks) { SMFA(ak0, A[ks], wk[0][ks]); SMFA(ak1, A[ks], wk[1][ks]); SMFV(av, A[ks], wv[ks]); }
            asm volatile("s_nop 7" : "+v"(av), "+v"(ak0), "+v"(ak1));
            unsigned d0 = pk(ak0[0] + ak0[2], ak0[1] + ak0[3]); dsw2<0, 1>(qtw + 4096, d0, d0);
            d0 = pk(ak1[0] + ak1[2], ak1[1] + ak1[3]); dsw2<128, 129>(qtw + 4096, d0, d0);
            ldsw32(vtw, pk(av[0] + av[2], av[1] + av[3]));
            bar();
        }
        {
            const u32x4 kv = trf(4096 + w * 1024);
            const u32x4 vv = {*(LAS unsigned*)vtr, *(LAS unsigned*)(vtr + 256), *(LAS unsigned*)(vtr + 512), *(LAS unsigned*)(vtr + 768)};
            const unsigned kb = kvm_off(ho, cfg, s * 32 + h);
            st16(rw, kso, kv, kb + w * 512); st16(rw, tso, vv, kb + 2048 + hf * 1024);
        }
        if constexpr (LAST) return; else {
        ST(4);
        u32x4 p0, p1, nv[3];
        unsigned pb = pblk(ring, b) + w * 2048, pb1 = pb + 1024, mnx = ho0 + (HO_MEM + rnext * MRS) * 16384 + b * 128;
        asm volatile("" : "+s"(pb), "+s"(pb1), "+s"(mnx));
        if (__builtin_expect((unsigned)s + 2 > (unsigned)__builtin_amdgcn_readfirstlane(*(LAS unsigned*)(lds + L_TS + 1024)), 0)) qkv_wait(s + 2);
        qkv_issue(s + 1, nv);
        tvo = look_at(min(s + 3, NSEG - 1));
        const unsigned lk = dec(look, tg), qmin = min(min(__builtin_amdgcn_readlane(lk, 32), __builtin_amdgcn_readlane(lk, 33)), min(__builtin_amdgcn_readlane(lk, 34), __builtin_amdgcn_readlane(lk, 35)));
        if (b == 0 && (s & 3) == 3) {
            const unsigned c = (unsigned)__builtin_amdgcn_s_memtime(), t = rt();
            const float tpc = (float)(t - pt) * __builtin_amdgcn_rcpf((float)(c - pc));
            { int xi = (int)(CX * tpc + 0.5f), yi = (int)(CY * tpc + 0.5f); asm volatile("" : "+v"(xi), "+v"(yi)); X = TX + __builtin_amdgcn_readfirstlane(xi); Y = TY + __builtin_amdgcn_readfirstlane(yi); }
            pc = c; pt = t; mg -= mg > 0;
        }
        wait_rt(T1, quant);
        ST(5);
        p0 = ld16(rw, l16, pb); p1 = ld16(rw, l16, pb1);
        SB();
        qkv_store(s + 1, nv);
        SB();
        u32x4 sel = ldsr128(lds + L_SEL + lane * 16);
        asm volatile("" : "+v"(sel));
        SB();
        f32x4 rr;
        MF0(rr, sel, p0); MF(rr, sel, p1);
        SB();
        if (__builtin_expect(!__all(mx4(p0, mx4(p1, 0u)) != SENT), 0)) {
            for (unsigned spins = 0;; ++spins) {
                asm volatile("" ::: "memory");
                p0 = ld16(rw, l16, pb); p1 = ld16(rw, l16, pb1);
                if (__all(mx4(p0, mx4(p1, 0u)) != SENT)) break;
                if (spins > cap) { cap = 0; break; }
            }
            MF0(rr, sel, p0); MF(rr, sel, p1);
            asm volatile("s_nop 7" : "+v"(rr));
        }
        ST(6);
        {
            asm volatile("s_nop 0" : "+v"(rr));
            unsigned q0, q1, q2, q3;
            asm volatile("v_add_f32_dpp %0, %4, %4 row_ror:8 row_mask:0xf bank_mask:0xf\n\tv_add_f32_dpp %1, %5, %5 row_ror:8 row_mask:0xf bank_mask:0xf\n\t"
                         "v_add_f32_dpp %2, %6, %6 row_ror:8 row_mask:0xf bank_mask:0xf\n\tv_add_f32_dpp %3, %7, %7 row_ror:8 row_mask:0xf bank_mask:0xf"
                         : "=&v"(q0), "=&v"(q1), "=&v"(q2), "=&v"(q3) : "v"(rr[0]), "v"(rr[1]), "v"(rr[2]), "v"(rr[3]));
            dsw2<0, 8>(rdw, q0, q1); dsw2<2, 10>(rdw, q2, q3);
        }
        bar();
        {
            unsigned mv;
            if (__builtin_expect(b == 0 && w >= 2, 0)) {
                const unsigned now = rt() + 2; const int dl = s == 0 ? 99 : (int)(now + TV2 - T2);
                if (dl > TS_ && s > 1) mg = min(mg + MUP, 24u);
                const unsigned n1 = min((dl > 0 ? now + TV2 + (dl > TS_ ? TPEN : 0) : T2) + X + mg, SENT - 1024);
                mv = (lane & 1) ? n1 + Y + (mg >> 1) : n1;
            } else {
                f32x2 a0, a1, a2, a3;
                asm volatile("ds_read_b64 %0, %4\n\tds_read_b64 %1, %4 offset:256\n\tds_read_b64 %2, %4 offset:512\n\tds_read_b64 %3, %4 offset:768\n\ts_waitcnt lgkmcnt(0)"
                             : "=&v"(a0), "=&v"(a1), "=&v"(a2), "=&v"(a3) : "v"(rdr) : "memory");
                const f32x2 t = (a0 + a1) + (a2 + a3);
                mv = pk(t.x, t.y);
            }
            st4(rw, mso, mv, mnx);
            SB();
            const unsigned now = rt(); const int dl = s == 0 ? 99 : (int)(now + TV2 - T2);
            SB();
            {
                const u32x4 z = sent4();
                st16(rw, l16, z, pb);
                st16(rw, l16, z, pb1);
                pold = pb1;
                st4(rw, mso, z.x, ho0 + (HO_MEM + ring * MRS) * 16384 + b * 128);
            }
            if (dl > 0) T2 = now + TV2 + (dl > TS_ ? TM : 0);
        }
        if (__builtin_expect(s == NSEG - 2, 0)) { const u32x2 sv = {s + 1 + tg.t, tg.e}; asm volatile("buffer_store_dwordx2 %0, %1, %2, %3 offen sc1" :: "v"(sv), "v"(tso - 16 * 63), "s"(rw), "s"(kflag) : "memory"); }
        if (w == 1) *(LAS unsigned*)(lds + L_TS + 1024) = qmin;
        ST(7);
        ring = rnext;
        ST(8);
        }
    };
    for (int s = 0; s < NSEG - 1; ++s) step(s, bool_c<false>{});
    step(NSEG - 1, bool_c<true>{});
    drain(); bar();
    stf(rw, t0o, NSEG, kflag, tg);
}

template <int ROLE>
DI void stream_role(const Params& p, lptr lds, const int j, const int tid) {
    const int w = __builtin_amdgcn_readfirstlane(tid >> 6), lane = tid & 63, n = lane & 15, g = lane >> 4, pp = lane & 3;
    const rsrc_t rw = mkrs(p.ws, WS_END);
    const Tag tg = get_tag(p.ws);
    auto modv = [&](unsigned col) { return __builtin_bit_cast(f32x4, ld16(rw, col * 4, WS_MOD)); };
    unsigned cap = 1u << 17;
    const unsigned l16 = lane * 16, t0o = tid == 64 ? 0u : OOB;
    const int r = ROLE == 0 ? j : ROLE == 1 ? j - 64 : ROLE == 2 ? j - 120 : ROLE == 3 ? j - 152 : ROLE == 5 ? j - 168 : j - 128;
    const int tt = ROLE == 3 ? r >> 3 : ROLE == 1 ? r & 1 : (r >> 3) & 1;
    const int cgx = ROLE == 1 ? r >> 1 : (r & 7) + 8 * (r >> 4);
    constexpr int QD = ROLE == 0 ? 2 : (ROLE == 1 || ROLE == 4) ? 1 : 0;
    const int qm = QD == 2 ? r >> 5 : 2, qh = QD == 2 ? r & 31 : (r & 3) + 4 * (r >> 3), qtt = QD == 2 ? 0 : (r >> 2) & 1;
    const unsigned qline = FL_QKV + (QD == 2 ? r : 64 + r);
    u32x4 pv[32];
    if (QD) { for (int i = 0; i < PDLY; ++i) __builtin_amdgcn_s_sleep(16);
        const u16* W = (qm == 0 ? p.Wq : qm == 1 ? p.Wk : p.Wv) + qh * 128; panel_issue(W, W + 64, HD, tid, pv); }
    else {
        const int sg = ROLE == 2 ? r : ROLE == 3 ? 32 + r : 48 + r;
        LAS float* red = (LAS float*)lds;
        if (ROLE == 5) {
            unsigned* ap = (unsigned*)(p.ws + WS_HO + 240 * 16384 + (r & 1) * 8192 + (r >> 1) * 1024 + w * 256);
            unsigned best = 0xffffu;
            for (int rep = 0; rep < PREP; ++rep) {
                unsigned long long t0, t1; unsigned q = 0;
                asm volatile("s_memtime %0\n\ts_waitcnt lgkmcnt(0)" : "=s"(t0) :: "memory");
                if (lane == 0) q = __hip_atomic_fetch_add(ap, 1u, __ATOMIC_RELAXED, __HIP_MEMORY_SCOPE_AGENT);
                asm volatile("s_waitcnt vmcnt(0)\n\ts_memtime %0\n\ts_waitcnt lgkmcnt(0)" : "=s"(t1), "+v"(q) :: "memory");
                best = min(best, (unsigned)(t1 - t0));
            }
            ((LAS unsigned*)lds)[w] = best;
            bar();
            const u32x4 b4 = ldsr128(lds);
            stf(rw, t0o, min(min(b4.x, b4.y), min(b4.z, b4.w)), WS_EARLY + (EF_PROBE + r) * 128, tg);
        }
        auto mod_part = [&](auto nw_, int col0, unsigned line, auto&& between) {
            constexpr int NW = decltype(nw_)::value, RP = 64 / NW, NIT = 128 / RP;
            u32x4 wv[NIT]; u16 cv[NIT];
            const int rr = lane / NW, cc = lane % NW;
#pragma unroll
            for (int it = 0; it < NIT; ++it) { wv[it] = *(const u32x4*)(p.wmod + (size_t)(w * 128 + it * RP + rr) * 3072 + col0 + cc * 8); cv[it] = p.c[w * 128 + it * RP + rr]; }
            between();
            SU(64 + j, 5);
            float acc[8] = {};
#pragma unroll
            for (int it = 0; it < NIT; ++it) {
                const float c_ = bflo(cv[it]), sl = rb(c_ * __builtin_amdgcn_rcpf(1.f + __expf(-c_)));
                acc[0] += sl * bflo(wv[it].x); acc[1] += sl * bfhi(wv[it].x); acc[2] += sl * bflo(wv[it].y); acc[3] += sl * bfhi(wv[it].y);
                acc[4] += sl * bflo(wv[it].z); acc[5] += sl * bfhi(wv[it].z); acc[6] += sl * bflo(wv[it].w); acc[7] += sl * bfhi(wv[it].w);
            }
#pragma unroll
            for (int i = 0; i < 8; ++i) { if (NW == 4) acc[i] += dpp<0x124>(acc[i]); acc[i] += dpp<0x128>(acc[i]); acc[i] = xsum(acc[i]); }
            bar();
            if (lane < NW) {
#pragma unroll
                for (int i = 0; i < 8; ++i) red[w * 64 + cc * 8 + i] = acc[i];
            }
            bar();
            if (w == 1) {
                st4(rw, lane < NW * 8 ? lane * 4 : OOB, __float_as_uint(rb((red[lane] + red[64 + lane]) + (red[128 + lane] + red[192 + lane]))), WS_MOD + col0 * 4);
                drain();
                stf(rw, t0o, 1, WS_EARLY + line * 128, tg);
            }
        };
        u32x4 xv[8];
        auto x_issue = [&]() {
#pragma unroll
            for (int q = 0; q < 8; ++q) xv[q] = *(const u32x4*)(p.x + (size_t)(sg * 32 + w * 8 + q) * D + lane * 8); };
        if (ROLE != 5 && sg >= 16) mod_part(int_c<4>{}, (sg - 16) * 32, EF_MOD + sg - 16, x_issue);
        else x_issue();
        SU(64 + j, 3);
        float mean[8], var[8];
#pragma unroll
        for (int q = 0; q < 8; ++q) mean[q] = ((bflo(xv[q].x) + bfhi(xv[q].x)) + (bflo(xv[q].y) + bfhi(xv[q].y))) + ((bflo(xv[q].z) + bfhi(xv[q].z)) + (bflo(xv[q].w) + bfhi(xv[q].w)));
        auto wsum8 = [&](float (&v)[8]) {
#pragma unroll
            for (int q = 0; q < 8; ++q) v[q] += dpp<0xB1>(v[q]);
#pragma unroll
            for (int q = 0; q < 8; ++q) v[q] += dpp<0x4E>(v[q]);
#pragma unroll
            for (int q = 0; q < 8; ++q) v[q] += dpp<0x124>(v[q]);
#pragma unroll
            for (int q = 0; q < 8; ++q) v[q] += dpp<0x128>(v[q]);
#pragma unroll
            for (int q = 0; q < 8; ++q) v[q] = xsum(v[q]) * (1.f / D);
        };
        wsum8(mean);
#pragma unroll
        for (int q = 0; q < 8; ++q) {
            var[q] = 0.f;
#pragma unroll
            for (int i = 0; i < 4; ++i) { const float d0_ = bflo(xv[q][i]) - mean[q], d1_ = bfhi(xv[q][i]) - mean[q]; var[q] += d0_ * d0_ + d1_ * d1_; }
        }
        wsum8(var);
#pragma unroll
        for (int q = 0; q < 8; ++q) {
            const float mb = rb(mean[q]), isd = __builtin_amdgcn_rcpf(rb(__builtin_amdgcn_sqrtf(rb(rb(var[q]) + 1e-5f))));
#pragma unroll
            for (int i = 0; i < 4; ++i) { const unsigned u = vcvt(bflo(xv[q][i]) - mb, bfhi(xv[q][i]) - mb); xv[q][i] = vcvt(bflo(u) * isd, bfhi(u) * isd); }
        }
        SU(64 + j, 6);
        wait_words(rw, tg, WS_EARLY + (EF_MOD + (lane & 31)) * 128, 1, cap);
        SU(64 + j, 7);
        float t[8], sh[8];
        { const f32x4 m0 = modv(lane * 8), m1 = modv(lane * 8 + 4), m2 = modv(512 + lane * 8), m3 = modv(516 + lane * 8);
#pragma unroll
          for (int i = 0; i < 4; ++i) { sh[i] = m0[i]; sh[4 + i] = m1[i]; t[i] = rb(1.f + m2[i]); t[4 + i] = rb(1.f + m3[i]); } }
#pragma unroll
        for (int q = 0; q < 8; ++q) {
            u32x4 o;
#pragma unroll
            for (int i = 0; i < 4; ++i) { const unsigned u = vcvt(bflo(xv[q][i]) * t[2 * i], bfhi(xv[q][i]) * t[2 * i + 1]); o[i] = pk(bflo(u) + sh[2 * i], bfhi(u) + sh[2 * i + 1]); }
            st16(rw, (lane >> 2) * 1024 + (lane & 3) * 16, o, WS_H1 + (unsigned)(sg * 2 + (w >> 1)) * 16384u + ((w & 1) * 8 + q) * 64);
        }
        drain(); bar();
        stf(rw, t0o, 1, WS_EARLY + (EF_LN + sg) * 128, tg);
        SU(64 + j, 4);
        if (ROLE == 2) mod_part(int_c<8>{}, 1024 + r * 64, EF_MOD2 + r, [] {});
    }
    unsigned cfg = 0;
    if (!QD) cfg = get_cfg(rw, tg, lane, cap);
    SU(64 + j, 0);
    u32x4 qa[16], qb[16], hv[QD ? 4 * QD : 1];
    const unsigned h1off = WS_H1 + (unsigned)(qtt * 16 + w * 4) * 1024u;
    auto h1_issue = [&](int s) {
#pragma unroll
        for (int i = 0; i < 4 * QD; ++i) hv[i] = ld16(rw, (n * 4 + g) * 16, h1off + (unsigned)s * 32768u + (i >> 2) * 16384 + (i & 3) * 1024); };
    auto qkv_in = [&]() {
#pragma unroll
        for (int i = 0; i < 4 * QD; ++i) ldsw128(lds + L_Q + (i >> 2) * 16384 + (w * 4 + (i & 3)) * 1024 + lane * 16, hv[i]);
    };
    auto qkv_mm = [&](int s) {
#pragma unroll
        for (int t = 0; t < QD; ++t) {
            f32x4 ca, cb;
            tile_mma<QD == 1>(lds + L_Q + t * 16384, lane, qa, qb, ca, cb);
            st16(rw, l16, (u32x4){pk(ca[0], ca[1]), pk(ca[2], ca[3]), pk(cb[0], cb[1]), pk(cb[2], cb[3])}, qkv_off(s * 32 + qh, qm, cfg) + (QD == 2 ? t * 4 + w : w * 2 + qtt) * 1024);
        }
    };
    auto qkv_seg = [&](int s) { qkv_in(); h1_issue(min(s + 1, NSEG - 1)); bar(); qkv_mm(s); };
    auto publish = [&](unsigned v) { drain(); bar(); stf(rw, t0o, v, flg(qline, cfg), tg); };
    u32x4 sa[16], sb[16];
    if (QD) {
        panel_finish(lds, tid, 32 * w + 8 * pp, 32 * w + 8 * pp + 4, pv, qa, qb);
        SU(64 + j, 1);
        cfg = get_cfg(rw, tg, lane, cap);
        wait_words(rw, tg, WS_EARLY + (EF_LN + (lane & 15)) * 128, 1, cap);
        SU(64 + j, 2);
        h1_issue(0);
        for (int s = 0; s < 7; ++s) { qkv_seg(s); publish(s + 1); }
        SU(64 + j, 4);
        if (ROLE == 0) { const u16* W = p.Wo + (size_t)(r & 7) * 512 * D + (r >> 4) * 128; panel_issue(W, W + 64, D, tid, pv); }
        if (ROLE == 1) panel_issue(p.wg + cgx * 64, p.wu + cgx * 64, F, tid, pv);
        SB();
        qkv_seg(7); publish(8);
        if (ROLE == 0) panel_finish(lds, tid, 32 * w + 4 * pp, 32 * w + 16 + 4 * pp, pv, sa, sb);
        if (ROLE == 1) panel_finish(lds, tid, 16 * w + 4 * pp, 64 + 16 * w + 4 * pp, pv, sa, sb);
    }
    if (ROLE == 2) {
        const rsrc_t rd = mkrs(p.wd + cgx * 32, (1791 * D + 32) * 2);
#pragma unroll
        for (int half = 0; half < 2; ++half) {
            u32x4 v[14];
#pragma unroll
            for (int i = 0; i < 14; ++i) v[i] = ldw(rd, ((tid >> 2) * D + (tid & 3) * 8) * 2, (half * 896 + i * 64) * D * 2);
            bar();
#pragma unroll
            for (int i = 0; i < 14; ++i) ldsw128(lds + ((tid >> 2) + 64 * i) * 80 + (tid & 3) * 16, v[i]);
            bar();
            if ((w >> 1) == half) {
                lptr pa = lds + ((w & 1) * 448 + 8 * g + (n >> 2)) * 80 + pp * 8;
#pragma unroll
                for (int ks = 0; ks < 14; ++ks) { sa[ks] = tr2(pa + ks * 32 * 80, 4 * 80); sb[ks] = tr2(pa + ks * 32 * 80 + 32, 4 * 80); }
            }
        }
        bar();
    }
    if (QD) { wait_words(rw, tg, WS_EARLY + (EF_LN + lane) * 128, 1, cap); for (int s = 8; s < LEAD; ++s) { qkv_seg(s); publish(s + 1); } }
    SU(64 + j, 5);
    unsigned poff; unsigned ahead = 0;
    if (ROLE == 0) poff = FL_A + (r & 7) * 2 + (lane & 1);
    if (ROLE == 1) poff = FL_L + tt * 8 + (lane & 7);
    if (ROLE == 2) poff = FL_C + tt + 2 * (lane < 28 ? lane : 0);
    if (ROLE == 3) poff = FL_B + (lane & 7) + tt * 8 + ((lane >> 3) & 3) * 16;
    if (ROLE == 4) poff = FL_G;
    if (ROLE == 5) {
        const int hh = 2 * r + ((lane >> 1) & 1), k = (lane >> 2) & 3, pr = (hh & 3) + 8 * (hh >> 2) + 4 * (lane & 1);
        poff = k == 0 ? FL_KM + pr : k == 3 ? FL_QKV + 64 + pr : FL_QKV + (k - 1) * 32 + hh;
        ahead = (lane & 12) != 0;
        if (lane == 63) { poff = FL_G; ahead = 0; }
    }
    poff = flg(poff, cfg);
    const unsigned oflag = flg((ROLE == 0 ? FL_B : ROLE == 1 ? FL_C : ROLE == 3 ? FL_L : FL_A) + r, cfg), qflag = flg(qline, cfg);
    unsigned seen = 0;
    auto ready = [&](unsigned nd) {
        if (ROLE != 5) return __all(seen >= nd);
        const unsigned gl = __builtin_amdgcn_readlane(seen, 63);
        return __all(((lane & 12) == 0 ? max(seen, gl) : lane == 63 ? nd : seen) >= nd);
    };
    auto wait_in = [&](unsigned need) {
        if (w == 0) {
            const unsigned nd = min(need + ahead, (unsigned)NSEG);
            for (unsigned spins = 0; !ready(nd); ++spins) {
                if (spins > cap) { cap = 0; break; }
                if (spins) __builtin_amdgcn_s_sleep(PSL);
                asm volatile("" ::: "memory");
                seen = ldf(rw, poff, tg);
            }
        }
        bar();
    };
    const unsigned pof0 = w == 0 ? poff : OOB;
    const int hh = 2 * r + (w >> 1), att = w & 1;
    u32x4 xq[4], ka[4], kb[4], vb[8];
    const unsigned l16b = att ? l16 : OOB;
    auto a_issue_qk = [&](int s) {
        const unsigned q_ = qkv_off(s * 32 + hh, 0, cfg) + att * 4096, k_ = qkv_off(s * 32 + hh, 1, cfg);
#pragma unroll
        for (int ks = 0; ks < 4; ++ks) { xq[ks] = ld16(rw, l16, q_ + ks * 1024); ka[ks] = ld16(rw, l16, k_ + ks * 1024); kb[ks] = ld16(rw, l16b, k_ + 4096 + ks * 1024); }
    };
    auto a_issue_v = [&](int s) {
        const unsigned v_ = qkv_off(s * 32 + hh, 2, cfg);
#pragma unroll
        for (int i = 0; i < 8; ++i) vb[i] = ld16(rw, (i & 1) ? l16b : l16, v_ + i * 1024);
    };
    if (ROLE == 5) { wait_in(0); a_issue_qk(0); a_issue_v(0); }
    const int d0 = 128 * w + 16 * (lane >> 3) + 4 * (lane & 3);
    f32x4 cga, cgb, cgc;
    if (ROLE == 2 || ROLE == 3) wait_words(rw, tg, WS_EARLY + (EF_MOD2 + (lane & 31)) * 128, 1, cap);
    if (ROLE == 3) { cga = modv(1024 + d0); cgb = modv(1536 + d0); cgc = modv(2048 + d0);
#pragma unroll
        for (int i = 0; i < 4; ++i) cgc[i] = rb(1.f + cgc[i]); }
    if (ROLE == 2) cga = modv(2560 + cgx * 32 + (w & 1) * 16 + 4 * g);
    u32x2 xnext = {0, 0};
    if (ROLE == 3) xnext = *(const u32x2*)(p.x + (size_t)(tt * 16 + 2 * (r & 7) + ((lane >> 2) & 1)) * D + d0);
    if (QD) qkv_in();
    auto iter = [&](const int s, auto doq_) {
        constexpr bool doq = decltype(doq_)::value;
        u32x2 xv = {0, 0};
        wait_in(s + 1);
        u32x4 a[14];
        if (ROLE == 0) {
#pragma unroll
            for (int i = 0; i < 4; ++i) a[i] = ld16(rw, l16, loc(WS_O, (unsigned)((s * 2 + tt) * 128 + (r & 7) * 16 + w * 4) * 1024u, cfg, 1) + i * 1024);
        }
        if (ROLE == 1) {
#pragma unroll
            for (int i = 0; i < 4; ++i) a[i] = ld16(rw, (n * 4 + g) * 16, loc(WS_H2, (unsigned)((s * 2 + tt) * 16 + w * 4) * 1024u, cfg, 0) + i * 1024);
        }
        if (ROLE == 2) {
#pragma unroll
            for (int ks = 0; ks < 14; ++ks) a[ks] = ld16(rw, (g >> 1) * 512 + n * 32 + (g & 1) * 16, loc(WS_G, (unsigned)((s * 2 + tt) * 56 + w * 14 + ks) * 1024u, cfg, 1));
            xv = ld8(rw, (n >> 3) * 16384 + (n & 7) * 1024 + ((w & 1) * 16 + 4 * g) * 2, loc(WS_X1, (s * 2 + tt) * 16384, cfg, 1) + cgx * 64);
        }
        if (ROLE == 3) {
#pragma unroll
            for (int kg = 0; kg < 8; ++kg) a[kg] = ld16(rw, (lane >> 3) * 1024 + (lane & 7) * 16, loc(WS_PB, (unsigned)(((s * 8 + kg) * 2 + tt) * 32 + 8 * w) * 1024u, cfg, 1) + (r & 7) * 128);
            xv = xnext;
            xnext = *(const u32x2*)(p.x + (size_t)(min(s + 1, NSEG - 1) * 32 + tt * 16 + 2 * (r & 7) + ((lane >> 2) & 1)) * D + d0);
        }
        if (ROLE == 5) {
            const unsigned kb_ = kvm_off(WS_HO, cfg, s * 32 + hh);
#pragma unroll
            for (int ks = 0; ks < 4; ++ks) a[ks] = ld16(rw, ((2 * (n >> 2) + (n & 1)) * 4 + g) * 16, kb_ + ks * 512);
            a[4] = ld16(rw, l16, kb_ + 2048); a[5] = ld16(rw, l16, kb_ + 3072);
        }
        const u32x2 sraw = ld8(rw, pof0, 0);
        if (doq) h1_issue(min(s + LEAD + 1, NSEG - 1));
        SB();
        if (doq) qkv_mm(s + LEAD);
        SB();
        if (ROLE == 0 || ROLE == 1) {
#pragma unroll
            for (int i = 0; i < 4; ++i) ldsw128(lds + L_A + (w * 4 + i) * 1024 + lane * 16, a[i]);
            bar();
            f32x4 ca, cb; tile_mma<false>(lds + L_A, lane, sa, sb, ca, cb);
            if (ROLE == 0) {
                const unsigned o = loc(WS_PB, (unsigned)(((s * 8 + (r & 7)) * 2 + tt) * 32 + (r >> 4) * 8 + w * 2) * 1024u, cfg, 1);
                st16(rw, (n * 4 + g) * 16, __builtin_bit_cast(u32x4, ca), o); st16(rw, (n * 4 + g) * 16, __builtin_bit_cast(u32x4, cb), o + 1024);
            } else {
                float q[4];
#pragma unroll
                for (int i = 0; i < 4; ++i) { const float gv = rb(ca[i]); q[i] = rb(gv * __builtin_amdgcn_rcpf(1.f + __expf(-gv))) * rb(cb[i]); }
                st8(rw, n * 32 + g * 8, (u32x2){pk(q[0], q[1]), pk(q[2], q[3])}, loc(WS_G, (unsigned)((s * 2 + tt) * 56 + cgx * 2 + (w >> 1)) * 1024u, cfg, 1) + (w & 1) * 512);
            }
        }
        if (ROLE == 2) {
            f32x4 c0 = {0, 0, 0, 0}, c1 = c0;
            asm volatile("s_nop 1" : "+v"(c0), "+v"(c1));
#pragma unroll
            for (int ks = 0; ks < 14; ++ks) { MFWA(c0, sa[ks], a[ks]); MFWA(c1, sb[ks], a[ks]); }
            asm volatile("s_nop 7" : "+v"(c0), "+v"(c1));
            *(LAS f32x4*)(lds + L_A + (w * 2) * 1024 + lane * 16) = c0; *(LAS f32x4*)(lds + L_A + (w * 2 + 1) * 1024 + lane * 16) = c1;
            bar();
            if (w < 2) {
                lptr q = lds + L_A + w * 1024 + lane * 16;
                const f32x4 y = (*(LAS f32x4*)q + *(LAS f32x4*)(q + 2048)) + (*(LAS f32x4*)(q + 4096) + *(LAS f32x4*)(q + 6144));
                const float o0 = bflo(xv.x) + rb(cga[0] * rb(y[0])), o1 = bfhi(xv.x) + rb(cga[1] * rb(y[1])), o2 = bflo(xv.y) + rb(cga[2] * rb(y[2])), o3 = bfhi(xv.y) + rb(cga[3] * rb(y[3]));
                *(u32x2*)(p.out + (size_t)(s * 32 + tt * 16 + n) * D + cgx * 32 + (w & 1) * 16 + 4 * g) = (u32x2){pk(o0, o1), pk(o2, o3)};
            }
        }
        if (ROLE == 5) {
            f32x4 x0, x1, x2;
            MF0(x1, ka[0], xq[0]); MF0(x2, kb[0], xq[0]);
#pragma unroll
            for (int ks = 1; ks < 4; ++ks) { MF(x1, ka[ks], xq[ks]); MF(x2, kb[ks], xq[ks]); }
            MF0(x0, a[0], xq[0]); MF(x0, a[1], xq[1]); MF(x0, a[2], xq[2]); MF(x0, a[3], xq[3]);
            a_issue_qk(min(s + 1, NSEG - 1));
            asm volatile("s_nop 7" : "+v"(x0), "+v"(x1), "+v"(x2));
            SB();
            const float raw[10] = {x1[0], x1[1], x1[2], x1[3], x2[0], x2[1], x2[2], x2[3], x0[0], x0[1]};
            float sc[10];
            const int qi = 16 * att + n - 4 * g;
#pragma unroll
            for (int i = 0; i < 5; ++i) {
                const unsigned u = vcvt(raw[2 * i], raw[2 * i + 1]);
                const unsigned v = pk(bflo(u) * 0.08838834764831845f, bfhi(u) * 0.08838834764831845f);
                sc[2 * i] = bflo(v); sc[2 * i + 1] = bfhi(v);
            }
#pragma unroll
            for (int i = 0; i < 8; ++i) sc[i] = ((i & 3) + 16 * (i >> 2) <= qi) ? sc[i] : -1e9f;
            float mx = vmax3(vmax3(vmax3(sc[0], sc[1], sc[2]), vmax3(sc[3], sc[4], sc[5]), vmax(sc[6], sc[7])), sc[8], sc[9]);
            mx = xmax(mx) * -1.4426950408889634f;
#pragma unroll
            for (int i = 0; i < 10; ++i) sc[i] = __builtin_amdgcn_exp2f(__builtin_fmaf(sc[i], 1.4426950408889634f, mx));
            const float inv = __builtin_amdgcn_rcpf(xsum((((sc[0] + sc[1]) + (sc[2] + sc[3])) + ((sc[4] + sc[5]) + (sc[6] + sc[7]))) + (sc[8] + sc[9])));
            u32x4 xpx = {pk(sc[0] * inv, sc[1] * inv), pk(sc[2] * inv, sc[3] * inv), pk(sc[4] * inv, sc[5] * inv), pk(sc[6] * inv, sc[7] * inv)};
            u32x4 xpm = {pk(sc[8] * inv, sc[9] * inv), 0u, 0u, 0u};
            f32x4 o[8];
#pragma unroll
            for (int e = 0; e < 4; ++e) {
                u32x4 fa = {vb[2 * e].x, vb[2 * e].y, vb[2 * e + 1].x, vb[2 * e + 1].y}, fb = {vb[2 * e].z, vb[2 * e].w, vb[2 * e + 1].z, vb[2 * e + 1].w};
                u32x4 ma = vb[2 * e], mb = vb[2 * e + 1];
                ma.x = a[4 + (e >> 1)][2 * (e & 1)]; mb.x = a[4 + (e >> 1)][2 * (e & 1) + 1];
                asm volatile("s_nop 1" : "+v"(fa), "+v"(fb), "+v"(ma), "+v"(mb), "+v"(xpx), "+v"(xpm));
                MF0(o[2 * e], fa, xpx); MF0(o[2 * e + 1], fb, xpx); MF(o[2 * e], ma, xpm); MF(o[2 * e + 1], mb, xpm);
            }
            asm volatile("s_nop 7" : "+v"(o[0]), "+v"(o[1]), "+v"(o[2]), "+v"(o[3]), "+v"(o[4]), "+v"(o[5]), "+v"(o[6]), "+v"(o[7]));
#pragma unroll
            for (int e = 0; e < 4; ++e)
                st16(rw, l16, (u32x4){pk(o[2 * e][0], o[2 * e][1]), pk(o[2 * e][2], o[2 * e][3]), pk(o[2 * e + 1][0], o[2 * e + 1][1]), pk(o[2 * e + 1][2], o[2 * e + 1][3])}, loc(WS_O, (unsigned)((s * 2 + att) * 128 + hh * 4) * 1024u, cfg, 1) + e * 1024);
            SB();
            a_issue_v(min(s + 1, NSEG - 1));
        }
        if (ROLE == 3) {
            LAS float* sm = (LAS float*)(lds + L_A);
            const int tk = (lane >> 2) & 1, tok = 2 * (r & 7) + tk;
            f32x4 acc = __builtin_bit_cast(f32x4, a[0]);
#pragma unroll
            for (int kg = 1; kg < 8; ++kg) acc += __builtin_bit_cast(f32x4, a[kg]);
            float x1[4] = {bflo(xv.x), bfhi(xv.x), bflo(xv.y), bfhi(xv.y)};
            float sum = 0.f;
#pragma unroll
            for (int i = 0; i < 4; ++i) { x1[i] = rb(x1[i] + rb(cga[i] * rb(acc[i]))); sum += x1[i]; }
            st8(rw, tk * 1024 + d0 * 2, (u32x2){pk(x1[0], x1[1]), pk(x1[2], x1[3])}, loc(WS_X1, (s * 32 + tt * 16 + 2 * (r & 7)) * 1024, cfg, 1));
            sum += dpp<0xB1>(sum); sum += dpp<0x4E>(sum); sum += dpp<0x128>(sum); sum = xsum(sum);
            const float mw_ = sum * (1.f / 128);
            float var = 0.f;
#pragma unroll
            for (int i = 0; i < 4; ++i) { const float d = x1[i] - mw_; var += d * d; }
            var += dpp<0xB1>(var); var += dpp<0x4E>(var); var += dpp<0x128>(var); var = xsum(var);
            if (lane == 0 || lane == 4) { sm[tk * 8 + w] = mw_; sm[tk * 8 + 4 + w] = var; }
            bar();
            const f32x4 ms = *(LAS f32x4*)(sm + tk * 8), vs = *(LAS f32x4*)(sm + tk * 8 + 4);
            const float mean = ((ms[0] + ms[1]) + (ms[2] + ms[3])) * 0.25f;
            const f32x4 dm = ms - mean;
            var = (((vs[0] + vs[1]) + (vs[2] + vs[3])) + 128.f * ((dm[0] * dm[0] + dm[1] * dm[1]) + (dm[2] * dm[2] + dm[3] * dm[3]))) * (1.f / D);
            const float mb = rb(mean), sd = __builtin_amdgcn_rcpf(rb(__builtin_amdgcn_sqrtf(rb(rb(var) + 1e-5f))));
            float hh2[4];
#pragma unroll
            for (int i = 0; i < 4; ++i) hh2[i] = lnmod(x1[i], mb, sd, cgc[i], cgb[i]);
            st8(rw, ((d0 >> 5) & 3) * 1024 + tok * 64 + (d0 & 31) * 2, (u32x2){pk(hh2[0], hh2[1]), pk(hh2[2], hh2[3])}, loc(WS_H2, (unsigned)((s * 2 + tt) * 16 + 4 * w) * 1024u, cfg, 0));
        }
        if (ROLE == 5) asm volatile("s_waitcnt vmcnt(8)" ::: "memory"); else drain();
        bar();
        if (ROLE != 2 && ROLE != 4) stf(rw, t0o, s + 1, oflag, tg);
        if (doq) stf(rw, t0o, s + LEAD + 1, qflag, tg);
        seen = dec(sraw, tg);
        if (doq) qkv_in();
    };
    int s = 0;
    if (QD) for (; s < NSEG - LEAD; ++s) iter(s, bool_c<QD != 0>{});
    if (ROLE != 4) for (; s < NSEG; ++s) iter(s, bool_c<false>{});
    if (ROLE == 2 && r == 0 && tid == 0) *(unsigned*)(p.ws + WS_EPOCH) = tg.e;
}

__global__ void __launch_bounds__(256, 1) k_main(Params p) {
    extern __shared__ __attribute__((aligned(16))) char smem[];
    const lptr lds = (lptr)smem;
    const int b = blockIdx.x, tid = threadIdx.x;
    if (b < 128 && (b & 7) < 4) chain_role(p, lds, (b >> 3) * 4 + (b & 3), tid);
    else {
        const int u = (b >> 3) * 4 + (b & 3);
        const int j = (b & 4) ? (u < 64 ? u : u + 56) : (u < 120 ? u : u + 64);
        if (j < 64) stream_role<0>(p, lds, j, tid);
        else if (j < 120) stream_role<1>(p, lds, j, tid);
        else if (j < 152) stream_role<2>(p, lds, j, tid);
        else if (j < 168) stream_role<3>(p, lds, j, tid);
        else if (j < 184) stream_role<5>(p, lds, j, tid);
        else stream_role<4>(p, lds, j, tid);
    }
}

extern "C" void kernel_launch(void* const* d_in, const int* in_sizes, int n_in, void* d_out, int out_size, void* d_ws, size_t ws_size, hipStream_t stream) {
    Params p;
    p.x = (const u16*)d_in[0]; p.mem0 = (const u16*)d_in[1]; p.Wq = (const u16*)d_in[2]; p.Wk = (const u16*)d_in[3]; p.Wv = (const u16*)d_in[4]; p.Wo = (const u16*)d_in[5];
    p.wg = (const u16*)d_in[8]; p.wu = (const u16*)d_in[9]; p.wd = (const u16*)d_in[10]; p.c = (const u16*)d_in[11]; p.wmod = (const u16*)d_in[12];
    p.out = (u16*)d_out; p.ws = (char*)d_ws + ((0x200000 - ((uintptr_t)d_ws & 0x1FFFFF)) & 0x1FFFFF);
    constexpr int LDSB = 140 * 1024;
    (void)hipFuncSetAttribute((const void*)k_main, hipFuncAttributeMaxDynamicSharedMemorySize, LDSB);
    k_main<<<256, 256, LDSB, stream>>>(p);
}
```

```cpp
#define CODEW
#define NOADAPT
#define MUP 0
#define TX 64
#define TY 46
#include <hip/hip_runtime.h>
#include <hip/hip_bf16.h>
#include <stdint.h>
#include <stdio.h>

typedef unsigned short u16;
typedef __bf16 bf16x2 __attribute__((ext_vector_type(2)));
typedef __bf16 bf16x8 __attribute__((ext_vector_type(8)));
typedef short s16x4 __attribute__((ext_vector_type(4)));
typedef float f32x4 __attribute__((ext_vector_type(4)));
typedef float f32x2 __attribute__((ext_vector_type(2)));
typedef unsigned u32x2 __attribute__((ext_vector_type(2)));
typedef unsigned u32x4 __attribute__((ext_vector_type(4)));
typedef unsigned u32x8 __attribute__((ext_vector_type(8)));
#define LAS __attribute__((address_space(3)))
typedef LAS char* lptr;
#define DI __device__ __forceinline__

#define TV1 8
#define TV2 8
#define TPEN 20
#define TM 10
#define SIDE 0
#define SLP 5
#define PREP 5
#define FDLY 0
#define CX 2150
#define CY 430
#define PSL 1
#define TS_ 6
#ifndef NVX
#define NVX 2
#endif
#define FW ""
#define PRE 2
#define PDLY 3
#define NPRE 4
#define LEAD 16

constexpr int S = 2048, D = 512, NSEG = 64, F = 1792, HD = 4096;
constexpr unsigned SENT = 0xFFFFFFFFu;
constexpr int NRING = 3;
constexpr unsigned WS_MOD = 0;
constexpr unsigned WS_FLAG = 131072;
constexpr unsigned FL_ARM = 64, FL_QKV = 128, FL_L = 320, FL_KM = 384, FL_B = 448, FL_C = 512, FL_A = 576, FL_G = 600, FL_N = 640;
constexpr unsigned WS_EPOCH = 12544;
constexpr unsigned WS_EARLY = 16384, EF_PROBE = 0, EF_MOD = 16, EF_LN = 64, EF_MOD2 = 128, WS_MODG = 65536;
#define NC 4
constexpr unsigned WS_HO = 220u << 20, HO_MEM = 256, MRS = NC + 1, HO_N = HO_MEM + 3 * MRS;
constexpr unsigned WS_N = 1u << 20, WS_DBG = 3u << 20;
constexpr unsigned WS_H1 = 4u << 20, WS_X1 = 6u << 20, WS_H2 = 10u << 20, WS_G = 14u << 20, WS_O = 28u << 20, WS_QKV = 60u << 20, WS_PB = 156u << 20, WS_END = 246u << 20;

DI float bflo(unsigned x) { return __uint_as_float(x << 16); }
DI float bfhi(unsigned x) { return __uint_as_float(x & 0xffff0000u); }
DI unsigned pk(float lo, float hi) { bf16x2 v = {(__bf16)lo, (__bf16)hi}; return __builtin_bit_cast(unsigned, v); }
DI float rb(float x) { return (float)(__bf16)x; }
DI void bar() { asm volatile("s_waitcnt lgkmcnt(0)\n\ts_barrier" ::: "memory"); }
DI void drain() { asm volatile("s_waitcnt vmcnt(0)" ::: "memory"); }
DI u32x2 trread(lptr p) { return __builtin_bit_cast(u32x2, __builtin_amdgcn_ds_read_tr16_b64_v4i16((LAS s16x4*)p)); }
DI u32x4 tr2(lptr p, int off) { const u32x2 a = trread(p), b = trread(p + off); return (u32x4){a.x, a.y, b.x, b.y}; }
DI u32x4 ldsr128(lptr p) { return *(LAS u32x4*)p; }
DI void ldsw128(lptr p, u32x4 v) { *(LAS u32x4*)p = v; }
DI void ldsw64(lptr p, u32x2 v) { *(LAS u32x2*)p = v; }
DI void ldsw32(lptr p, unsigned v) { *(LAS unsigned*)p = v; }
DI f32x4 mfma32(u32x4 a, u32x4 b, f32x4 c) { return __builtin_amdgcn_mfma_f32_16x16x32_bf16(__builtin_bit_cast(bf16x8, a), __builtin_bit_cast(bf16x8, b), c, 0, 0, 0); }
DI float swadd32(float a, float b) {
    auto r = __builtin_amdgcn_permlane32_swap(__float_as_uint(a), __float_as_uint(b), false, false);
    return __uint_as_float(r[0]) + __uint_as_float(r[1]);
}
DI float vmax(float a, float b) { float r; asm("v_max_f32 %0, %1, %2" : "=v"(r) : "v"(a), "v"(b)); return r; }
DI float vmax3(float a, float b, float c) { float r; asm("v_max3_f32 %0, %1, %2, %3" : "=v"(r) : "v"(a), "v"(b), "v"(c)); return r; }
DI float xmax(float x) {
    auto s = __builtin_amdgcn_permlane16_swap(__float_as_uint(x), __float_as_uint(x), false, false);
    x = vmax(__uint_as_float(s[0]), __uint_as_float(s[1]));
    auto t = __builtin_amdgcn_permlane32_swap(__float_as_uint(x), __float_as_uint(x), false, false);
    return vmax(__uint_as_float(t[0]), __uint_as_float(t[1]));
}
DI float xsum(float x) {
    auto s = __builtin_amdgcn_permlane16_swap(__float_as_uint(x), __float_as_uint(x), false, false);
    x = __uint_as_float(s[0]) + __uint_as_float(s[1]);
    auto t = __builtin_amdgcn_permlane32_swap(__float_as_uint(x), __float_as_uint(x), false, false);
    return __uint_as_float(t[0]) + __uint_as_float(t[1]);
}
template <int CTRL> DI float dpp(float x) { return __builtin_bit_cast(float, __builtin_amdgcn_mov_dpp(__builtin_bit_cast(int, x), CTRL, 0xf, 0xf, true)); }
template <int CTRL> DI int dppi(int x) { return __builtin_amdgcn_mov_dpp(x, CTRL, 0xf, 0xf, true); }
DI unsigned mx4(u32x4 v, unsigned m) { return max(max(max(v.x, v.y), max(v.z, v.w)), m); }
typedef __amdgpu_buffer_rsrc_t rsrc_t;
DI rsrc_t mkrs(const void* p, unsigned bytes) { return __builtin_amdgcn_make_buffer_rsrc((void*)p, 0, bytes, 0x00020000); }
DI u32x4 ld16(rsrc_t r, unsigned off, unsigned soff = 0) { return __builtin_amdgcn_raw_buffer_load_b128(r, off, soff, 16); }
DI u32x2 ld8(rsrc_t r, unsigned off, unsigned soff) { return __builtin_amdgcn_raw_buffer_load_b64(r, off, soff, 16); }
DI unsigned ld4(rsrc_t r, unsigned off, unsigned soff = 0) { return __builtin_amdgcn_raw_buffer_load_b32(r, off, soff, 16); }
DI void st16(rsrc_t r, unsigned off, u32x4 v, unsigned soff = 0) { __builtin_amdgcn_raw_buffer_store_b128(v, r, off, soff, 16); }
DI void st8(rsrc_t r, unsigned off, u32x2 v, unsigned soff) { __builtin_amdgcn_raw_buffer_store_b64(v, r, off, soff, 16); }
DI void st4(rsrc_t r, unsigned off, unsigned v, unsigned soff = 0) { __builtin_amdgcn_raw_buffer_store_b32(v, r, off, soff, 16); }
DI u32x4 ldw(rsrc_t r, unsigned voff, unsigned soff) { return __builtin_amdgcn_raw_buffer_load_b128(r, voff, soff, 0); }
DI unsigned rt() { return (unsigned)__builtin_amdgcn_s_memrealtime(); }
DI void wait_rt(unsigned t, unsigned q = 1) {
    int d;
    do d = (int)(t - rt()); while (d >= (int)q && d < 4096);
    d = min(d, 8);
    for (; d > 0; --d) asm volatile("");
    asm volatile("" ::: "memory");
}

template <int V> struct int_c { static constexpr int value = V; };
template <bool V> struct bool_c { static constexpr bool value = V; };
DI unsigned loc(unsigned base, unsigned off, unsigned cfg, unsigned far) { const unsigned x = base + ((off >> 13) << 14) + (off & 8191); return x + ((((cfg >> ((x >> 21) & 1)) ^ far) & 1) << 13); }
DI unsigned qkv_off(unsigned i, unsigned sec, unsigned cfg) { return loc(WS_QKV, (i * 3 + sec) * 8192u, cfg, 1); }
DI unsigned flg(unsigned line, unsigned cfg) { return loc(WS_FLAG, line * 128, cfg, line >= FL_KM); }
DI unsigned kvm_off(unsigned ho, unsigned cfg, unsigned i) { return ho + (8u << 20) + (i >> 1) * 16384 + ((cfg >> ((i >> 8) & 1)) & 1) * 8192 + (i & 1) * 4096; }
struct Tag { unsigned e, t; };
DI Tag get_tag(const char* ws) { const unsigned e = *(const __attribute__((address_space(4))) unsigned*)(uintptr_t)(ws + WS_EPOCH) + 1; return Tag{e, e * 0x9E3779B1u}; }
DI unsigned dec(u32x2 v, Tag tg) { const unsigned c = v.x - tg.t; return (v.y == tg.e && c < 0x100000u) ? c : 0u; }
DI unsigned ldf(rsrc_t r, unsigned off, Tag tg) { return dec(ld8(r, off, 0), tg); }
DI void stf(rsrc_t r, unsigned voff, unsigned v, unsigned soff, Tag tg) { st8(r, voff, (u32x2){v + tg.t, tg.e}, soff); }
struct Params {
    const u16 *x, *mem0, *Wq, *Wk, *Wv, *Wo, *wg, *wu, *wd, *c, *wmod;
    u16* out; char* ws;
};

DI float lnmod(float v, float mb, float isd, float t, float sh) { return rb(rb(rb(v - mb) * isd) * t) + sh; }

DI unsigned wait_words(rsrc_t rw, Tag tg, unsigned off, unsigned need, unsigned& cap) {
    unsigned v;
    for (unsigned spins = 0;; ++spins) {
        asm volatile("" ::: "memory");
        v = ldf(rw, off, tg);
        if (__all(v >= need)) break;
        if (spins > cap) { cap = 0; break; }
        __builtin_amdgcn_s_sleep(2);
    }
    asm volatile("" ::: "memory");
    return v;
}
DI unsigned get_cfg(rsrc_t rw, Tag tg, int lane, unsigned& cap) {
    int v = wait_words(rw, tg, WS_EARLY + (EF_PROBE + (lane & 15)) * 128, 1, cap);
    v = min(v, dppi<0x4E>(v)); v = min(v, dppi<0x124>(v)); v = min(v, dppi<0x128>(v));
    return __builtin_amdgcn_readlane(v, 1) < __builtin_amdgcn_readlane(v, 0) ? 1u : 2u;
}

constexpr int PSTR = 272;
DI void panel_issue(const u16* srcA, const u16* srcB, int ld, int tid, u32x4 (&v)[32]) {
    const rsrc_t ra = mkrs(srcA, (511 * ld + 64) * 2), rb_ = mkrs(srcB, (511 * ld + 64) * 2);
    const unsigned voff = ((tid >> 3) * ld + (tid & 7) * 8) * 2;
#pragma unroll
    for (int i = 0; i < 16; ++i) { v[2 * i] = ldw(ra, voff, i * 32 * ld * 2); v[2 * i + 1] = ldw(rb_, voff, i * 32 * ld * 2); }
}
DI void panel_finish(lptr lds, int tid, int colA, int colB, u32x4 (&v)[32], u32x4 (&wa)[16], u32x4 (&wb)[16]) {
    const int lane = tid & 63, g = lane >> 4, q = (lane & 15) >> 2;
    const lptr wp = lds + (tid >> 3) * PSTR + (tid & 7) * 16;
#pragma unroll
    for (int half = 0; half < 2; ++half) {
        bar();
#pragma unroll
        for (int i = 0; i < 8; ++i) { ldsw128(wp + i * 32 * PSTR, v[16 * half + 2 * i]); ldsw128(wp + i * 32 * PSTR + 128, v[16 * half + 2 * i + 1]); }
        bar();
        lptr pa = lds + (8 * g + q) * PSTR + colA * 2, pb = lds + (8 * g + q) * PSTR + colB * 2;
#pragma unroll
        for (int ks = 0; ks < 8; ++ks) { wa[half * 8 + ks] = tr2(pa + ks * 32 * PSTR, 4 * PSTR); wb[half * 8 + ks] = tr2(pb + ks * 32 * PSTR, 4 * PSTR); asm volatile("" : "+a"(wa[half * 8 + ks]), "+a"(wb[half * 8 + ks])); }
    }
    bar();
}
constexpr int L_A = 0, L_Q = 73728;
#define MFWA(acc, wgt, x) asm volatile("v_mfma_f32_16x16x32_bf16 %0, %1, %2, %0" : "+v"(acc) : "a"(wgt), "v"(x))
#define MFWB(acc, wgt, x) asm volatile("v_mfma_f32_16x16x32_bf16 %0, %2, %1, %0" : "+v"(acc) : "a"(wgt), "v"(x))
template <bool SWAP> DI void tile_mma(lptr lds, int lane, const u32x4 (&wa)[16], const u32x4 (&wb)[16], f32x4& ca, f32x4& cb) {
    ca = (f32x4){0, 0, 0, 0}; cb = ca;
    u32x4 a[16];
#pragma unroll
    for (int ks = 0; ks < 4; ++ks) a[ks] = ldsr128(lds + ks * 1024 + lane * 16);
    asm volatile("s_nop 1" : "+v"(ca), "+v"(cb));
    __builtin_amdgcn_sched_barrier(0);
#pragma unroll
    for (int ks = 0; ks < 16; ++ks) {
        if (SWAP) MFWB(ca, wa[ks], a[ks]); else MFWA(ca, wa[ks], a[ks]);
        if (ks + 4 < 16) a[ks + 4] = ldsr128(lds + (ks + 4) * 1024 + lane * 16);
        __builtin_amdgcn_sched_barrier(0);
        if (SWAP) MFWB(cb, wb[ks], a[ks]); else MFWA(cb, wb[ks], a[ks]);
    }
    asm volatile("s_nop 7" : "+v"(ca), "+v"(cb));
}

constexpr int CSTR = 656, OSTR = 1040;
constexpr int L_MEMF = 0, L_QT = 8192, L_KT = 12288, L_VT = 16384, L_RED = 20480, L_TS = 21504, L_SEL = 23552, L_ONES = 24576, L_QKV = 25600;
constexpr float SC_L2E = 0.08838834764831845f * 1.4426950408889634f;
constexpr unsigned OOB = 0x80000000u;
#define SMFA(acc, a, w) asm volatile("v_smfmac_f32_16x16x64_bf16 %0, %1, %2, %3" : "+v"(acc) : "v"(a), "a"(w), "v"(sidx))
#define SMFV(acc, a, w) asm volatile("v_smfmac_f32_16x16x64_bf16 %0, %1, %2, %3" : "+v"(acc) : "v"(a), "v"(w), "v"(sidx))
#define MF0(acc, a, b) asm volatile("v_mfma_f32_16x16x32_bf16 %0, %1, %2, 0" : "=&v"(acc) : "v"(a), "v"(b))
#define MF(acc, a, b) asm volatile("v_mfma_f32_16x16x32_bf16 %0, %1, %2, %0" : "+v"(acc) : "v"(a), "v"(b))
#define NOP2() asm volatile("s_nop 1")

#define SB() __builtin_amdgcn_sched_barrier(0)
DI f32x4 zero4() { f32x2 a, b; asm volatile("v_mov_b64 %0, 0\n\tv_mov_b64 %1, 0" : "=v"(a), "=v"(b)); return (f32x4){a.x, a.y, b.x, b.y}; }
DI u32x4 sent4() { u32x2 a, b; asm volatile("v_mov_b64 %0, -1\n\tv_mov_b64 %1, -1" : "=v"(a), "=v"(b)); return (u32x4){a.x, a.y, b.x, b.y}; }
DI float vadd(float a, float b) { float r; asm volatile("v_add_f32 %0, %1, %2" : "=v"(r) : "v"(a), "v"(b)); return r; }
DI unsigned vcvt(float a, float b) { unsigned r; asm volatile("v_cvt_pk_bf16_f32 %0, %1, %2" : "=v"(r) : "v"(a), "v"(b)); return r; }
template <int O0, int O1> DI void dsw2(lptr p, unsigned a, unsigned b) { asm volatile("ds_write2_b32 %0, %1, %2 offset0:%3 offset1:%4" :: "v"(p), "v"(a), "v"(b), "i"(O0), "i"(O1) : "memory"); }
#define ST(k)
#define ST2(k)
#define TS(k)
#define SU(id, k)
DI int wave_max_rel(unsigned v, unsigned base) {
    int d = (int)(v - base);
    d = max(d, dppi<0xB1>(d)); d = max(d, dppi<0x4E>(d)); d = max(d, dppi<0x124>(d)); d = max(d, dppi<0x128>(d));
    auto s = __builtin_amdgcn_permlane16_swap((unsigned)d, (unsigned)d, false, false); d = max((int)s[0], (int)s[1]);
    auto t = __builtin_amdgcn_permlane32_swap((unsigned)d, (unsigned)d, false, false); d = max((int)t[0], (int)t[1]);
    return __builtin_amdgcn_readfirstlane(d);
}

DI int lane_id() { int l; asm volatile("v_mbcnt_lo_u32_b32 %0, -1, 0\n\tv_mbcnt_hi_u32_b32 %0, -1, %0" : "=v"(l)); return l; }
DI void chain_role(const Params& p, lptr lds, const int b, const int tid0) {
    const int w = __builtin_amdgcn_readfirstlane(tid0 >> 6);
    const int h = (b & 3) + 4 * (b >> 3), hf = (b >> 2) & 1;
    u32x8 wq[2][8], wk[2][8], wv[8], wo[8];
    u32x4 kv0[3];
    unsigned quant;
    const Tag tg = get_tag(p.ws);
    unsigned cap = 1u << 17, cfg, ho0, ho1;
    constexpr unsigned ho = WS_HO;
    auto pblk = [&](unsigned ring_, unsigned r) { return (ring_ == 2 ? ho1 : ho0) + (ring_ * 64 + r) * 16384; };
    {
        const int tid = tid0;
        constexpr int WSTR = 272;
        u32x4 v[3][16];
        const unsigned vo = ((tid >> 3) * HD + (tid & 7) * 8) * 2;
        auto issue = [&](int r, u32x4 (&d)[16]) {
            if (r < 5) {
                const rsrc_t rs = mkrs((r < 2 ? p.Wq : r < 4 ? p.Wk : p.Wv) + h * 128 + (r < 4 ? (r & 1) : hf) * 64, (511 * HD + 64) * 2);
#pragma unroll
                for (int i = 0; i < 16; ++i) d[i] = ldw(rs, vo, (r < 4 ? (i ^ (8 * hf)) : i) * 32 * HD * 2);
            } else {
                const rsrc_t rs = mkrs(p.Wo + (size_t)(h * 128 + hf * 64) * D, 65536);
#pragma unroll
                for (int i = 0; i < 16; ++i) d[i] = ldw(rs, lane_id() * 16, w * 1024 + i * 4096);
            } };
        unsigned m0w;
        { const int c = 8 * (b & 1) + ((tid >> 2) & 7); m0w = *(const unsigned*)(p.mem0 + (2 * (c >> 2) + (c & 1)) * D + 16 * (b >> 1) + 4 * (tid & 3) + (c & 2)); }
        issue(0, v[0]); issue(1, v[1]); issue(2, v[2]);
        SB();
        { const unsigned a = rt(); unsigned c, e; do c = rt(); while (c == a); do e = rt(); while (e == c); quant = min(e - c, 8u); }
        const int lane = lane_id();
        const lptr wp = lds + (w * 8 + (lane >> 3)) * WSTR + (lane & 7) * 16;
        const int g = lane >> 4, q = (lane & 15) >> 2, pp = lane & 3;
        auto stage = [&](u32x4 (&d)[16], int half) {
#pragma unroll
            for (int i = 0; i < 16; ++i) ldsw128(wp + (i < 8 ? 1 : -1) * hf * 256 * WSTR + i * 32 * WSTR + half * 128, d[i]);
        };
        auto frag = [&](int j, int colbyte) { u32x8 f;
            const lptr base = lds + (((2 * w + j) & 7) * 64 + 8 * g + q) * WSTR + colbyte;
#pragma unroll
            for (int m = 0; m < 4; ++m) { const u32x2 t = trread(base + (32 * (m >> 1) + 4 * (m & 1)) * WSTR); f[2 * m] = t.x; f[2 * m + 1] = t.y; }
            return f; };
        auto take = [&](u32x8 (&dst)[2][8]) {
#pragma unroll
            for (int j = 0; j < 8; ++j) { dst[0][j] = frag(j, w * 64 + 8 * pp); dst[1][j] = frag(j, w * 64 + 32 + 8 * pp); asm volatile("" : "+a"(dst[0][j]), "+a"(dst[1][j])); } };
        stage(v[0], 0); issue(3, v[0]); stage(v[1], 1); issue(4, v[1]); bar(); take(wq); bar();
        {
            const rsrc_t rw = mkrs(p.ws, WS_END);
            cfg = get_cfg(rw, tg, lane, cap);
            ho0 = ho + (cfg & 1) * 8192; ho1 = ho + ((cfg >> 1) & 1) * 8192;
            const u32x4 z = sent4();
#pragma unroll
            for (int r = 0; r < 3; ++r) { st16(rw, lane * 16, z, pblk(r, b) + w * 2048); st16(rw, lane * 16, z, pblk(r, b) + w * 2048 + 1024); }
            const unsigned mso = w < 2 ? (2 * w + (lane >> 5)) * 16384 + (lane & 31) * 4 : b == 0 ? NC * 16384 + (w - 2) * 256 + lane * 4 : OOB;
            st4(rw, mso, w < 2 ? min(m0w, SENT - 1) : 0u, ho0 + HO_MEM * 16384 + b * 128);
            st4(rw, mso, SENT, ho0 + (HO_MEM + MRS) * 16384 + b * 128); st4(rw, mso, SENT, ho0 + (HO_MEM + 2 * MRS) * 16384 + b * 128);
            SB();
        }
        stage(v[2], 0); issue(5, v[2]); stage(v[0], 1);
        asm volatile("s_waitcnt vmcnt(16)" ::: "memory");
        bar();
        stf(mkrs(p.ws, WS_END), lane + w == 0 ? 0u : OOB, 1, flg(FL_ARM + b, cfg), tg);
        take(wk);
        {
            const rsrc_t rw = mkrs(p.ws, WS_END);
            const unsigned fa = flg(FL_ARM + lane, cfg), fq = flg(FL_QKV + ((lane & 2) ? 64 + (h & 3) + 8 * (h >> 2) + 4 * (lane & 1) : 32 + h), cfg);
            for (unsigned spins = 0;; ++spins) {
                asm volatile("" ::: "memory");
                const u32x2 a = ld8(rw, fa, 0), c = ld8(rw, fq, 0);
                if (__all(min(dec(a, tg), dec(c, tg)) >= 1)) break;
                if (spins > cap) { cap = 0; break; }
                __builtin_amdgcn_s_sleep(1);
            }
            asm volatile("" ::: "memory");
            const unsigned kb_ = qkv_off(h, 1, cfg) + w * 2048;
            kv0[0] = ld16(rw, lane * 16, kb_); kv0[1] = ld16(rw, lane * 16, kb_ + 1024); kv0[2] = ld16(rw, lane * 16, qkv_off(h, 2, cfg) + hf * 4096 + w * 1024);
            SB();
        }
        bar();
        constexpr int VSTR = 144, L_WO = 512 * VSTR;
#pragma unroll
        for (int i = 0; i < 16; ++i) { ldsw128(lds + (w * 8 + (lane >> 3) + i * 32) * VSTR + (lane & 7) * 16, v[1][i]); ldsw128(lds + L_WO + (w + 4 * i) * OSTR + lane * 16, v[2][i]); }
        bar();
#pragma unroll
        for (int j = 0; j < 8; ++j) {
            const lptr vb_ = lds + (((2 * w + j) & 7) * 64 + 8 * g + q) * VSTR + (32 * (w >> 1) + 8 * pp + 4 * (w & 1)) * 2;
#pragma unroll
            for (int m = 0; m < 4; ++m) { const u32x2 t = trread(vb_ + (32 * (m >> 1) + 4 * (m & 1)) * VSTR); wv[j][2 * m] = t.x; wv[j][2 * m + 1] = t.y; }
        }
        lptr base = lds + L_WO + (32 * (q >> 1) + 4 * g + (q & 1)) * OSTR;
#pragma unroll
        for (int t = 0; t < 8; ++t) {
            const int col = 128 * w + 64 * (t >> 2) + 16 * pp + 4 * (t & 3);
#pragma unroll
            for (int m = 0; m < 4; ++m) { const u32x2 tv = trread(base + (16 * (m >> 1) + 2 * (m & 1)) * OSTR + col * 2); wo[t][2 * m] = tv.x; wo[t][2 * m + 1] = tv.y; }
        }
        bar();
    }
    const int lane = lane_id(), tid = w * 64 + lane, n = lane & 15, g = lane >> 4;
    const rsrc_t rw = mkrs(p.ws, WS_END);
    const int hi = (n >> 1) & 1, rowi = 2 * (n >> 2) + (n & 1);
    const int sidx = hi ? 0xEEEE : 0x4444;
    const unsigned l16 = lane * 16;
    const unsigned xc = (b & 3) + 4 * ((b >> 2) % (NC / 4));
    const unsigned t0o = tid == 0 ? 0u : OOB;
    auto qkv_issue = [&](int s, u32x4 (&v)[3]) {
        const unsigned kb_ = qkv_off(s * 32 + h, 1, cfg) + w * 2048;
        v[0] = ld16(rw, l16, kb_); v[1] = ld16(rw, l16, kb_ + 1024);
        v[2] = ld16(rw, l16, qkv_off(s * 32 + h, 2, cfg) + hf * 4096 + w * 1024);
    };
    auto qkv_store = [&](int s, u32x4 (&v)[3]) {
        lptr d = lds + L_QKV + (s & 1) * 12288 + lane * 16;
        ldsw128(d + w * 2048, v[0]); ldsw128(d + w * 2048 + 1024, v[1]);
        ldsw64(d + 8192 + (w >> 1) * 2048 + (w & 1) * 8, (u32x2){v[2].x, v[2].y}); ldsw64(d + 8192 + (w >> 1) * 2048 + 1024 + (w & 1) * 8, (u32x2){v[2].z, v[2].w});
    };
    const unsigned qflag = flg(FL_QKV + ((lane & 2) ? 64 + (h & 3) + 8 * (h >> 2) + 4 * (lane & 1) : 32 + h), cfg), kflag = __builtin_amdgcn_readfirstlane(flg(FL_KM + b, cfg)), gflag = __builtin_amdgcn_readfirstlane(flg(FL_G, cfg));
    auto qkv_wait = [&](unsigned need) { for (unsigned spins = 0;; ++spins) { asm volatile("" ::: "memory"); if (__all(ldf(rw, qflag, tg) >= need)) break; if (spins > cap) { cap = 0; break; } __builtin_amdgcn_s_sleep(1); } asm volatile("" ::: "memory"); };
    SU(b, 0);
    qkv_store(0, kv0);
    const lptr qtw = lds + L_QT + w * 1024 + (n >> 3) * 256 + (((n & 7) * 32) ^ ((n >> 3) * 128)) + g * 8;
    const lptr tr0 = lds + L_QT + g * 256 + (n >> 2) * 32 + (lane & 3) * 8 + (g & 1) * 128, tr1 = tr0 + ((g & 1) ? -128 : 128);
    auto trf = [&](int off) { const u32x2 a = trread(tr0 + off), c = trread(tr1 + off); return (u32x4){a.x, a.y, c.x, c.y}; };
    const lptr mw = lds + L_MEMF + tid * 16 + w * 1024, tsw = lds + L_TS + tid * 4, vtw = lds + L_VT + tid * 4, vtr = lds + L_VT + lane * 4;
    const lptr rdw = lds + L_RED + (w * 64 + 32 * ((n >> 2) & 1) + 16 * ((n >> 1) & 1) + (n & 1) + 4 * (g & 1)) * 4, rdr = lds + L_RED + (lane & 31) * 8;
    const unsigned tso = w == 3 ? l16 : OOB, sco = w == 3 ? (lane & 1) * 4 : OOB, mso = w < 2 ? (2 * w + (lane >> 5)) * 16384 + (lane & 31) * 4 : b == 0 ? NC * 16384 + (w - 2) * 256 + lane * 4 : OOB,
         kso = (hf | hi) ? OOB : (rowi * 4 + g) * 16, pvo = (2 * (n >> 2) + (g >> 1)) * 16384 + ((n & 3) + 4 * (g & 1)) * 16;
    if (w == 1) ldsw128(lds + L_ONES + lane * 16, (u32x4){0x3f803f80u, 0x3f803f80u, 0x3f803f80u, 0x3f803f80u});
    if (w == 0) { const unsigned one = 0x3f80u << (16 * (n & 1)), k2 = (n >> 1) & 3; ldsw128(lds + L_SEL + lane * 16, (u32x4){k2 == 0 ? one : 0u, k2 == 1 ? one : 0u, k2 == 2 ? one : 0u, k2 == 3 ? one : 0u}); }
    if (tid == 0) *(LAS unsigned*)(lds + L_TS + 1024) = 0;
    const unsigned long long himask = 0xCCCCCCCCCCCCCCCCull;
    bar();
    auto look_at = [&](int sg) { const unsigned o = qkv_off(sg * 32 + h, w == 0 ? 1 : 2, cfg) + (w == 0 ? 0 : hf * 4096); return w >= 2 ? OOB : (w == 1 && lane >= 32) ? qflag : o + l16 * 8; };
    unsigned tvo = look_at(2);
    unsigned pold = pblk(2, b) + w * 2048 + 1024;
    unsigned T2 = rt(), T1 = 0, ring = 0;
    unsigned pc = (unsigned)__builtin_amdgcn_s_memtime(), pt = T2, X = TX + CX / 24, Y = TY + CY / 24, mg = 0;
    auto step = [&](const int s, auto last_) {
        constexpr bool LAST = decltype(last_)::value;
        f32x4 aq0, aq1, ak0, ak1, av;
        const unsigned rnext = ring == NRING - 1 ? 0 : ring + 1;
        unsigned mo = ho0 + (HO_MEM + ring * MRS + xc) * 16384 + w * 2048, mo1 = mo + 1024, to = ho0 + (HO_MEM + ring * MRS + NC) * 16384 + xc * 128;
        asm volatile("" : "+s"(mo), "+s"(mo1), "+s"(to));
        u32x4 A[8], kx[2][4], qf[4], kf[4], vx[4], ones; unsigned tsv = 0;
        wait_rt(T2, quant);
        ST(0);
        A[0] = ld16(rw, l16, mo); A[1] = ld16(rw, l16, mo1); tsv = ld4(rw, sco, to);
        SB();
        SB();
        lptr qb = lds + L_QKV + (s & 1) * 12288 + lane * 16;
        lptr ar0 = lds + L_MEMF + lane * 16 + ((2 * w + 2) & 7) * 1024, ar1 = lds + L_MEMF + lane * 16 + ((2 * w + 4) & 7) * 1024, ar2 = lds + L_MEMF + lane * 16 + ((2 * w + 6) & 7) * 1024;
        asm volatile("" : "+v"(qb), "+v"(ar0), "+v"(ar1), "+v"(ar2));
        if constexpr (!LAST) {
#pragma unroll
            for (int ks = 0; ks < NPRE; ++ks) { kx[0][ks] = ldsr128(qb + ks * 1024); kx[1][ks] = ldsr128(qb + 4096 + ks * 1024); }
#pragma unroll
            for (int T = 0; T < NVX; ++T) vx[T] = ldsr128(qb + 8192 + T * 1024);
        }
        aq0 = zero4(); aq1 = zero4(); ak0 = zero4(); ak1 = zero4(); av = zero4();
        if constexpr (!LAST) {
#pragma unroll
            for (int ks = 0; ks < NPRE; ++ks) asm volatile("" : "+v"(kx[0][ks]), "+v"(kx[1][ks]));
#pragma unroll
            for (int T = 0; T < NVX; ++T) asm volatile("" : "+v"(vx[T]));
        }
        SB();
        ldsw128(mw, A[0]); ldsw128(mw + 1024, A[1]); ldsw32(tsw, tsv);
        SB();
        if (__builtin_expect(!__all(mx4(A[0], mx4(A[1], tsv)) != SENT), 0)) {
            for (unsigned spins = 0;; ++spins) {
                asm volatile("" ::: "memory");
                A[0] = ld16(rw, l16, mo); A[1] = ld16(rw, l16, mo1); tsv = ld4(rw, sco, to);
                if (__all(mx4(A[0], mx4(A[1], tsv)) != SENT)) break;
                if (spins > cap) { cap = 0; break; }
            }
            ldsw128(mw, A[0]); ldsw128(mw + 1024, A[1]); ldsw32(tsw, tsv);
        }
        ST(1);
        TS(0);
        NOP2();
        if constexpr (!LAST) { SMFA(aq0, A[0], wq[0][0]); SMFA(aq1, A[0], wq[1][0]); }
        SMFA(ak0, A[0], wk[0][0]);
        bar();
        ST2(9);
        TS(1);
        A[2] = ldsr128(ar0); A[3] = ldsr128(ar0 + 1024); SB();
        SMFA(ak1, A[0], wk[1][0]);
        A[4] = ldsr128(ar1); A[5] = ldsr128(ar1 + 1024); SB();
        SMFV(av, A[0], wv[0]);
        A[6] = ldsr128(ar2); A[7] = ldsr128(ar2 + 1024); SB();
        u32x2 tm = {0, 0}, look = {0, 0};
#define SM_(a, w) "v_smfmac_f32_16x16x64_bf16 %0, %" #a ", %" #w ", %15\n\t"
#define POST6(C, acc, W, src, dst, MID, WR) { float t0_, t1_; asm volatile( \
                SM_(3, 9) SM_(4, 10) SM_(5, 11) "v_add_f32 %1, %16, %18\n\t" SM_(6, 12) "v_add_f32 %2, %17, %19\n\t" SM_(7, 13) \
                "v_cvt_pk_bf16_f32 %1, %1, %2\n\t" MID SM_(8, 14) "ds_write2_b32 %20, %1, %1 " WR \
                : "+v"(acc), "=&v"(t0_), "=&v"(t1_) : "v"(A[2]), "v"(A[3]), "v"(A[4]), "v"(A[5]), "v"(A[6]), "v"(A[7]), C(W[2]), C(W[3]), C(W[4]), C(W[5]), C(W[6]), C(W[7]), "v"(sidx), \
                  "v"(src[0]), "v"(src[1]), "v"(src[2]), "v"(src[3]), "v"(dst) : "memory"); }
#define POST3(C, acc, K, W, src, dst, WR) { float t0_, t1_; asm volatile( \
                "v_add_f32 %1, %10, %12\n\tv_smfmac_f32_16x16x64_bf16 %0, %3, %6, %9\n\tv_add_f32 %2, %11, %13\n\tv_smfmac_f32_16x16x64_bf16 %0, %4, %7, %9\n\t" \
                "v_cvt_pk_bf16_f32 %1, %1, %2\n\tv_smfmac_f32_16x16x64_bf16 %0, %5, %8, %9\n\tds_write2_b32 %14, %1, %1 " WR \
                : "+v"(acc), "=&v"(t0_), "=&v"(t1_) : "v"(A[K]), "v"(A[K + 1]), "v"(A[K + 2]), C(W[K]), C(W[K + 1]), C(W[K + 2]), "v"(sidx), \
                  "v"(src[0]), "v"(src[1]), "v"(src[2]), "v"(src[3]), "v"(dst) : "memory"); }
        if constexpr (!LAST) {
            SMFA(aq0, A[1], wq[0][1]);
            SMFA(aq1, A[1], wq[1][1]);
            tm = *(LAS u32x2*)(lds + L_TS + 768); SB();
            SMFA(ak0, A[1], wk[0][1]);
            SMFA(ak1, A[1], wk[1][1]); SMFV(av, A[1], wv[1]);
            TS(2);
            SMFA(aq0, A[2], wq[0][2]); SMFA(aq0, A[3], wq[0][3]); SMFA(aq0, A[4], wq[0][4]); SMFA(aq0, A[5], wq[0][5]);
            look = ld8(rw, tvo, 0); SB();
            SMFA(aq0, A[6], wq[0][6]);
            if (__builtin_expect(b == 0, 0)) {
                const u32x2 sv = {s + tg.t, tg.e};
                asm volatile("buffer_store_dwordx2 %0, %1, %2, %3 offen sc1" :: "v"(sv), "v"(t0o), "s"(rw), "s"(gflag) : "memory");
            }
            SMFA(aq0, A[7], wq[0][7]);
            POST6("a", aq1, wq[1], aq0, qtw, "", "offset0:0 offset1:1");
            TS(3);
            POST6("a", ak0, wk[0], aq1, qtw, "", "offset0:128 offset1:129");
            POST6("a", ak1, wk[1], ak0, qtw + 4096, "s_waitcnt lgkmcnt(0)\n\ts_barrier\n\t", "offset0:0 offset1:1");
                TS(4);
            qf[0] = trf(0); SB();
            SMFV(av, A[2], wv[2]);
            qf[1] = trf(1024); SB();
            SMFV(av, A[3], wv[3]);
            qf[2] = trf(2048); SB();
            SMFV(av, A[4], wv[4]);
            qf[3] = trf(3072); SB();
            POST3("v", av, 5, wv, ak1, qtw + 4096, "offset0:128 offset1:129");
            TS(5);
            f32x4 s0, s1, s2, c[8];
            MF0(s1, kx[0][0], qf[0]);
            ones = ldsr128(lds + L_ONES + lane * 16);
            if (NVX < 3) vx[2] = ldsr128(qb + 8192 + 2048);
            SB();
            MF0(s2, kx[1][0], qf[0]);
            if (NVX < 4) vx[3] = ldsr128(qb + 8192 + 3072);
            SB();
            MF(s1, kx[0][1], qf[1]);
            {
                float t0_, t1_;
                asm volatile("v_add_f32 %2, %8, %10\n\tv_add_f32 %3, %9, %11\n\tv_mfma_f32_16x16x32_bf16 %1, %4, %6, %1\n\tv_cvt_pk_bf16_f32 %2, %2, %3\n\t"
                             "v_mfma_f32_16x16x32_bf16 %0, %5, %7, %0\n\ts_waitcnt lgkmcnt(0)\n\ts_barrier\n\tds_write_b32 %12, %2"
                             : "+v"(s1), "+v"(s2), "=&v"(t0_), "=&v"(t1_) : "v"(kx[1][1]), "v"(kx[0][2]), "v"(qf[1]), "v"(qf[2]), "v"(av[0]), "v"(av[1]), "v"(av[2]), "v"(av[3]), "v"(vtw) : "memory");
            }
                TS(6);
            kf[0] = trf(4096); SB();
            MF(s2, kx[1][2], qf[2]);
            kf[1] = trf(4096 + 1024); SB();
            MF(s1, kx[0][3], qf[3]);
            kf[2] = trf(4096 + 2048); SB();
            MF(s2, kx[1][3], qf[3]);
            kf[3] = trf(4096 + 3072);
            SB();
            ST(2);
            TS(7);
            u32x4 pm;
            float mx;
            {
                float t_;
                MF0(s0, kf[0], qf[0]);
                pm = __builtin_bit_cast(u32x4, zero4());
                asm volatile("v_mfma_f32_16x16x32_bf16 %1, %2, %3, %1\n\tv_max3_f32 %0, %4, %5, %6" : "=&v"(mx), "+v"(s0) : "v"(kf[1]), "v"(qf[1]), "v"(s1[0]), "v"(s1[1]), "v"(s1[2]));
                asm volatile("v_mfma_f32_16x16x32_bf16 %2, %3, %4, %2\n\tv_max3_f32 %1, %5, %6, %7\n\tv_max3_f32 %0, %0, %8, %9"
                             : "+v"(mx), "=&v"(t_), "+v"(s0) : "v"(kf[2]), "v"(qf[2]), "v"(s1[3]), "v"(s2[0]), "v"(s2[1]), "v"(s2[2]), "v"(s2[3]));
                asm volatile("v_mfma_f32_16x16x32_bf16 %2, %3, %4, %2\n\tv_max_f32 %0, %0, %1\n\tv_mov_b32 %1, %0" : "+v"(mx), "+v"(t_), "+v"(s0) : "v"(kf[3]), "v"(qf[3]));
                asm volatile("s_barrier" ::: "memory");
                asm volatile("s_nop 0\n\tv_permlane16_swap_b32 %0, %1\n\tv_max_f32 %0, %0, %1\n\t"
                             "v_mov_b32 %1, %0\n\ts_nop 1\n\tv_permlane32_swap_b32 %0, %1\n\tv_max_f32 %0, %0, %1\n\tv_mul_f32 %0, 0xbe0293ee, %0" FW
                             : "+v"(mx), "+v"(t_) :: "memory");
            }
                ST2(12);
            TS(8);
            u32x4 px;
            float e0, e1, em;
            {
                float a_, b_, c_;
#define FE(d, src) "v_fmamk_f32 " d ", " src ", 0x3e0293ee, %20\n\t"
                asm volatile(FE("%7", "%10") FE("%8", "%11") "v_exp_f32 %7, %7\n\t" FE("%9", "%12") "v_exp_f32 %8, %8\n\t" FE("%1", "%13") "v_exp_f32 %9, %9\n\tv_cvt_pk_bf16_f32 %0, %7, %8\n\t"
                             "v_exp_f32 %1, %1\n\t" FE("%7", "%14") FE("%8", "%15") "v_exp_f32 %7, %7\n\tv_cvt_pk_bf16_f32 %1, %9, %1\n\tv_exp_f32 %8, %8\n\t" FE("%9", "%16") FE("%3", "%17")
                             "v_exp_f32 %9, %9\n\tv_cvt_pk_bf16_f32 %2, %7, %8\n\tv_exp_f32 %3, %3\n\t" FE("%4", "%18") FE("%5", "%19") "v_cvt_pk_bf16_f32 %3, %9, %3\n\tv_max_f32 %6, %4, %5"
                             : "=&v"(px.x), "=&v"(px.y), "=&v"(px.z), "=&v"(px.w), "=&v"(e0), "=&v"(e1), "=&v"(em), "=&v"(a_), "=&v"(b_), "=&v"(c_)
                             : "v"(s1[0]), "v"(s1[1]), "v"(s1[2]), "v"(s1[3]), "v"(s2[0]), "v"(s2[1]), "v"(s2[2]), "v"(s2[3]), "v"(s0[0]), "v"(s0[1]), "v"(mx), "v"(s0));
            }
            const bool slow = __any(em > 0.f);
            SB();
            TS(9);
            f32x4 o[4], ls;
            MF0(o[0], vx[0], px);
            kf[0].x = *(LAS unsigned*)vtr; SB(); kf[2].x = *(LAS unsigned*)(vtr + 512); SB();
            MF0(o[2], vx[2], px);
            kf[1].x = *(LAS unsigned*)(vtr + 256); SB(); kf[3].x = *(LAS unsigned*)(vtr + 768); SB();
            float up = 0.f;
            if (__builtin_expect(slow, 0)) {
                up = xmax(vmax(em, 0.f)); e0 -= up; e1 -= up;
            }
            SB();
            MF0(o[1], vx[1], px);
            asm volatile("v_exp_f32 %1, %1\n\tv_mfma_f32_16x16x32_bf16 %0, %2, %3, 0" : "=&v"(o[3]), "+v"(e0) : "v"(vx[3]), "v"(px));
            asm volatile("v_exp_f32 %2, %2\n\tv_mfma_f32_16x16x32_bf16 %0, %4, %5, 0\n\tv_cvt_pk_bf16_f32 %1, %3, %2" : "=&v"(ls), "=&v"(pm.x), "+v"(e1) : "v"(e0), "v"(ones), "v"(px));
            SB();
            if (__builtin_expect(slow, 0)) {
                asm volatile("s_nop 7" : "+v"(o[0]), "+v"(o[1]), "+v"(o[2]), "+v"(o[3]), "+v"(ls));
                const float al = __builtin_amdgcn_exp2f(-up);
                o[0] *= al; o[1] *= al; o[2] *= al; o[3] *= al; ls *= al;
            }
            c[0] = zero4();
            asm volatile("" : "+v"(pm), "+v"(o[0]), "+v"(o[1]), "+v"(o[2]), "+v"(o[3]), "+v"(ls));
            TS(10);
            MF(ls, ones, pm);
            c[1] = zero4();
            MF(o[0], kf[0], pm);
            c[2] = zero4();
            MF(o[2], kf[2], pm);
            c[3] = zero4();
            MF(o[1], kf[1], pm);
            c[4] = zero4();
            u32x4 o8;
            {
                float inv; f32x2 il, ih, t0_, t1_;
                asm volatile("v_mfma_f32_16x16x32_bf16 %0, %3, %4, %0\n\tv_rcp_f32 %1, %5\n\ts_nop 0\n\tv_cndmask_b32_e64 %2, 0, %1, %6\n\tv_cndmask_b32_e64 %1, %1, 0, %6"
                             : "+v"(o[3]), "=&v"(il.x), "=&v"(ih.x) : "v"(kf[3]), "v"(pm), "v"(ls[0]), "s"(himask), "v"(ls));
                (void)inv; (void)t0_; (void)t1_;
                TS(11);
                c[5] = zero4();
                f32x4 oa = o[0] * il.x + o[2] * ih.x;
                o8.x = pk(oa[0], oa[1]); o8.y = pk(oa[2], oa[3]);
                asm volatile("" : "+v"(o8.x), "+v"(o8.y), "+v"(o[1]), "+v"(o[3]));
                const f32x4 ob = o[1] * il.x + o[3] * ih.x;
                o8.z = pk(ob[0], ob[1]); o8.w = pk(ob[2], ob[3]);
            }
            ST2(13);
            asm volatile("s_nop 1" : "+v"(o8));
            TS(12);
            SB();
            SMFV(c[0], o8, wo[0]); c[6] = zero4(); SMFV(c[1], o8, wo[1]); c[7] = zero4(); SMFV(c[2], o8, wo[2]); SMFV(c[3], o8, wo[3]);
            const unsigned slot = pblk(ring, 16 * w) + b * 128;
            u32x4 pa;
            {
                float t_;
                asm volatile("v_add_f32 %4, %15, %17\n\tv_smfmac_f32_16x16x64_bf16 %0, %9, %10, %14\n\tv_add_f32 %8, %16, %18\n\tv_smfmac_f32_16x16x64_bf16 %1, %9, %11, %14\n\t"
                             "v_cvt_pk_bf16_f32 %4, %4, %8\n\tv_smfmac_f32_16x16x64_bf16 %2, %9, %12, %14\n\tv_add_f32 %5, %19, %21\n\tv_smfmac_f32_16x16x64_bf16 %3, %9, %13, %14\n\t"
                             "v_add_f32 %8, %20, %22\n\tv_cvt_pk_bf16_f32 %5, %5, %8\n\tv_add_f32 %6, %23, %25\n\tv_add_f32 %8, %24, %26\n\tv_cvt_pk_bf16_f32 %6, %6, %8"
                             : "+v"(c[4]), "+v"(c[5]), "+v"(c[6]), "+v"(c[7]), "=&v"(pa.x), "=&v"(pa.y), "=&v"(pa.z), "=&v"(pa.w), "=&v"(t_)
                             : "v"(o8), "v"(wo[4]), "v"(wo[5]), "v"(wo[6]), "v"(wo[7]), "v"(sidx),
                               "v"(c[0][0]), "v"(c[0][1]), "v"(c[0][2]), "v"(c[0][3]), "v"(c[1][0]), "v"(c[1][1]), "v"(c[1][2]), "v"(c[1][3]), "v"(c[2][0]), "v"(c[2][1]), "v"(c[2][2]), "v"(c[2][3]));
            }
            auto fold = [](f32x4 v) { const f32x2 t = (f32x2){v[0], v[1]} + (f32x2){v[2], v[3]}; return pk(t.x, t.y); };
            pa.w = fold(c[3]);
            st16(rw, pvo, pa, slot);
            TS(13);
            SB();
            st16(rw, pvo, (u32x4){fold(c[4]), fold(c[5]), fold(c[6]), fold(c[7])}, slot + 131072);
            TS(14);
            SB();
            const unsigned now = rt();
            ST(3);
            T1 = __builtin_amdgcn_readfirstlane(tm.x); T2 = __builtin_amdgcn_readfirstlane(tm.y);
            { const int dl = s == 0 ? 99 : (int)(now + TV1 - T1); if (dl > 0) T1 = now + TV1 + (dl > TS_ ? TM : 0);
            }
        } else {
            stf(rw, b == 0 ? t0o : OOB, s, gflag, tg);
#pragma unroll
            for (int ks = 1; ks < 8; ++ks) { SMFA(ak0, A[ks], wk[0][ks]); SMFA(ak1, A[ks], wk[1][ks]); SMFV(av, A[ks], wv[ks]); }
            asm volatile("s_nop 7" : "+v"(av), "+v"(ak0), "+v"(ak1));
            unsigned d0 = pk(ak0[0] + ak0[2], ak0[1] + ak0[3]); dsw2<0, 1>(qtw + 4096, d0, d0);
            d0 = pk(ak1[0] + ak1[2], ak1[1] + ak1[3]); dsw2<128, 129>(qtw + 4096, d0, d0);
            ldsw32(vtw, pk(av[0] + av[2], av[1] + av[3]));
            bar();
        }
        {
            const u32x4 kv = trf(4096 + w * 1024);
            const u32x4 vv = {*(LAS unsigned*)vtr, *(LAS unsigned*)(vtr + 256), *(LAS unsigned*)(vtr + 512), *(LAS unsigned*)(vtr + 768)};
            const unsigned kb = kvm_off(ho, cfg, s * 32 + h);
            st16(rw, kso, kv, kb + w * 512); st16(rw, tso, vv, kb + 2048 + hf * 1024);
        }
        if constexpr (LAST) return; else {
        ST(4);
        u32x4 p0, p1, nv[3];
        unsigned pb = pblk(ring, b) + w * 2048, pb1 = pb + 1024, mnx = ho0 + (HO_MEM + rnext * MRS) * 16384 + b * 128;
        asm volatile("" : "+s"(pb), "+s"(pb1), "+s"(mnx));
        if (__builtin_expect((unsigned)s + 2 > (unsigned)__builtin_amdgcn_readfirstlane(*(LAS unsigned*)(lds + L_TS + 1024)), 0)) qkv_wait(s + 2);
        qkv_issue(s + 1, nv);
        tvo = look_at(min(s + 3, NSEG - 1));
        const unsigned lk = dec(look, tg), qmin = min(min(__builtin_amdgcn_readlane(lk, 32), __builtin_amdgcn_readlane(lk, 33)), min(__builtin_amdgcn_readlane(lk, 34), __builtin_amdgcn_readlane(lk, 35)));
        wait_rt(T1, quant);
        ST(5);
        p0 = ld16(rw, l16, pb); p1 = ld16(rw, l16, pb1);
        SB();
        qkv_store(s + 1, nv);
        SB();
        u32x4 sel = ldsr128(lds + L_SEL + lane * 16);
        asm volatile("" : "+v"(sel));
        SB();
        f32x4 rr;
        MF0(rr, sel, p0); MF(rr, sel, p1);
        SB();
        if (__builtin_expect(!__all(mx4(p0, mx4(p1, 0u)) != SENT), 0)) {
            for (unsigned spins = 0;; ++spins) {
                asm volatile("" ::: "memory");
                p0 = ld16(rw, l16, pb); p1 = ld16(rw, l16, pb1);
                if (__all(mx4(p0, mx4(p1, 0u)) != SENT)) break;
                if (spins > cap) { cap = 0; break; }
            }
            MF0(rr, sel, p0); MF(rr, sel, p1);
            asm volatile("s_nop 7" : "+v"(rr));
        }
        ST(6);
        {
            asm volatile("s_nop 0" : "+v"(rr));
            unsigned q0, q1, q2, q3;
            asm volatile("v_add_f32_dpp %0, %4, %4 row_ror:8 row_mask:0xf bank_mask:0xf\n\tv_add_f32_dpp %1, %5, %5 row_ror:8 row_mask:0xf bank_mask:0xf\n\t"
                         "v_add_f32_dpp %2, %6, %6 row_ror:8 row_mask:0xf bank_mask:0xf\n\tv_add_f32_dpp %3, %7, %7 row_ror:8 row_mask:0xf bank_mask:0xf"
                         : "=&v"(q0), "=&v"(q1), "=&v"(q2), "=&v"(q3) : "v"(rr[0]), "v"(rr[1]), "v"(rr[2]), "v"(rr[3]));
            dsw2<0, 8>(rdw, q0, q1); dsw2<2, 10>(rdw, q2, q3);
        }
        bar();
        {
            unsigned mv;
            if (__builtin_expect(b == 0 && w >= 2, 0)) {
                const unsigned now = rt() + 2; const int dl = s == 0 ? 99 : (int)(now + TV2 - T2);
                if (dl > TS_ && s > 1) mg = min(mg + MUP, 24u);
                const unsigned n1 = min((dl > 0 ? now + TV2 + (dl > TS_ ? TPEN : 0) : T2) + X + mg, SENT - 1024);
                mv = (lane & 1) ? n1 + Y + (mg >> 1) : n1;
            } else {
                f32x2 a0, a1, a2, a3;
                asm volatile("ds_read_b64 %0, %4\n\tds_read_b64 %1, %4 offset:256\n\tds_read_b64 %2, %4 offset:512\n\tds_read_b64 %3, %4 offset:768\n\ts_waitcnt lgkmcnt(0)"
                             : "=&v"(a0), "=&v"(a1), "=&v"(a2), "=&v"(a3) : "v"(rdr) : "memory");
                const f32x2 t = (a0 + a1) + (a2 + a3);
                mv = pk(t.x, t.y);
            }
            st4(rw, mso, mv, mnx);
            SB();
            const unsigned now = rt(); const int dl = s == 0 ? 99 : (int)(now + TV2 - T2);
            SB();
            {
                const u32x4 z = sent4();
                st16(rw, l16, z, pb);
                st16(rw, l16, z, pb1);
                pold = pb1;
                st4(rw, mso, z.x, ho0 + (HO_MEM + ring * MRS) * 16384 + b * 128);
            }
            if (dl > 0) T2 = now + TV2 + (dl > TS_ ? TM : 0);
        }
        if (__builtin_expect(s == NSEG - 2, 0)) { const u32x2 sv = {s + 1 + tg.t, tg.e}; asm volatile("buffer_store_dwordx2 %0, %1, %2, %3 offen sc1" :: "v"(sv), "v"(tso - 16 * 63), "s"(rw), "s"(kflag) : "memory"); }
        if (w == 1) *(LAS unsigned*)(lds + L_TS + 1024) = qmin;
        ST(7);
        ring = rnext;
        ST(8);
        }
    };
    for (int s = 0; s < NSEG - 1; ++s) step(s, bool_c<false>{});
    step(NSEG - 1, bool_c<true>{});
    drain(); bar();
    stf(rw, t0o, NSEG, kflag, tg);
}

template <int ROLE>
DI void stream_role(const Params& p, lptr lds, const int j, const int tid) {
    const int w = __builtin_amdgcn_readfirstlane(tid >> 6), lane = tid & 63, n = lane & 15, g = lane >> 4, pp = lane & 3;
    const rsrc_t rw = mkrs(p.ws, WS_END);
    const Tag tg = get_tag(p.ws);
    auto modv = [&](unsigned col) { return __builtin_bit_cast(f32x4, ld16(rw, col * 4, WS_MOD)); };
    unsigned cap = 1u << 17;
    const unsigned l16 = lane * 16, t0o = tid == 64 ? 0u : OOB;
    const int r = ROLE == 0 ? j : ROLE == 1 ? j - 64 : ROLE == 2 ? j - 120 : ROLE == 3 ? j - 152 : ROLE == 5 ? j - 168 : j - 128;
    const int tt = ROLE == 3 ? r >> 3 : ROLE == 1 ? r & 1 : (r >> 3) & 1;
    const int cgx = ROLE == 1 ? r >> 1 : (r & 7) + 8 * (r >> 4);
    constexpr int QD = ROLE == 0 ? 2 : (ROLE == 1 || ROLE == 4) ? 1 : 0;
    const int qm = QD == 2 ? r >> 5 : 2, qh = QD == 2 ? r & 31 : (r & 3) + 4 * (r >> 3), qtt = QD == 2 ? 0 : (r >> 2) & 1;
    const unsigned qline = FL_QKV + (QD == 2 ? r : 64 + r);
    u32x4 pv[32];
    if (QD) {
        unsigned long long pc; unsigned len; int rel;
        asm volatile("s_getpc_b64 %0\n1:\n\ts_mov_b32 %1, _Z5k_endv-_Z6k_main6Params\n\ts_mov_b32 %2, _Z6k_main6Params-1b" : "=s"(pc), "=s"(len), "=s"(rel));
        const unsigned o = (r >> 2) * 8192 + (tid & 63) * 128 + 0 * w;
        if (w == 0 && o + 4 <= len) { const unsigned v = *(const unsigned*)(pc + rel + o); asm volatile("" :: "v"(v)); }
    }
    if (QD) { for (int i = 0; i < PDLY; ++i) __builtin_amdgcn_s_sleep(16);
        const u16* W = (qm == 0 ? p.Wq : qm == 1 ? p.Wk : p.Wv) + qh * 128; panel_issue(W, W + 64, HD, tid, pv); }
    else {
        const int sg = ROLE == 2 ? r : ROLE == 3 ? 32 + r : 48 + r;
        LAS float* red = (LAS float*)lds;
        if (ROLE == 5) {
            unsigned* ap = (unsigned*)(p.ws + WS_HO + 240 * 16384 + (r & 1) * 8192 + (r >> 1) * 1024 + w * 256);
            unsigned best = 0xffffu;
            for (int rep = 0; rep < PREP; ++rep) {
                unsigned long long t0, t1; unsigned q = 0;
                asm volatile("s_memtime %0\n\ts_waitcnt lgkmcnt(0)" : "=s"(t0) :: "memory");
                if (lane == 0) q = __hip_atomic_fetch_add(ap, 1u, __ATOMIC_RELAXED, __HIP_MEMORY_SCOPE_AGENT);
                asm volatile("s_waitcnt vmcnt(0)\n\ts_memtime %0\n\ts_waitcnt lgkmcnt(0)" : "=s"(t1), "+v"(q) :: "memory");
                best = min(best, (unsigned)(t1 - t0));
            }
            ((LAS unsigned*)lds)[w] = best;
            bar();
            const u32x4 b4 = ldsr128(lds);
            stf(rw, t0o, min(min(b4.x, b4.y), min(b4.z, b4.w)), WS_EARLY + (EF_PROBE + r) * 128, tg);
        }
        auto mod_part = [&](auto nw_, int col0, unsigned line, auto&& between) {
            constexpr int NW = decltype(nw_)::value, RP = 64 / NW, NIT = 128 / RP;
            u32x4 wv[NIT]; u16 cv[NIT];
            const int rr = lane / NW, cc = lane % NW;
#pragma unroll
            for (int it = 0; it < NIT; ++it) { wv[it] = *(const u32x4*)(p.wmod + (size_t)(w * 128 + it * RP + rr) * 3072 + col0 + cc * 8); cv[it] = p.c[w * 128 + it * RP + rr]; }
            between();
            SU(64 + j, 5);
            float acc[8] = {};
#pragma unroll
            for (int it = 0; it < NIT; ++it) {
                const float c_ = bflo(cv[it]), sl = rb(c_ * __builtin_amdgcn_rcpf(1.f + __expf(-c_)));
                acc[0] += sl * bflo(wv[it].x); acc[1] += sl * bfhi(wv[it].x); acc[2] += sl * bflo(wv[it].y); acc[3] += sl * bfhi(wv[it].y);
                acc[4] += sl * bflo(wv[it].z); acc[5] += sl * bfhi(wv[it].z); acc[6] += sl * bflo(wv[it].w); acc[7] += sl * bfhi(wv[it].w);
            }
#pragma unroll
            for (int i = 0; i < 8; ++i) { if (NW == 4) acc[i] += dpp<0x124>(acc[i]); acc[i] += dpp<0x128>(acc[i]); acc[i] = xsum(acc[i]); }
            bar();
            if (lane < NW) {
#pragma unroll
                for (int i = 0; i < 8; ++i) red[w * 64 + cc * 8 + i] = acc[i];
            }
            bar();
            if (w == 1) {
                const unsigned mv_ = __float_as_uint(rb((red[lane] + red[64 + lane]) + (red[128 + lane] + red[192 + lane])));
                if (NW == 4) st8(rw, lane < 32 ? lane * 8 : OOB, (u32x2){mv_, tg.e}, WS_MODG + col0 * 8);
                else { st4(rw, lane * 4, mv_, WS_MOD + col0 * 4); drain(); stf(rw, t0o, 1, WS_EARLY + line * 128, tg); }
            }
        };
        u32x4 xv[8];
        auto x_issue = [&]() {
#pragma unroll
            for (int q = 0; q < 8; ++q) xv[q] = *(const u32x4*)(p.x + (size_t)(sg * 32 + w * 8 + q) * D + lane * 8); };
        if (ROLE != 5 && sg >= 16) mod_part(int_c<4>{}, (sg - 16) * 32, EF_MOD + sg - 16, x_issue);
        else x_issue();
        SU(64 + j, 3);
        float mean[8], var[8];
#pragma unroll
        for (int q = 0; q < 8; ++q) mean[q] = ((bflo(xv[q].x) + bfhi(xv[q].x)) + (bflo(xv[q].y) + bfhi(xv[q].y))) + ((bflo(xv[q].z) + bfhi(xv[q].z)) + (bflo(xv[q].w) + bfhi(xv[q].w)));
        auto wsum8 = [&](float (&v)[8]) {
#pragma unroll
            for (int q = 0; q < 8; ++q) v[q] += dpp<0xB1>(v[q]);
#pragma unroll
            for (int q = 0; q < 8; ++q) v[q] += dpp<0x4E>(v[q]);
#pragma unroll
            for (int q = 0; q < 8; ++q) v[q] += dpp<0x124>(v[q]);
#pragma unroll
            for (int q = 0; q < 8; ++q) v[q] += dpp<0x128>(v[q]);
#pragma unroll
            for (int q = 0; q < 8; ++q) v[q] = xsum(v[q]) * (1.f / D);
        };
        wsum8(mean);
#pragma unroll
        for (int q = 0; q < 8; ++q) {
            var[q] = 0.f;
#pragma unroll
            for (int i = 0; i < 4; ++i) { const float d0_ = bflo(xv[q][i]) - mean[q], d1_ = bfhi(xv[q][i]) - mean[q]; var[q] += d0_ * d0_ + d1_ * d1_; }
        }
        wsum8(var);
#pragma unroll
        for (int q = 0; q < 8; ++q) {
            const float mb = rb(mean[q]), isd = __builtin_amdgcn_rcpf(rb(__builtin_amdgcn_sqrtf(rb(rb(var[q]) + 1e-5f))));
#pragma unroll
            for (int i = 0; i < 4; ++i) { const unsigned u = vcvt(bflo(xv[q][i]) - mb, bfhi(xv[q][i]) - mb); xv[q][i] = vcvt(bflo(u) * isd, bfhi(u) * isd); }
        }
        SU(64 + j, 6);
        float t[8], sh[8];
        for (unsigned spins = 0;; ++spins) {
            asm volatile("" ::: "memory");
            u32x4 gq[8]; unsigned bad = 0;
#pragma unroll
            for (int i = 0; i < 8; ++i) gq[i] = ld16(rw, lane * 64, WS_MODG + (i >> 2) * 4096 + (i & 3) * 16);
#pragma unroll
            for (int i = 0; i < 8; ++i) bad |= (gq[i].y ^ tg.e) | (gq[i].w ^ tg.e);
#pragma unroll
            for (int i = 0; i < 4; ++i) { sh[2 * i] = __uint_as_float(gq[i].x); sh[2 * i + 1] = __uint_as_float(gq[i].z); t[2 * i] = rb(1.f + __uint_as_float(gq[4 + i].x)); t[2 * i + 1] = rb(1.f + __uint_as_float(gq[4 + i].z)); }
            if (__all(bad == 0)) break;
            if (spins > cap) { cap = 0; break; }
            __builtin_amdgcn_s_sleep(4);
        }
        SU(64 + j, 7);
#pragma unroll
        for (int q = 0; q < 8; ++q) {
            u32x4 o;
#pragma unroll
            for (int i = 0; i < 4; ++i) { const unsigned u = vcvt(bflo(xv[q][i]) * t[2 * i], bfhi(xv[q][i]) * t[2 * i + 1]); o[i] = pk(bflo(u) + sh[2 * i], bfhi(u) + sh[2 * i + 1]); }
            st16(rw, (lane >> 2) * 1024 + (lane & 3) * 16, o, WS_H1 + (unsigned)(sg * 2 + (w >> 1)) * 16384u + ((w & 1) * 8 + q) * 64);
        }
        drain(); bar();
        stf(rw, t0o, 1, WS_EARLY + (EF_LN + sg) * 128, tg);
        SU(64 + j, 4);
        if (ROLE == 2) mod_part(int_c<8>{}, 1024 + r * 64, EF_MOD2 + r, [] {});
    }
    unsigned cfg = 0;
    if (!QD) cfg = get_cfg(rw, tg, lane, cap);
    SU(64 + j, 0);
    u32x4 qa[16], qb[16], hv[QD ? 4 * QD : 1];
    const unsigned h1off = WS_H1 + (unsigned)(qtt * 16 + w * 4) * 1024u;
    auto h1_issue = [&](int s) {
#pragma unroll
        for (int i = 0; i < 4 * QD; ++i) hv[i] = ld16(rw, (n * 4 + g) * 16, h1off + (unsigned)s * 32768u + (i >> 2) * 16384 + (i & 3) * 1024); };
    auto qkv_in = [&]() {
#pragma unroll
        for (int i = 0; i < 4 * QD; ++i) ldsw128(lds + L_Q + (i >> 2) * 16384 + (w * 4 + (i & 3)) * 1024 + lane * 16, hv[i]);
    };
    auto qkv_mm = [&](int s) {
#pragma unroll
        for (int t = 0; t < QD; ++t) {
            f32x4 ca, cb;
            tile_mma<QD == 1>(lds + L_Q + t * 16384, lane, qa, qb, ca, cb);
            st16(rw, l16, (u32x4){pk(ca[0], ca[1]), pk(ca[2], ca[3]), pk(cb[0], cb[1]), pk(cb[2], cb[3])}, qkv_off(s * 32 + qh, qm, cfg) + (QD == 2 ? t * 4 + w : w * 2 + qtt) * 1024);
        }
    };
    auto qkv_seg = [&](int s) { qkv_in(); h1_issue(min(s + 1, NSEG - 1)); bar(); qkv_mm(s); };
    auto publish = [&](unsigned v) { drain(); bar(); stf(rw, t0o, v, flg(qline, cfg), tg); };
    u32x4 sa[16], sb[16];
    if (QD) {
        panel_finish(lds, tid, 32 * w + 8 * pp, 32 * w + 8 * pp + 4, pv, qa, qb);
        SU(64 + j, 1);
        cfg = get_cfg(rw, tg, lane, cap);
        wait_words(rw, tg, WS_EARLY + (EF_LN + (lane & 15)) * 128, 1, cap);
        SU(64 + j, 2);
        h1_issue(0);
        for (int s = 0; s < 7; ++s) { qkv_seg(s); publish(s + 1); }
        SU(64 + j, 4);
        if (ROLE == 0) { const u16* W = p.Wo + (size_t)(r & 7) * 512 * D + (r >> 4) * 128; panel_issue(W, W + 64, D, tid, pv); }
        if (ROLE == 1) panel_issue(p.wg + cgx * 64, p.wu + cgx * 64, F, tid, pv);
        SB();
        qkv_seg(7); publish(8);
        if (ROLE == 0) panel_finish(lds, tid, 32 * w + 4 * pp, 32 * w + 16 + 4 * pp, pv, sa, sb);
        if (ROLE == 1) panel_finish(lds, tid, 16 * w + 4 * pp, 64 + 16 * w + 4 * pp, pv, sa, sb);
    }
    if (ROLE == 2) {
        const rsrc_t rd = mkrs(p.wd + cgx * 32, (1791 * D + 32) * 2);
#pragma unroll
        for (int half = 0; half < 2; ++half) {
            u32x4 v[14];
#pragma unroll
            for (int i = 0; i < 14; ++i) v[i] = ldw(rd, ((tid >> 2) * D + (tid & 3) * 8) * 2, (half * 896 + i * 64) * D * 2);
            bar();
#pragma unroll
            for (int i = 0; i < 14; ++i) ldsw128(lds + ((tid >> 2) + 64 * i) * 80 + (tid & 3) * 16, v[i]);
            bar();
            if ((w >> 1) == half) {
                lptr pa = lds + ((w & 1) * 448 + 8 * g + (n >> 2)) * 80 + pp * 8;
#pragma unroll
                for (int ks = 0; ks < 14; ++ks) { sa[ks] = tr2(pa + ks * 32 * 80, 4 * 80); sb[ks] = tr2(pa + ks * 32 * 80 + 32, 4 * 80); }
            }
        }
        bar();
    }
    if (QD) { wait_words(rw, tg, WS_EARLY + (EF_LN + lane) * 128, 1, cap); for (int s = 8; s < LEAD; ++s) { qkv_seg(s); publish(s + 1); } }
    SU(64 + j, 5);
    unsigned poff; unsigned ahead = 0;
    if (ROLE == 0) poff = FL_A + (r & 7) * 2 + (lane & 1);
    if (ROLE == 1) poff = FL_L + tt * 8 + (lane & 7);
    if (ROLE == 2) poff = FL_C + tt + 2 * (lane < 28 ? lane : 0);
    if (ROLE == 3) poff = FL_B + (lane & 7) + tt * 8 + ((lane >> 3) & 3) * 16;
    if (ROLE == 4) poff = FL_G;
    if (ROLE == 5) {
        const int hh = 2 * r + ((lane >> 1) & 1), k = (lane >> 2) & 3, pr = (hh & 3) + 8 * (hh >> 2) + 4 * (lane & 1);
        poff = k == 0 ? FL_KM + pr : k == 3 ? FL_QKV + 64 + pr : FL_QKV + (k - 1) * 32 + hh;
        ahead = (lane & 12) != 0;
        if (lane == 63) { poff = FL_G; ahead = 0; }
    }
    poff = flg(poff, cfg);
    const unsigned oflag = flg((ROLE == 0 ? FL_B : ROLE == 1 ? FL_C : ROLE == 3 ? FL_L : FL_A) + r, cfg), qflag = flg(qline, cfg);
    unsigned seen = 0;
    auto ready = [&](unsigned nd) {
        if (ROLE != 5) return __all(seen >= nd);
        const unsigned gl = __builtin_amdgcn_readlane(seen, 63);
        return __all(((lane & 12) == 0 ? max(seen, gl) : lane == 63 ? nd : seen) >= nd);
    };
    auto wait_in = [&](unsigned need) {
        if (w == 0) {
            const unsigned nd = min(need + ahead, (unsigned)NSEG);
            for (unsigned spins = 0; !ready(nd); ++spins) {
                if (spins > cap) { cap = 0; break; }
                if (spins) __builtin_amdgcn_s_sleep(PSL);
                asm volatile("" ::: "memory");
                seen = ldf(rw, poff, tg);
            }
        }
        bar();
    };
    const unsigned pof0 = w == 0 ? poff : OOB;
    const int hh = 2 * r + (w >> 1), att = w & 1;
    u32x4 xq[4], ka[4], kb[4], vb[8];
    const unsigned l16b = att ? l16 : OOB;
    auto a_issue_qk = [&](int s) {
        const unsigned q_ = qkv_off(s * 32 + hh, 0, cfg) + att * 4096, k_ = qkv_off(s * 32 + hh, 1, cfg);
#pragma unroll
        for (int ks = 0; ks < 4; ++ks) { xq[ks] = ld16(rw, l16, q_ + ks * 1024); ka[ks] = ld16(rw, l16, k_ + ks * 1024); kb[ks] = ld16(rw, l16b, k_ + 4096 + ks * 1024); }
    };
    auto a_issue_v = [&](int s) {
        const unsigned v_ = qkv_off(s * 32 + hh, 2, cfg);
#pragma unroll
        for (int i = 0; i < 8; ++i) vb[i] = ld16(rw, (i & 1) ? l16b : l16, v_ + i * 1024);
    };
    if (ROLE == 5) { wait_in(0); a_issue_qk(0); a_issue_v(0); }
    const int d0 = 128 * w + 16 * (lane >> 3) + 4 * (lane & 3);
    f32x4 cga, cgb, cgc;
    if (ROLE == 2 || ROLE == 3) wait_words(rw, tg, WS_EARLY + (EF_MOD2 + (lane & 31)) * 128, 1, cap);
    if (ROLE == 3) { cga = modv(1024 + d0); cgb = modv(1536 + d0); cgc = modv(2048 + d0);
#pragma unroll
        for (int i = 0; i < 4; ++i) cgc[i] = rb(1.f + cgc[i]); }
    if (ROLE == 2) cga = modv(2560 + cgx * 32 + (w & 1) * 16 + 4 * g);
    u32x2 xnext = {0, 0};
    if (ROLE == 3) xnext = *(const u32x2*)(p.x + (size_t)(tt * 16 + 2 * (r & 7) + ((lane >> 2) & 1)) * D + d0);
    if (QD) qkv_in();
    auto iter = [&](const int s, auto doq_) {
        constexpr bool doq = decltype(doq_)::value;
        u32x2 xv = {0, 0};
        wait_in(s + 1);
        u32x4 a[14];
        if (ROLE == 0) {
#pragma unroll
            for (int i = 0; i < 4; ++i) a[i] = ld16(rw, l16, loc(WS_O, (unsigned)((s * 2 + tt) * 128 + (r & 7) * 16 + w * 4) * 1024u, cfg, 1) + i * 1024);
        }
        if (ROLE == 1) {
#pragma unroll
            for (int i = 0; i < 4; ++i) a[i] = ld16(rw, (n * 4 + g) * 16, loc(WS_H2, (unsigned)((s * 2 + tt) * 16 + w * 4) * 1024u, cfg, 0) + i * 1024);
        }
        if (ROLE == 2) {
#pragma unroll
            for (int ks = 0; ks < 14; ++ks) a[ks] = ld16(rw, (g >> 1) * 512 + n * 32 + (g & 1) * 16, loc(WS_G, (unsigned)((s * 2 + tt) * 56 + w * 14 + ks) * 1024u, cfg, 1));
            xv = ld8(rw, (n >> 3) * 16384 + (n & 7) * 1024 + ((w & 1) * 16 + 4 * g) * 2, loc(WS_X1, (s * 2 + tt) * 16384, cfg, 1) + cgx * 64);
        }
        if (ROLE == 3) {
#pragma unroll
            for (int kg = 0; kg < 8; ++kg) a[kg] = ld16(rw, (lane >> 3) * 1024 + (lane & 7) * 16, loc(WS_PB, (unsigned)(((s * 8 + kg) * 2 + tt) * 32 + 8 * w) * 1024u, cfg, 1) + (r & 7) * 128);
            xv = xnext;
            xnext = *(const u32x2*)(p.x + (size_t)(min(s + 1, NSEG - 1) * 32 + tt * 16 + 2 * (r & 7) + ((lane >> 2) & 1)) * D + d0);
        }
        if (ROLE == 5) {
            const unsigned kb_ = kvm_off(WS_HO, cfg, s * 32 + hh);
#pragma unroll
            for (int ks = 0; ks < 4; ++ks) a[ks] = ld16(rw, ((2 * (n >> 2) + (n & 1)) * 4 + g) * 16, kb_ + ks * 512);
            a[4] = ld16(rw, l16, kb_ + 2048); a[5] = ld16(rw, l16, kb_ + 3072);
        }
        const u32x2 sraw = ld8(rw, pof0, 0);
        if (doq) h1_issue(min(s + LEAD + 1, NSEG - 1));
        SB();
        if (doq) qkv_mm(s + LEAD);
        SB();
        if (ROLE == 0 || ROLE == 1) {
#pragma unroll
            for (int i = 0; i < 4; ++i) ldsw128(lds + L_A + (w * 4 + i) * 1024 + lane * 16, a[i]);
            bar();
            f32x4 ca, cb; tile_mma<false>(lds + L_A, lane, sa, sb, ca, cb);
            if (ROLE == 0) {
                const unsigned o = loc(WS_PB, (unsigned)(((s * 8 + (r & 7)) * 2 + tt) * 32 + (r >> 4) * 8 + w * 2) * 1024u, cfg, 1);
                st16(rw, (n * 4 + g) * 16, __builtin_bit_cast(u32x4, ca), o); st16(rw, (n * 4 + g) * 16, __builtin_bit_cast(u32x4, cb), o + 1024);
            } else {
                float q[4];
#pragma unroll
                for (int i = 0; i < 4; ++i) { const float gv = rb(ca[i]); q[i] = rb(gv * __builtin_amdgcn_rcpf(1.f + __expf(-gv))) * rb(cb[i]); }
                st8(rw, n * 32 + g * 8, (u32x2){pk(q[0], q[1]), pk(q[2], q[3])}, loc(WS_G, (unsigned)((s * 2 + tt) * 56 + cgx * 2 + (w >> 1)) * 1024u, cfg, 1) + (w & 1) * 512);
            }
        }
        if (ROLE == 2) {
            f32x4 c0 = {0, 0, 0, 0}, c1 = c0;
            asm volatile("s_nop 1" : "+v"(c0), "+v"(c1));
#pragma unroll
            for (int ks = 0; ks < 14; ++ks) { MFWA(c0, sa[ks], a[ks]); MFWA(c1, sb[ks], a[ks]); }
            asm volatile("s_nop 7" : "+v"(c0), "+v"(c1));
            *(LAS f32x4*)(lds + L_A + (w * 2) * 1024 + lane * 16) = c0; *(LAS f32x4*)(lds + L_A + (w * 2 + 1) * 1024 + lane * 16) = c1;
            bar();
            if (w < 2) {
                lptr q = lds + L_A + w * 1024 + lane * 16;
                const f32x4 y = (*(LAS f32x4*)q + *(LAS f32x4*)(q + 2048)) + (*(LAS f32x4*)(q + 4096) + *(LAS f32x4*)(q + 6144));
                const float o0 = bflo(xv.x) + rb(cga[0] * rb(y[0])), o1 = bfhi(xv.x) + rb(cga[1] * rb(y[1])), o2 = bflo(xv.y) + rb(cga[2] * rb(y[2])), o3 = bfhi(xv.y) + rb(cga[3] * rb(y[3]));
                *(u32x2*)(p.out + (size_t)(s * 32 + tt * 16 + n) * D + cgx * 32 + (w & 1) * 16 + 4 * g) = (u32x2){pk(o0, o1), pk(o2, o3)};
            }
        }
        if (ROLE == 5) {
            f32x4 x0, x1, x2;
            MF0(x1, ka[0], xq[0]); MF0(x2, kb[0], xq[0]);
#pragma unroll
            for (int ks = 1; ks < 4; ++ks) { MF(x1, ka[ks], xq[ks]); MF(x2, kb[ks], xq[ks]); }
            MF0(x0, a[0], xq[0]); MF(x0, a[1], xq[1]); MF(x0, a[2], xq[2]); MF(x0, a[3], xq[3]);
            a_issue_qk(min(s + 1, NSEG - 1));
            asm volatile("s_nop 7" : "+v"(x0), "+v"(x1), "+v"(x2));
            SB();
            const float raw[10] = {x1[0], x1[1], x1[2], x1[3], x2[0], x2[1], x2[2], x2[3], x0[0], x0[1]};
            float sc[10];
            const int qi = 16 * att + n - 4 * g;
#pragma unroll
            for (int i = 0; i < 5; ++i) {
                const unsigned u = vcvt(raw[2 * i], raw[2 * i + 1]);
                const unsigned v = pk(bflo(u) * 0.08838834764831845f, bfhi(u) * 0.08838834764831845f);
                sc[2 * i] = bflo(v); sc[2 * i + 1] = bfhi(v);
            }
#pragma unroll
            for (int i = 0; i < 8; ++i) sc[i] = ((i & 3) + 16 * (i >> 2) <= qi) ? sc[i] : -1e9f;
            float mx = vmax3(vmax3(vmax3(sc[0], sc[1], sc[2]), vmax3(sc[3], sc[4], sc[5]), vmax(sc[6], sc[7])), sc[8], sc[9]);
            mx = xmax(mx) * -1.4426950408889634f;
#pragma unroll
            for (int i = 0; i < 10; ++i) sc[i] = __builtin_amdgcn_exp2f(__builtin_fmaf(sc[i], 1.4426950408889634f, mx));
            const float inv = __builtin_amdgcn_rcpf(xsum((((sc[0] + sc[1]) + (sc[2] + sc[3])) + ((sc[4] + sc[5]) + (sc[6] + sc[7]))) + (sc[8] + sc[9])));
            u32x4 xpx = {pk(sc[0] * inv, sc[1] * inv), pk(sc[2] * inv, sc[3] * inv), pk(sc[4] * inv, sc[5] * inv), pk(sc[6] * inv, sc[7] * inv)};
            u32x4 xpm = {pk(sc[8] * inv, sc[9] * inv), 0u, 0u, 0u};
            f32x4 o[8];
#pragma unroll
            for (int e = 0; e < 4; ++e) {
                u32x4 fa = {vb[2 * e].x, vb[2 * e].y, vb[2 * e + 1].x, vb[2 * e + 1].y}, fb = {vb[2 * e].z, vb[2 * e].w, vb[2 * e + 1].z, vb[2 * e + 1].w};
                u32x4 ma = vb[2 * e], mb = vb[2 * e + 1];
                ma.x = a[4 + (e >> 1)][2 * (e & 1)]; mb.x = a[4 + (e >> 1)][2 * (e & 1) + 1];
                asm volatile("s_nop 1" : "+v"(fa), "+v"(fb), "+v"(ma), "+v"(mb), "+v"(xpx), "+v"(xpm));
                MF0(o[2 * e], fa, xpx); MF0(o[2 * e + 1], fb, xpx); MF(o[2 * e], ma, xpm); MF(o[2 * e + 1], mb, xpm);
            }
            asm volatile("s_nop 7" : "+v"(o[0]), "+v"(o[1]), "+v"(o[2]), "+v"(o[3]), "+v"(o[4]), "+v"(o[5]), "+v"(o[6]), "+v"(o[7]));
#pragma unroll
            for (int e = 0; e < 4; ++e)
                st16(rw, l16, (u32x4){pk(o[2 * e][0], o[2 * e][1]), pk(o[2 * e][2], o[2 * e][3]), pk(o[2 * e + 1][0], o[2 * e + 1][1]), pk(o[2 * e + 1][2], o[2 * e + 1][3])}, loc(WS_O, (unsigned)((s * 2 + att) * 128 + hh * 4) * 1024u, cfg, 1) + e * 1024);
            SB();
            a_issue_v(min(s + 1, NSEG - 1));
        }
        if (ROLE == 3) {
            LAS float* sm = (LAS float*)(lds + L_A);
            const int tk = (lane >> 2) & 1, tok = 2 * (r & 7) + tk;
            f32x4 acc = __builtin_bit_cast(f32x4, a[0]);
#pragma unroll
            for (int kg = 1; kg < 8; ++kg) acc += __builtin_bit_cast(f32x4, a[kg]);
            float x1[4] = {bflo(xv.x), bfhi(xv.x), bflo(xv.y), bfhi(xv.y)};
            float sum = 0.f;
#pragma unroll
            for (int i = 0; i < 4; ++i) { x1[i] = rb(x1[i] + rb(cga[i] * rb(acc[i]))); sum += x1[i]; }
            st8(rw, tk * 1024 + d0 * 2, (u32x2){pk(x1[0], x1[1]), pk(x1[2], x1[3])}, loc(WS_X1, (s * 32 + tt * 16 + 2 * (r & 7)) * 1024, cfg, 1));
            sum += dpp<0xB1>(sum); sum += dpp<0x4E>(sum); sum += dpp<0x128>(sum); sum = xsum(sum);
            const float mw_ = sum * (1.f / 128);
            float var = 0.f;
#pragma unroll
            for (int i = 0; i < 4; ++i) { const float d = x1[i] - mw_; var += d * d; }
            var += dpp<0xB1>(var); var += dpp<0x4E>(var); var += dpp<0x128>(var); var = xsum(var);
            if (lane == 0 || lane == 4) { sm[tk * 8 + w] = mw_; sm[tk * 8 + 4 + w] = var; }
            bar();
            const f32x4 ms = *(LAS f32x4*)(sm + tk * 8), vs = *(LAS f32x4*)(sm + tk * 8 + 4);
            const float mean = ((ms[0] + ms[1]) + (ms[2] + ms[3])) * 0.25f;
            const f32x4 dm = ms - mean;
            var = (((vs[0] + vs[1]) + (vs[2] + vs[3])) + 128.f * ((dm[0] * dm[0] + dm[1] * dm[1]) + (dm[2] * dm[2] + dm[3] * dm[3]))) * (1.f / D);
            const float mb = rb(mean), sd = __builtin_amdgcn_rcpf(rb(__builtin_amdgcn_sqrtf(rb(rb(var) + 1e-5f))));
            float hh2[4];
#pragma unroll
            for (int i = 0; i < 4; ++i) hh2[i] = lnmod(x1[i], mb, sd, cgc[i], cgb[i]);
            st8(rw, ((d0 >> 5) & 3) * 1024 + tok * 64 + (d0 & 31) * 2, (u32x2){pk(hh2[0], hh2[1]), pk(hh2[2], hh2[3])}, loc(WS_H2, (unsigned)((s * 2 + tt) * 16 + 4 * w) * 1024u, cfg, 0));
        }
        if (ROLE == 5) asm volatile("s_waitcnt vmcnt(8)" ::: "memory"); else drain();
        bar();
        if (ROLE != 2 && ROLE != 4) stf(rw, t0o, s + 1, oflag, tg);
        if (doq) stf(rw, t0o, s + LEAD + 1, qflag, tg);
        seen = dec(sraw, tg);
        if (doq) qkv_in();
    };
    int s = 0;
    if (QD) for (; s < NSEG - LEAD; ++s) iter(s, bool_c<QD != 0>{});
    if (ROLE != 4) for (; s < NSEG; ++s) iter(s, bool_c<false>{});
    if (ROLE == 2 && r == 0 && tid == 0) *(unsigned*)(p.ws + WS_EPOCH) = tg.e;
}

__global__ void __launch_bounds__(256, 1) k_main(Params p) {
    extern __shared__ __attribute__((aligned(16))) char smem[];
    const lptr lds = (lptr)smem;
    const int b = blockIdx.x, tid = threadIdx.x;
    if (b < 128 && (b & 7) < 4) chain_role(p, lds, (b >> 3) * 4 + (b & 3), tid);
    else {
        const int u = (b >> 3) * 4 + (b & 3);
        const int j = (b & 4) ? (u < 64 ? u : u + 56) : (u < 120 ? u : u + 64);
        if (j < 64) stream_role<0>(p, lds, j, tid);
        else if (j < 120) stream_role<1>(p, lds, j, tid);
        else if (j < 152) stream_role<2>(p, lds, j, tid);
        else if (j < 168) stream_role<3>(p, lds, j, tid);
        else if (j < 184) stream_role<5>(p, lds, j, tid);
        else stream_role<4>(p, lds, j, tid);
    }
}

__global__ void k_end() {}
extern "C" void kernel_launch(void* const* d_in, const int* in_sizes, int n_in, void* d_out, int out_size, void* d_ws, size_t ws_size, hipStream_t stream) {
    Params p;
    p.x = (const u16*)d_in[0]; p.mem0 = (const u16*)d_in[1]; p.Wq = (const u16*)d_in[2]; p.Wk = (const u16*)d_in[3]; p.Wv = (const u16*)d_in[4]; p.Wo = (const u16*)d_in[5];
    p.wg = (const u16*)d_in[8]; p.wu = (const u16*)d_in[9]; p.wd = (const u16*)d_in[10]; p.c = (const u16*)d_in[11]; p.wmod = (const u16*)d_in[12];
    p.out = (u16*)d_out; p.ws = (char*)d_ws + ((0x200000 - ((uintptr_t)d_ws & 0x1FFFFF)) & 0x1FFFFF);
    constexpr int LDSB = 140 * 1024;
    (void)hipFuncSetAttribute((const void*)k_main, hipFuncAttributeMaxDynamicSharedMemorySize, LDSB);
    k_main<<<256, 256, LDSB, stream>>>(p);
}
```

```cpp
#include <hip/hip_runtime.h>
#include <hip/hip_bf16.h>
#include <stdint.h>

typedef unsigned short u16;
typedef __bf16 bf16x2 __attribute__((ext_vector_type(2)));
typedef __bf16 bf16x8 __attribute__((ext_vector_type(8)));
typedef short s16x4 __attribute__((ext_vector_type(4)));
typedef float f32x4 __attribute__((ext_vector_type(4)));
typedef float f32x2 __attribute__((ext_vector_type(2)));
typedef unsigned u32x2 __attribute__((ext_vector_type(2)));
typedef unsigned u32x4 __attribute__((ext_vector_type(4)));
typedef unsigned u32x8 __attribute__((ext_vector_type(8)));
#define LAS __attribute__((address_space(3)))
typedef LAS char* lptr;
#define DI __device__ __forceinline__

constexpr int TX = 56, TY = 44, CX = 2150, CY = 430;
constexpr int TV1 = 8, TV2 = 8, TLATE = 6, TM = 10, TPEN = 20, MUP = 2;
constexpr int LEAD = 16;

constexpr int S = 2048, D = 512, NSEG = 64, F = 1792, HD = 4096;
constexpr unsigned SENT = 0xFFFFFFFFu;
constexpr int NRING = 3;
constexpr unsigned WS_MOD = 0;
constexpr unsigned WS_FLAG = 131072;
constexpr unsigned FL_ARM = 64, FL_QKV = 128, FL_L = 320, FL_KM = 384, FL_B = 448, FL_C = 512, FL_A = 576, FL_G = 600;
constexpr unsigned WS_EPOCH = 12544;
constexpr unsigned WS_EARLY = 16384, EF_PROBE = 0, EF_LN = 64, EF_MOD2 = 128, WS_MODG = 65536;
constexpr unsigned NC = 4;
constexpr unsigned WS_HO = 220u << 20, HO_MEM = 256, MRS = NC + 1;
constexpr unsigned WS_H1 = 4u << 20, WS_X1 = 6u << 20, WS_H2 = 10u << 20, WS_G = 14u << 20, WS_O = 28u << 20, WS_QKV = 60u << 20, WS_PB = 156u << 20, WS_END = 246u << 20;

DI float bflo(unsigned x) { return __uint_as_float(x << 16); }
DI float bfhi(unsigned x) { return __uint_as_float(x & 0xffff0000u); }
DI unsigned pk(float lo, float hi) { bf16x2 v = {(__bf16)lo, (__bf16)hi}; return __builtin_bit_cast(unsigned, v); }
DI float rb(float x) { return (float)(__bf16)x; }
DI void bar() { asm volatile("s_waitcnt lgkmcnt(0)\n\ts_barrier" ::: "memory"); }
DI void drain() { asm volatile("s_waitcnt vmcnt(0)" ::: "memory"); }
DI u32x2 trread(lptr p) { return __builtin_bit_cast(u32x2, __builtin_amdgcn_ds_read_tr16_b64_v4i16((LAS s16x4*)p)); }
DI u32x4 tr2(lptr p, int off) { const u32x2 a = trread(p), b = trread(p + off); return (u32x4){a.x, a.y, b.x, b.y}; }
DI u32x4 ldsr128(lptr p) { return *(LAS u32x4*)p; }
DI void ldsw128(lptr p, u32x4 v) { *(LAS u32x4*)p = v; }
DI void ldsw64(lptr p, u32x2 v) { *(LAS u32x2*)p = v; }
DI void ldsw32(lptr p, unsigned v) { *(LAS unsigned*)p = v; }
DI float vmax(float a, float b) { float r; asm("v_max_f32 %0, %1, %2" : "=v"(r) : "v"(a), "v"(b)); return r; }
DI float vmax3(float a, float b, float c) { float r; asm("v_max3_f32 %0, %1, %2, %3" : "=v"(r) : "v"(a), "v"(b), "v"(c)); return r; }
DI float xmax(float x) {
    auto s = __builtin_amdgcn_permlane16_swap(__float_as_uint(x), __float_as_uint(x), false, false);
    x = vmax(__uint_as_float(s[0]), __uint_as_float(s[1]));
    auto t = __builtin_amdgcn_permlane32_swap(__float_as_uint(x), __float_as_uint(x), false, false);
    return vmax(__uint_as_float(t[0]), __uint_as_float(t[1]));
}
DI float xsum(float x) {
    auto s = __builtin_amdgcn_permlane16_swap(__float_as_uint(x), __float_as_uint(x), false, false);
    x = __uint_as_float(s[0]) + __uint_as_float(s[1]);
    auto t = __builtin_amdgcn_permlane32_swap(__float_as_uint(x), __float_as_uint(x), false, false);
    return __uint_as_float(t[0]) + __uint_as_float(t[1]);
}
template <int CTRL> DI float dpp(float x) { return __builtin_bit_cast(float, __builtin_amdgcn_mov_dpp(__builtin_bit_cast(int, x), CTRL, 0xf, 0xf, true)); }
template <int CTRL> DI int dppi(int x) { return __builtin_amdgcn_mov_dpp(x, CTRL, 0xf, 0xf, true); }
DI unsigned mx4(u32x4 v, unsigned m) { return max(max(max(v.x, v.y), max(v.z, v.w)), m); }
typedef __amdgpu_buffer_rsrc_t rsrc_t;
DI rsrc_t mkrs(const void* p, unsigned bytes) { return __builtin_amdgcn_make_buffer_rsrc((void*)p, 0, bytes, 0x00020000); }
DI u32x4 ld16(rsrc_t r, unsigned off, unsigned soff = 0) { return __builtin_amdgcn_raw_buffer_load_b128(r, off, soff, 16); }
DI u32x2 ld8(rsrc_t r, unsigned off, unsigned soff) { return __builtin_amdgcn_raw_buffer_load_b64(r, off, soff, 16); }
DI unsigned ld4(rsrc_t r, unsigned off, unsigned soff = 0) { return __builtin_amdgcn_raw_buffer_load_b32(r, off, soff, 16); }
DI void st16(rsrc_t r, unsigned off, u32x4 v, unsigned soff = 0) { __builtin_amdgcn_raw_buffer_store_b128(v, r, off, soff, 16); }
DI void st8(rsrc_t r, unsigned off, u32x2 v, unsigned soff) { __builtin_amdgcn_raw_buffer_store_b64(v, r, off, soff, 16); }
DI void st4(rsrc_t r, unsigned off, unsigned v, unsigned soff = 0) { __builtin_amdgcn_raw_buffer_store_b32(v, r, off, soff, 16); }
DI u32x4 ldw(rsrc_t r, unsigned voff, unsigned soff) { return __builtin_amdgcn_raw_buffer_load_b128(r, voff, soff, 0); }
DI unsigned rt() { return (unsigned)__builtin_amdgcn_s_memrealtime(); }
DI void wait_rt(unsigned t, unsigned q = 1) {
    int d;
    do d = (int)(t - rt()); while (d >= (int)q && d < 4096);
    d = min(d, 8);
    for (; d > 0; --d) asm volatile("");
    asm volatile("" ::: "memory");
}

template <int V> struct int_c { static constexpr int value = V; };
template <bool V> struct bool_c { static constexpr bool value = V; };
DI unsigned loc(unsigned base, unsigned off, unsigned cfg, unsigned far) { const unsigned x = base + ((off >> 13) << 14) + (off & 8191); return x + ((((cfg >> ((x >> 21) & 1)) ^ far) & 1) << 13); }
DI unsigned qkv_off(unsigned i, unsigned sec, unsigned cfg) { return loc(WS_QKV, (i * 3 + sec) * 8192u, cfg, 1); }
DI unsigned flg(unsigned line, unsigned cfg) { return loc(WS_FLAG, line * 128, cfg, line >= FL_KM); }
DI unsigned kvm_off(unsigned ho, unsigned cfg, unsigned i) { return ho + (8u << 20) + (i >> 1) * 16384 + ((cfg >> ((i >> 8) & 1)) & 1) * 8192 + (i & 1) * 4096; }
struct Tag { unsigned e, t; };
DI Tag get_tag(const char* ws) { const unsigned e = *(const __attribute__((address_space(4))) unsigned*)(uintptr_t)(ws + WS_EPOCH) + 1; return Tag{e, e * 0x9E3779B1u}; }
DI unsigned dec(u32x2 v, Tag tg) { const unsigned c = v.x - tg.t; return (v.y == tg.e && c < 0x100000u) ? c : 0u; }
DI unsigned ldf(rsrc_t r, unsigned off, Tag tg) { return dec(ld8(r, off, 0), tg); }
DI void stf(rsrc_t r, unsigned voff, unsigned v, unsigned soff, Tag tg) { st8(r, voff, (u32x2){v + tg.t, tg.e}, soff); }
struct Params {
    const u16 *x, *mem0, *Wq, *Wk, *Wv, *Wo, *wg, *wu, *wd, *c, *wmod;
    u16* out; char* ws;
};

DI float lnmod(float v, float mb, float isd, float t, float sh) { return rb(rb(rb(v - mb) * isd) * t) + sh; }

DI unsigned wait_words(rsrc_t rw, Tag tg, unsigned off, unsigned need, unsigned& cap) {
    unsigned v;
    for (unsigned spins = 0;; ++spins) {
        asm volatile("" ::: "memory");
        v = ldf(rw, off, tg);
        if (__all(v >= need)) break;
        if (spins > cap) { cap = 0; break; }
        __builtin_amdgcn_s_sleep(2);
    }
    asm volatile("" ::: "memory");
    return v;
}
DI unsigned get_cfg(rsrc_t rw, Tag tg, int lane, unsigned& cap, unsigned also = EF_PROBE) {
    int v = wait_words(rw, tg, WS_EARLY + (((lane & 16) ? also : EF_PROBE) + (lane & 15)) * 128, 1, cap);
    v = min(v, dppi<0x4E>(v)); v = min(v, dppi<0x124>(v)); v = min(v, dppi<0x128>(v));
    return __builtin_amdgcn_readlane(v, 1) < __builtin_amdgcn_readlane(v, 0) ? 1u : 2u;
}

constexpr int PSTR = 272;
DI void panel_issue(const u16* srcA, const u16* srcB, int ld, int tid, u32x4 (&v)[32]) {
    const rsrc_t ra = mkrs(srcA, (511 * ld + 64) * 2), rb_ = mkrs(srcB, (511 * ld + 64) * 2);
    const unsigned voff = ((tid >> 3) * ld + (tid & 7) * 8) * 2;
#pragma unroll
    for (int i = 0; i < 16; ++i) { v[2 * i] = ldw(ra, voff, i * 32 * ld * 2); v[2 * i + 1] = ldw(rb_, voff, i * 32 * ld * 2); }
}
DI void panel_finish(lptr lds, int tid, int colA, int colB, u32x4 (&v)[32], u32x4 (&wa)[16], u32x4 (&wb)[16]) {
    const int lane = tid & 63, g = lane >> 4, q = (lane & 15) >> 2;
    const lptr wp = lds + (tid >> 3) * PSTR + (tid & 7) * 16;
#pragma unroll
    for (int half = 0; half < 2; ++half) {
        bar();
#pragma unroll
        for (int i = 0; i < 8; ++i) { ldsw128(wp + i * 32 * PSTR, v[16 * half + 2 * i]); ldsw128(wp + i * 32 * PSTR + 128, v[16 * half + 2 * i + 1]); }
        bar();
        lptr pa = lds + (8 * g + q) * PSTR + colA * 2, pb = lds + (8 * g + q) * PSTR + colB * 2;
#pragma unroll
        for (int ks = 0; ks < 8; ++ks) { wa[half * 8 + ks] = tr2(pa + ks * 32 * PSTR, 4 * PSTR); wb[half * 8 + ks] = tr2(pb + ks * 32 * PSTR, 4 * PSTR); asm volatile("" : "+a"(wa[half * 8 + ks]), "+a"(wb[half * 8 + ks])); }
    }
    bar();
}
constexpr int L_A = 0, L_Q = 73728;
#define MFWA(acc, wgt, x) asm volatile("v_mfma_f32_16x16x32_bf16 %0, %1, %2, %0" : "+v"(acc) : "a"(wgt), "v"(x))
#define MFWB(acc, wgt, x) asm volatile("v_mfma_f32_16x16x32_bf16 %0, %2, %1, %0" : "+v"(acc) : "a"(wgt), "v"(x))
template <bool SWAP> DI void tile_mma(lptr lds, int lane, const u32x4 (&wa)[16], const u32x4 (&wb)[16], f32x4& ca, f32x4& cb) {
    ca = (f32x4){0, 0, 0, 0}; cb = ca;
    u32x4 a[16];
#pragma unroll
    for (int ks = 0; ks < 4; ++ks) a[ks] = ldsr128(lds + ks * 1024 + lane * 16);
    asm volatile("s_nop 1" : "+v"(ca), "+v"(cb));
    __builtin_amdgcn_sched_barrier(0);
#pragma unroll
    for (int ks = 0; ks < 16; ++ks) {
        if (SWAP) MFWB(ca, wa[ks], a[ks]); else MFWA(ca, wa[ks], a[ks]);
        if (ks + 4 < 16) a[ks + 4] = ldsr128(lds + (ks + 4) * 1024 + lane * 16);
        __builtin_amdgcn_sched_barrier(0);
        if (SWAP) MFWB(cb, wb[ks], a[ks]); else MFWA(cb, wb[ks], a[ks]);
    }
    asm volatile("s_nop 7" : "+v"(ca), "+v"(cb));
}

constexpr int OSTR = 1040;
constexpr int L_MEMF = 0, L_QT = 8192, L_VT = 16384, L_RED = 20480, L_TS = 21504, L_SEL = 23552, L_ONES = 24576, L_QKV = 25600;
constexpr float SC_L2E = 0.08838834764831845f * 1.4426950408889634f;
constexpr unsigned OOB = 0x80000000u;
#define SMFA(acc, a, w) asm volatile("v_smfmac_f32_16x16x64_bf16 %0, %1, %2, %3" : "+v"(acc) : "v"(a), "a"(w), "v"(sidx))
#define SMFV(acc, a, w) asm volatile("v_smfmac_f32_16x16x64_bf16 %0, %1, %2, %3" : "+v"(acc) : "v"(a), "v"(w), "v"(sidx))
#define MF0(acc, a, b) asm volatile("v_mfma_f32_16x16x32_bf16 %0, %1, %2, 0" : "=&v"(acc) : "v"(a), "v"(b))
#define MF(acc, a, b) asm volatile("v_mfma_f32_16x16x32_bf16 %0, %1, %2, %0" : "+v"(acc) : "v"(a), "v"(b))
#define NOP2() asm volatile("s_nop 1")

#define SB() __builtin_amdgcn_sched_barrier(0)
DI f32x4 zero4() { f32x2 a, b; asm volatile("v_mov_b64 %0, 0\n\tv_mov_b64 %1, 0" : "=v"(a), "=v"(b)); return (f32x4){a.x, a.y, b.x, b.y}; }
DI u32x4 sent4() { u32x2 a, b; asm volatile("v_mov_b64 %0, -1\n\tv_mov_b64 %1, -1" : "=v"(a), "=v"(b)); return (u32x4){a.x, a.y, b.x, b.y}; }
DI unsigned vcvt(float a, float b) { unsigned r; asm volatile("v_cvt_pk_bf16_f32 %0, %1, %2" : "=v"(r) : "v"(a), "v"(b)); return r; }
template <int O0, int O1> DI void dsw2(lptr p, unsigned a, unsigned b) { asm volatile("ds_write2_b32 %0, %1, %2 offset0:%3 offset1:%4" :: "v"(p), "v"(a), "v"(b), "i"(O0), "i"(O1) : "memory"); }

DI int lane_id() { int l; asm volatile("v_mbcnt_lo_u32_b32 %0, -1, 0\n\tv_mbcnt_hi_u32_b32 %0, -1, %0" : "=v"(l)); return l; }
DI void chain_role(const Params& p, lptr lds, const int b, const int tid0) {
    const int w = __builtin_amdgcn_readfirstlane(tid0 >> 6);
    const int h = (b & 3) + 4 * (b >> 3), hf = (b >> 2) & 1;
    u32x8 wq[2][8], wk[2][8], wv[8], wo[8];
    u32x4 kv0[3];
    unsigned quant, pc, pt;
    const Tag tg = get_tag(p.ws);
    unsigned cap = 1u << 17, cfg, ho0, ho1;
    constexpr unsigned ho = WS_HO;
    auto pblk = [&](unsigned ring_, unsigned r) { return (ring_ == 2 ? ho1 : ho0) + (ring_ * 64 + r) * 16384; };
    {
        const int tid = tid0;
        constexpr int WSTR = 272;
        u32x4 v[3][16];
        const unsigned vo = ((tid >> 3) * HD + (tid & 7) * 8) * 2;
        auto issue = [&](int r, u32x4 (&d)[16]) {
            if (r < 5) {
                const rsrc_t rs = mkrs((r < 2 ? p.Wq : r < 4 ? p.Wk : p.Wv) + h * 128 + (r < 4 ? (r & 1) : hf) * 64, (511 * HD + 64) * 2);
#pragma unroll
                for (int i = 0; i < 16; ++i) d[i] = ldw(rs, vo, (r < 4 ? (i ^ (8 * hf)) : i) * 32 * HD * 2);
            } else {
                const rsrc_t rs = mkrs(p.Wo + (size_t)(h * 128 + hf * 64) * D, 65536);
#pragma unroll
                for (int i = 0; i < 16; ++i) d[i] = ldw(rs, lane_id() * 16, w * 1024 + i * 4096);
            } };
        unsigned m0w;
        { const int c = 8 * (b & 1) + ((tid >> 2) & 7); m0w = *(const unsigned*)(p.mem0 + (2 * (c >> 2) + (c & 1)) * D + 16 * (b >> 1) + 4 * (tid & 3) + (c & 2)); }
        issue(0, v[0]); issue(1, v[1]); issue(2, v[2]);
        SB();
        { const unsigned a = rt(); unsigned c, e; do c = rt(); while (c == a); do e = rt(); while (e == c); quant = min(e - c, 8u); pt = e; pc = (unsigned)__builtin_amdgcn_s_memtime(); }
        const int lane = lane_id();
        const lptr wp = lds + (w * 8 + (lane >> 3)) * WSTR + (lane & 7) * 16;
        const int g = lane >> 4, q = (lane & 15) >> 2, pp = lane & 3;
        auto stage = [&](u32x4 (&d)[16], int half) {
#pragma unroll
            for (int i = 0; i < 16; ++i) ldsw128(wp + (i < 8 ? 1 : -1) * hf * 256 * WSTR + i * 32 * WSTR + half * 128, d[i]);
        };
        auto frag = [&](int j, int colbyte) { u32x8 f;
            const lptr base = lds + (((2 * w + j) & 7) * 64 + 8 * g + q) * WSTR + colbyte;
#pragma unroll
            for (int m = 0; m < 4; ++m) { const u32x2 t = trread(base + (32 * (m >> 1) + 4 * (m & 1)) * WSTR); f[2 * m] = t.x; f[2 * m + 1] = t.y; }
            return f; };
        auto take = [&](u32x8 (&dst)[2][8]) {
#pragma unroll
            for (int j = 0; j < 8; ++j) { dst[0][j] = frag(j, w * 64 + 8 * pp); dst[1][j] = frag(j, w * 64 + 32 + 8 * pp); asm volatile("" : "+a"(dst[0][j]), "+a"(dst[1][j])); } };
        stage(v[0], 0); issue(3, v[0]); stage(v[1], 1); issue(4, v[1]); bar(); take(wq); bar();
        {
            const rsrc_t rw = mkrs(p.ws, WS_END);
            cfg = get_cfg(rw, tg, lane, cap);
            ho0 = ho + (cfg & 1) * 8192; ho1 = ho + ((cfg >> 1) & 1) * 8192;
            const u32x4 z = sent4();
#pragma unroll
            for (int r = 0; r < 3; ++r) { st16(rw, lane * 16, z, pblk(r, b) + w * 2048); st16(rw, lane * 16, z, pblk(r, b) + w * 2048 + 1024); }
            const unsigned mso = w < 2 ? (2 * w + (lane >> 5)) * 16384 + (lane & 31) * 4 : b == 0 ? NC * 16384 + (w - 2) * 256 + lane * 4 : OOB;
            st4(rw, mso, w < 2 ? min(m0w, SENT - 1) : 0u, ho0 + HO_MEM * 16384 + b * 128);
            st4(rw, mso, SENT, ho0 + (HO_MEM + MRS) * 16384 + b * 128); st4(rw, mso, SENT, ho0 + (HO_MEM + 2 * MRS) * 16384 + b * 128);
            SB();
        }
        stage(v[2], 0); issue(5, v[2]); stage(v[0], 1);
        asm volatile("s_waitcnt vmcnt(16)" ::: "memory");
        bar();
        stf(mkrs(p.ws, WS_END), lane + w == 0 ? 0u : OOB, 1, flg(FL_ARM + b, cfg), tg);
        take(wk);
        {
            const rsrc_t rw = mkrs(p.ws, WS_END);
            const unsigned fa = flg(FL_ARM + lane, cfg), fq = flg(FL_QKV + ((lane & 2) ? 64 + (h & 3) + 8 * (h >> 2) + 4 * (lane & 1) : 32 + h), cfg);
            for (unsigned spins = 0;; ++spins) {
                asm volatile("" ::: "memory");
                const u32x2 a = ld8(rw, fa, 0), c = ld8(rw, fq, 0);
                if (__all(min(dec(a, tg), dec(c, tg)) >= 1)) break;
                if (spins > cap) { cap = 0; break; }
                __builtin_amdgcn_s_sleep(1);
            }
            asm volatile("" ::: "memory");
            const unsigned kb_ = qkv_off(h, 1, cfg) + w * 2048;
            kv0[0] = ld16(rw, lane * 16, kb_); kv0[1] = ld16(rw, lane * 16, kb_ + 1024); kv0[2] = ld16(rw, lane * 16, qkv_off(h, 2, cfg) + hf * 4096 + w * 1024);
            SB();
        }
        bar();
        constexpr int VSTR = 144, L_WO = 512 * VSTR;
#pragma unroll
        for (int i = 0; i < 16; ++i) { ldsw128(lds + (w * 8 + (lane >> 3) + i * 32) * VSTR + (lane & 7) * 16, v[1][i]); ldsw128(lds + L_WO + (w + 4 * i) * OSTR + lane * 16, v[2][i]); }
        bar();
#pragma unroll
        for (int j = 0; j < 8; ++j) {
            const lptr vb_ = lds + (((2 * w + j) & 7) * 64 + 8 * g + q) * VSTR + (32 * (w >> 1) + 8 * pp + 4 * (w & 1)) * 2;
#pragma unroll
            for (int m = 0; m < 4; ++m) { const u32x2 t = trread(vb_ + (32 * (m >> 1) + 4 * (m & 1)) * VSTR); wv[j][2 * m] = t.x; wv[j][2 * m + 1] = t.y; }
        }
        lptr base = lds + L_WO + (32 * (q >> 1) + 4 * g + (q & 1)) * OSTR;
#pragma unroll
        for (int t = 0; t < 8; ++t) {
            const int col = 128 * w + 64 * (t >> 2) + 16 * pp + 4 * (t & 3);
#pragma unroll
            for (int m = 0; m < 4; ++m) { const u32x2 tv = trread(base + (16 * (m >> 1) + 2 * (m & 1)) * OSTR + col * 2); wo[t][2 * m] = tv.x; wo[t][2 * m + 1] = tv.y; }
        }
        bar();
    }
    const int lane = lane_id(), tid = w * 64 + lane, n = lane & 15, g = lane >> 4;
    const rsrc_t rw = mkrs(p.ws, WS_END);
    const int hi = (n >> 1) & 1, rowi = 2 * (n >> 2) + (n & 1);
    const int sidx = hi ? 0xEEEE : 0x4444;
    const unsigned l16 = lane * 16;
    const unsigned xc = (b & 3) + 4 * ((b >> 2) % (NC / 4));
    const unsigned t0o = tid == 0 ? 0u : OOB;
    auto qkv_issue = [&](int s, u32x4 (&v)[3]) {
        const unsigned kb_ = qkv_off(s * 32 + h, 1, cfg) + w * 2048;
        v[0] = ld16(rw, l16, kb_); v[1] = ld16(rw, l16, kb_ + 1024);
        v[2] = ld16(rw, l16, qkv_off(s * 32 + h, 2, cfg) + hf * 4096 + w * 1024);
    };
    auto qkv_store = [&](int s, u32x4 (&v)[3]) {
        lptr d = lds + L_QKV + (s & 1) * 12288 + lane * 16;
        ldsw128(d + w * 2048, v[0]); ldsw128(d + w * 2048 + 1024, v[1]);
        ldsw64(d + 8192 + (w >> 1) * 2048 + (w & 1) * 8, (u32x2){v[2].x, v[2].y}); ldsw64(d + 8192 + (w >> 1) * 2048 + 1024 + (w & 1) * 8, (u32x2){v[2].z, v[2].w});
    };
    const unsigned qflag = flg(FL_QKV + ((lane & 2) ? 64 + (h & 3) + 8 * (h >> 2) + 4 * (lane & 1) : 32 + h), cfg), kflag = __builtin_amdgcn_readfirstlane(flg(FL_KM + b, cfg)), gflag = __builtin_amdgcn_readfirstlane(flg(FL_G, cfg));
    auto qkv_wait = [&](unsigned need) { for (unsigned spins = 0;; ++spins) { asm volatile("" ::: "memory"); if (__all(ldf(rw, qflag, tg) >= need)) break; if (spins > cap) { cap = 0; break; } __builtin_amdgcn_s_sleep(1); } asm volatile("" ::: "memory"); };
    qkv_store(0, kv0);
    const lptr qtw = lds + L_QT + w * 1024 + (n >> 3) * 256 + (((n & 7) * 32) ^ ((n >> 3) * 128)) + g * 8;
    const lptr tr0 = lds + L_QT + g * 256 + (n >> 2) * 32 + (lane & 3) * 8 + (g & 1) * 128, tr1 = tr0 + ((g & 1) ? -128 : 128);
    auto trf = [&](int off) { const u32x2 a = trread(tr0 + off), c = trread(tr1 + off); return (u32x4){a.x, a.y, c.x, c.y}; };
    const lptr mw = lds + L_MEMF + tid * 16 + w * 1024, tsw = lds + L_TS + tid * 4, vtw = lds + L_VT + tid * 4, vtr = lds + L_VT + lane * 4;
    const lptr rdw = lds + L_RED + (w * 64 + 32 * ((n >> 2) & 1) + 16 * ((n >> 1) & 1) + (n & 1) + 4 * (g & 1)) * 4, rdr = lds + L_RED + (lane & 31) * 8;
    const unsigned tso = w == 3 ? l16 : OOB, sco = w == 3 ? (lane & 1) * 4 : OOB, mso = w < 2 ? (2 * w + (lane >> 5)) * 16384 + (lane & 31) * 4 : b == 0 ? NC * 16384 + (w - 2) * 256 + lane * 4 : OOB,
         kso = (hf | hi) ? OOB : (rowi * 4 + g) * 16, pvo = (2 * (n >> 2) + (g >> 1)) * 16384 + ((n & 3) + 4 * (g & 1)) * 16;
    if (w == 1) ldsw128(lds + L_ONES + lane * 16, (u32x4){0x3f803f80u, 0x3f803f80u, 0x3f803f80u, 0x3f803f80u});
    if (w == 0) { const unsigned one = 0x3f80u << (16 * (n & 1)), k2 = (n >> 1) & 3; ldsw128(lds + L_SEL + lane * 16, (u32x4){k2 == 0 ? one : 0u, k2 == 1 ? one : 0u, k2 == 2 ? one : 0u, k2 == 3 ? one : 0u}); }
    if (tid == 0) *(LAS unsigned*)(lds + L_TS + 1024) = 0;
    const unsigned long long himask = 0xCCCCCCCCCCCCCCCCull;
    bar();
    auto look_at = [&](int sg) { const unsigned o = qkv_off(sg * 32 + h, w == 0 ? 1 : 2, cfg) + (w == 0 ? 0 : hf * 4096); return w >= 2 ? OOB : (w == 1 && lane >= 32) ? qflag : o + l16 * 8; };
    unsigned tvo = look_at(2);
    unsigned T2 = rt(), T1 = 0, ring = 0;
    unsigned X, Y, mg = 1;
    auto pace = [&]() {
        const unsigned c = (unsigned)__builtin_amdgcn_s_memtime(), t = rt();
        const float tpc = (float)(t - pt) * __builtin_amdgcn_rcpf((float)(c - pc));
        int xi = (int)(CX * tpc + 0.5f), yi = (int)(CY * tpc + 0.5f);
        asm volatile("" : "+v"(xi), "+v"(yi));
        X = TX + __builtin_amdgcn_readfirstlane(xi); Y = TY + __builtin_amdgcn_readfirstlane(yi);
        pc = c; pt = t; mg -= mg > 0;
    };
    pace();
    auto step = [&](const int s, auto last_) {
        constexpr bool LAST = decltype(last_)::value;
        f32x4 aq0, aq1, ak0, ak1, av;
        const unsigned rnext = ring == NRING - 1 ? 0 : ring + 1;
        unsigned mo = ho0 + (HO_MEM + ring * MRS + xc) * 16384 + w * 2048, mo1 = mo + 1024, to = ho0 + (HO_MEM + ring * MRS + NC) * 16384 + xc * 128;
        asm volatile("" : "+s"(mo), "+s"(mo1), "+s"(to));
        u32x4 A[8], kx[2][4], qf[4], kf[4], vx[4], ones; unsigned tsv = 0;
        wait_rt(T2, quant);
        A[0] = ld16(rw, l16, mo); A[1] = ld16(rw, l16, mo1); tsv = ld4(rw, sco, to);
        SB();
        SB();
        lptr qb = lds + L_QKV + (s & 1) * 12288 + lane * 16;
        lptr ar0 = lds + L_MEMF + lane * 16 + ((2 * w + 2) & 7) * 1024, ar1 = lds + L_MEMF + lane * 16 + ((2 * w + 4) & 7) * 1024, ar2 = lds + L_MEMF + lane * 16 + ((2 * w + 6) & 7) * 1024;
        asm volatile("" : "+v"(qb), "+v"(ar0), "+v"(ar1), "+v"(ar2));
        if constexpr (!LAST) {
#pragma unroll
            for (int ks = 0; ks < 4; ++ks) { kx[0][ks] = ldsr128(qb + ks * 1024); kx[1][ks] = ldsr128(qb + 4096 + ks * 1024); }
#pragma unroll
            for (int T = 0; T < 2; ++T) vx[T] = ldsr128(qb + 8192 + T * 1024);
        }
        aq0 = zero4(); aq1 = zero4(); ak0 = zero4(); ak1 = zero4(); av = zero4();
        if constexpr (!LAST) {
#pragma unroll
            for (int ks = 0; ks < 4; ++ks) asm volatile("" : "+v"(kx[0][ks]), "+v"(kx[1][ks]));
#pragma unroll
            for (int T = 0; T < 2; ++T) asm volatile("" : "+v"(vx[T]));
        }
        SB();
        ldsw128(mw, A[0]); ldsw128(mw + 1024, A[1]); ldsw32(tsw, tsv);
        SB();
        if (__builtin_expect(!__all(mx4(A[0], mx4(A[1], tsv)) != SENT), 0)) {
            for (unsigned spins = 0;; ++spins) {
                asm volatile("" ::: "memory");
                A[0] = ld16(rw, l16, mo); A[1] = ld16(rw, l16, mo1); tsv = ld4(rw, sco, to);
                if (__all(mx4(A[0], mx4(A[1], tsv)) != SENT)) break;
                if (spins > cap) { cap = 0; break; }
            }
            ldsw128(mw, A[0]); ldsw128(mw + 1024, A[1]); ldsw32(tsw, tsv);
        }
        NOP2();
        if constexpr (!LAST) { SMFA(aq0, A[0], wq[0][0]); SMFA(aq1, A[0], wq[1][0]); }
        SMFA(ak0, A[0], wk[0][0]);
        bar();
        A[2] = ldsr128(ar0); A[3] = ldsr128(ar0 + 1024); SB();
        SMFA(ak1, A[0], wk[1][0]);
        A[4] = ldsr128(ar1); A[5] = ldsr128(ar1 + 1024); SB();
        SMFV(av, A[0], wv[0]);
        A[6] = ldsr128(ar2); A[7] = ldsr128(ar2 + 1024); SB();
        u32x2 tm = {0, 0}, look = {0, 0};
#define SM_(a, w) "v_smfmac_f32_16x16x64_bf16 %0, %" #a ", %" #w ", %15\n\t"
#define POST6(C, acc, W, src, dst, MID, WR) { float t0_, t1_; asm volatile( \
                SM_(3, 9) SM_(4, 10) SM_(5, 11) "v_add_f32 %1, %16, %18\n\t" SM_(6, 12) "v_add_f32 %2, %17, %19\n\t" SM_(7, 13) \
                "v_cvt_pk_bf16_f32 %1, %1, %2\n\t" MID SM_(8, 14) "ds_write2_b32 %20, %1, %1 " WR \
                : "+v"(acc), "=&v"(t0_), "=&v"(t1_) : "v"(A[2]), "v"(A[3]), "v"(A[4]), "v"(A[5]), "v"(A[6]), "v"(A[7]), C(W[2]), C(W[3]), C(W[4]), C(W[5]), C(W[6]), C(W[7]), "v"(sidx), \
                  "v"(src[0]), "v"(src[1]), "v"(src[2]), "v"(src[3]), "v"(dst) : "memory"); }
#define POST3(C, acc, K, W, src, dst, WR) { float t0_, t1_; asm volatile( \
                "v_add_f32 %1, %10, %12\n\tv_smfmac_f32_16x16x64_bf16 %0, %3, %6, %9\n\tv_add_f32 %2, %11, %13\n\tv_smfmac_f32_16x16x64_bf16 %0, %4, %7, %9\n\t" \
                "v_cvt_pk_bf16_f32 %1, %1, %2\n\tv_smfmac_f32_16x16x64_bf16 %0, %5, %8, %9\n\tds_write2_b32 %14, %1, %1 " WR \
                : "+v"(acc), "=&v"(t0_), "=&v"(t1_) : "v"(A[K]), "v"(A[K + 1]), "v"(A[K + 2]), C(W[K]), C(W[K + 1]), C(W[K + 2]), "v"(sidx), \
                  "v"(src[0]), "v"(src[1]), "v"(src[2]), "v"(src[3]), "v"(dst) : "memory"); }
        if constexpr (!LAST) {
            SMFA(aq0, A[1], wq[0][1]);
            SMFA(aq1, A[1], wq[1][1]);
            tm = *(LAS u32x2*)(lds + L_TS + 768); SB();
            SMFA(ak0, A[1], wk[0][1]);
            SMFA(ak1, A[1], wk[1][1]); SMFV(av, A[1], wv[1]);
            SMFA(aq0, A[2], wq[0][2]); SMFA(aq0, A[3], wq[0][3]); SMFA(aq0, A[4], wq[0][4]); SMFA(aq0, A[5], wq[0][5]);
            look = ld8(rw, tvo, 0); SB();
            SMFA(aq0, A[6], wq[0][6]);
            if (__builtin_expect(b == 0, 0)) {
                const u32x2 sv = {s + tg.t, tg.e};
                asm volatile("buffer_store_dwordx2 %0, %1, %2, %3 offen sc1" :: "v"(sv), "v"(t0o), "s"(rw), "s"(gflag) : "memory");
            }
            SMFA(aq0, A[7], wq[0][7]);
            POST6("a", aq1, wq[1], aq0, qtw, "", "offset0:0 offset1:1");
            POST6("a", ak0, wk[0], aq1, qtw, "", "offset0:128 offset1:129");
            POST6("a", ak1, wk[1], ak0, qtw + 4096, "s_waitcnt lgkmcnt(0)\n\ts_barrier\n\t", "offset0:0 offset1:1");
            qf[0] = trf(0); SB();
            SMFV(av, A[2], wv[2]);
            qf[1] = trf(1024); SB();
            SMFV(av, A[3], wv[3]);
            qf[2] = trf(2048); SB();
            SMFV(av, A[4], wv[4]);
            qf[3] = trf(3072); SB();
            POST3("v", av, 5, wv, ak1, qtw + 4096, "offset0:128 offset1:129");
            f32x4 s0, s1, s2, c[8];
            MF0(s1, kx[0][0], qf[0]);
            ones = ldsr128(lds + L_ONES + lane * 16);
            vx[2] = ldsr128(qb + 8192 + 2048);
            SB();
            MF0(s2, kx[1][0], qf[0]);
            vx[3] = ldsr128(qb + 8192 + 3072);
            SB();
            MF(s1, kx[0][1], qf[1]);
            {
                float t0_, t1_;
                asm volatile("v_add_f32 %2, %8, %10\n\tv_add_f32 %3, %9, %11\n\tv_mfma_f32_16x16x32_bf16 %1, %4, %6, %1\n\tv_cvt_pk_bf16_f32 %2, %2, %3\n\t"
                             "v_mfma_f32_16x16x32_bf16 %0, %5, %7, %0\n\ts_waitcnt lgkmcnt(0)\n\ts_barrier\n\tds_write_b32 %12, %2"
                             : "+v"(s1), "+v"(s2), "=&v"(t0_), "=&v"(t1_) : "v"(kx[1][1]), "v"(kx[0][2]), "v"(qf[1]), "v"(qf[2]), "v"(av[0]), "v"(av[1]), "v"(av[2]), "v"(av[3]), "v"(vtw) : "memory");
            }
            kf[0] = trf(4096); SB();
            MF(s2, kx[1][2], qf[2]);
            kf[1] = trf(4096 + 1024); SB();
            MF(s1, kx[0][3], qf[3]);
            kf[2] = trf(4096 + 2048); SB();
            MF(s2, kx[1][3], qf[3]);
            kf[3] = trf(4096 + 3072);
            SB();
            u32x4 pm;
            float mx;
            {
                float t_;
                MF0(s0, kf[0], qf[0]);
                pm = __builtin_bit_cast(u32x4, zero4());
                asm volatile("v_mfma_f32_16x16x32_bf16 %1, %2, %3, %1\n\tv_max3_f32 %0, %4, %5, %6" : "=&v"(mx), "+v"(s0) : "v"(kf[1]), "v"(qf[1]), "v"(s1[0]), "v"(s1[1]), "v"(s1[2]));
                asm volatile("v_mfma_f32_16x16x32_bf16 %2, %3, %4, %2\n\tv_max3_f32 %1, %5, %6, %7\n\tv_max3_f32 %0, %0, %8, %9"
                             : "+v"(mx), "=&v"(t_), "+v"(s0) : "v"(kf[2]), "v"(qf[2]), "v"(s1[3]), "v"(s2[0]), "v"(s2[1]), "v"(s2[2]), "v"(s2[3]));
                asm volatile("v_mfma_f32_16x16x32_bf16 %2, %3, %4, %2\n\tv_max_f32 %0, %0, %1\n\tv_mov_b32 %1, %0" : "+v"(mx), "+v"(t_), "+v"(s0) : "v"(kf[3]), "v"(qf[3]));
                asm volatile("s_barrier" ::: "memory");
                asm volatile("s_nop 0\n\tv_permlane16_swap_b32 %0, %1\n\tv_max_f32 %0, %0, %1\n\t"
                             "v_mov_b32 %1, %0\n\ts_nop 1\n\tv_permlane32_swap_b32 %0, %1\n\tv_max_f32 %0, %0, %1\n\tv_mul_f32 %0, 0xbe0293ee, %0"
                             : "+v"(mx), "+v"(t_) :: "memory");
            }
            u32x4 px;
            float e0, e1, em;
            {
                float a_, b_, c_;
#define FE(d, src) "v_fmamk_f32 " d ", " src ", 0x3e0293ee, %20\n\t"
                asm volatile(FE("%7", "%10") FE("%8", "%11") "v_exp_f32 %7, %7\n\t" FE("%9", "%12") "v_exp_f32 %8, %8\n\t" FE("%1", "%13") "v_exp_f32 %9, %9\n\tv_cvt_pk_bf16_f32 %0, %7, %8\n\t"
                             "v_exp_f32 %1, %1\n\t" FE("%7", "%14") FE("%8", "%15") "v_exp_f32 %7, %7\n\tv_cvt_pk_bf16_f32 %1, %9, %1\n\tv_exp_f32 %8, %8\n\t" FE("%9", "%16") FE("%3", "%17")
                             "v_exp_f32 %9, %9\n\tv_cvt_pk_bf16_f32 %2, %7, %8\n\tv_exp_f32 %3, %3\n\t" FE("%4", "%18") FE("%5", "%19") "v_cvt_pk_bf16_f32 %3, %9, %3\n\tv_max_f32 %6, %4, %5"
                             : "=&v"(px.x), "=&v"(px.y), "=&v"(px.z), "=&v"(px.w), "=&v"(e0), "=&v"(e1), "=&v"(em), "=&v"(a_), "=&v"(b_), "=&v"(c_)
                             : "v"(s1[0]), "v"(s1[1]), "v"(s1[2]), "v"(s1[3]), "v"(s2[0]), "v"(s2[1]), "v"(s2[2]), "v"(s2[3]), "v"(s0[0]), "v"(s0[1]), "v"(mx), "v"(s0));
            }
            const bool slow = __any(em > 0.f);
            SB();
            f32x4 o[4], ls;
            MF0(o[0], vx[0], px);
            kf[0].x = *(LAS unsigned*)vtr; SB(); kf[2].x = *(LAS unsigned*)(vtr + 512); SB();
            MF0(o[2], vx[2], px);
            kf[1].x = *(LAS unsigned*)(vtr + 256); SB(); kf[3].x = *(LAS unsigned*)(vtr + 768); SB();
            float up = 0.f;
            if (__builtin_expect(slow, 0)) {
                up = xmax(vmax(em, 0.f)); e0 -= up; e1 -= up;
            }
            SB();
            MF0(o[1], vx[1], px);
            asm volatile("v_exp_f32 %1, %1\n\tv_mfma_f32_16x16x32_bf16 %0, %2, %3, 0" : "=&v"(o[3]), "+v"(e0) : "v"(vx[3]), "v"(px));
            asm volatile("v_exp_f32 %2, %2\n\tv_mfma_f32_16x16x32_bf16 %0, %4, %5, 0\n\tv_cvt_pk_bf16_f32 %1, %3, %2" : "=&v"(ls), "=&v"(pm.x), "+v"(e1) : "v"(e0), "v"(ones), "v"(px));
            SB();
            if (__builtin_expect(slow, 0)) {
                asm volatile("s_nop 7" : "+v"(o[0]), "+v"(o[1]), "+v"(o[2]), "+v"(o[3]), "+v"(ls));
                const float al = __builtin_amdgcn_exp2f(-up);
                o[0] *= al; o[1] *= al; o[2] *= al; o[3] *= al; ls *= al;
            }
            c[0] = zero4();
            asm volatile("" : "+v"(pm), "+v"(o[0]), "+v"(o[1]), "+v"(o[2]), "+v"(o[3]), "+v"(ls));
            MF(ls, ones, pm);
            c[1] = zero4();
            MF(o[0], kf[0], pm);
            c[2] = zero4();
            MF(o[2], kf[2], pm);
            c[3] = zero4();
            MF(o[1], kf[1], pm);
            c[4] = zero4();
            u32x4 o8;
            {
                float inv; f32x2 il, ih, t0_, t1_;
                asm volatile("v_mfma_f32_16x16x32_bf16 %0, %3, %4, %0\n\tv_rcp_f32 %1, %5\n\ts_nop 0\n\tv_cndmask_b32_e64 %2, 0, %1, %6\n\tv_cndmask_b32_e64 %1, %1, 0, %6"
                             : "+v"(o[3]), "=&v"(il.x), "=&v"(ih.x) : "v"(kf[3]), "v"(pm), "v"(ls[0]), "s"(himask), "v"(ls));
                (void)inv; (void)t0_; (void)t1_;
                c[5] = zero4();
                f32x4 oa = o[0] * il.x + o[2] * ih.x;
                o8.x = pk(oa[0], oa[1]); o8.y = pk(oa[2], oa[3]);
                asm volatile("" : "+v"(o8.x), "+v"(o8.y), "+v"(o[1]), "+v"(o[3]));
                const f32x4 ob = o[1] * il.x + o[3] * ih.x;
                o8.z = pk(ob[0], ob[1]); o8.w = pk(ob[2], ob[3]);
            }
            asm volatile("s_nop 1" : "+v"(o8));
            SB();
            SMFV(c[0], o8, wo[0]); c[6] = zero4(); SMFV(c[1], o8, wo[1]); c[7] = zero4(); SMFV(c[2], o8, wo[2]); SMFV(c[3], o8, wo[3]);
            const unsigned slot = pblk(ring, 16 * w) + b * 128;
            u32x4 pa;
            {
                float t_;
                asm volatile("v_add_f32 %4, %15, %17\n\tv_smfmac_f32_16x16x64_bf16 %0, %9, %10, %14\n\tv_add_f32 %8, %16, %18\n\tv_smfmac_f32_16x16x64_bf16 %1, %9, %11, %14\n\t"
                             "v_cvt_pk_bf16_f32 %4, %4, %8\n\tv_smfmac_f32_16x16x64_bf16 %2, %9, %12, %14\n\tv_add_f32 %5, %19, %21\n\tv_smfmac_f32_16x16x64_bf16 %3, %9, %13, %14\n\t"
                             "v_add_f32 %8, %20, %22\n\tv_cvt_pk_bf16_f32 %5, %5, %8\n\tv_add_f32 %6, %23, %25\n\tv_add_f32 %8, %24, %26\n\tv_cvt_pk_bf16_f32 %6, %6, %8"
                             : "+v"(c[4]), "+v"(c[5]), "+v"(c[6]), "+v"(c[7]), "=&v"(pa.x), "=&v"(pa.y), "=&v"(pa.z), "=&v"(pa.w), "=&v"(t_)
                             : "v"(o8), "v"(wo[4]), "v"(wo[5]), "v"(wo[6]), "v"(wo[7]), "v"(sidx),
                               "v"(c[0][0]), "v"(c[0][1]), "v"(c[0][2]), "v"(c[0][3]), "v"(c[1][0]), "v"(c[1][1]), "v"(c[1][2]), "v"(c[1][3]), "v"(c[2][0]), "v"(c[2][1]), "v"(c[2][2]), "v"(c[2][3]));
            }
            auto fold = [](f32x4 v) { const f32x2 t = (f32x2){v[0], v[1]} + (f32x2){v[2], v[3]}; return pk(t.x, t.y); };
            pa.w = fold(c[3]);
            st16(rw, pvo, pa, slot);
            SB();
            st16(rw, pvo, (u32x4){fold(c[4]), fold(c[5]), fold(c[6]), fold(c[7])}, slot + 131072);
            SB();
            const unsigned now = rt();
            T1 = __builtin_amdgcn_readfirstlane(tm.x); T2 = __builtin_amdgcn_readfirstlane(tm.y);
            { const int dl = s == 0 ? 99 : (int)(now + TV1 - T1); if (dl > 0) T1 = now + TV1 + (dl > TLATE ? TM : 0);
            }
        } else {
            stf(rw, b == 0 ? t0o : OOB, s, gflag, tg);
#pragma unroll
            for (int ks = 1; ks < 8; ++ks) { SMFA(ak0, A[ks], wk[0][ks]); SMFA(ak1, A[ks], wk[1][ks]); SMFV(av, A[ks], wv[ks]); }
            asm volatile("s_nop 7" : "+v"(av), "+v"(ak0), "+v"(ak1));
            unsigned d0 = pk(ak0[0] + ak0[2], ak0[1] + ak0[3]); dsw2<0, 1>(qtw + 4096, d0, d0);
            d0 = pk(ak1[0] + ak1[2], ak1[1] + ak1[3]); dsw2<128, 129>(qtw + 4096, d0, d0);
            ldsw32(vtw, pk(av[0] + av[2], av[1] + av[3]));
            bar();
        }
        {
            const u32x4 kv = trf(4096 + w * 1024);
            const u32x4 vv = {*(LAS unsigned*)vtr, *(LAS unsigned*)(vtr + 256), *(LAS unsigned*)(vtr + 512), *(LAS unsigned*)(vtr + 768)};
            const unsigned kb = kvm_off(ho, cfg, s * 32 + h);
            st16(rw, kso, kv, kb + w * 512); st16(rw, tso, vv, kb + 2048 + hf * 1024);
        }
        if constexpr (LAST) return; else {
        u32x4 p0, p1, nv[3];
        unsigned pb = pblk(ring, b) + w * 2048, pb1 = pb + 1024, mnx = ho0 + (HO_MEM + rnext * MRS) * 16384 + b * 128;
        asm volatile("" : "+s"(pb), "+s"(pb1), "+s"(mnx));
        if (__builtin_expect((unsigned)s + 2 > (unsigned)__builtin_amdgcn_readfirstlane(*(LAS unsigned*)(lds + L_TS + 1024)), 0)) qkv_wait(s + 2);
        qkv_issue(s + 1, nv);
        tvo = look_at(min(s + 3, NSEG - 1));
        const unsigned lk = dec(look, tg), qmin = min(min(__builtin_amdgcn_readlane(lk, 32), __builtin_amdgcn_readlane(lk, 33)), min(__builtin_amdgcn_readlane(lk, 34), __builtin_amdgcn_readlane(lk, 35)));
        if (b == 0 && (s & 3) == 3) pace();
        wait_rt(T1, quant);
        p0 = ld16(rw, l16, pb); p1 = ld16(rw, l16, pb1);
        SB();
        qkv_store(s + 1, nv);
        SB();
        u32x4 sel = ldsr128(lds + L_SEL + lane * 16);
        asm volatile("" : "+v"(sel));
        SB();
        f32x4 rr;
        MF0(rr, sel, p0); MF(rr, sel, p1);
        SB();
        if (__builtin_expect(!__all(mx4(p0, mx4(p1, 0u)) != SENT), 0)) {
            for (unsigned spins = 0;; ++spins) {
                asm volatile("" ::: "memory");
                p0 = ld16(rw, l16, pb); p1 = ld16(rw, l16, pb1);
                if (__all(mx4(p0, mx4(p1, 0u)) != SENT)) break;
                if (spins > cap) { cap = 0; break; }
            }
            MF0(rr, sel, p0); MF(rr, sel, p1);
            asm volatile("s_nop 7" : "+v"(rr));
        }
        {
            asm volatile("s_nop 0" : "+v"(rr));
            unsigned q0, q1, q2, q3;
            asm volatile("v_add_f32_dpp %0, %4, %4 row_ror:8 row_mask:0xf bank_mask:0xf\n\tv_add_f32_dpp %1, %5, %5 row_ror:8 row_mask:0xf bank_mask:0xf\n\t"
                         "v_add_f32_dpp %2, %6, %6 row_ror:8 row_mask:0xf bank_mask:0xf\n\tv_add_f32_dpp %3, %7, %7 row_ror:8 row_mask:0xf bank_mask:0xf"
                         : "=&v"(q0), "=&v"(q1), "=&v"(q2), "=&v"(q3) : "v"(rr[0]), "v"(rr[1]), "v"(rr[2]), "v"(rr[3]));
            dsw2<0, 8>(rdw, q0, q1); dsw2<2, 10>(rdw, q2, q3);
        }
        bar();
        {
            unsigned mv;
            if (__builtin_expect(b == 0 && w >= 2, 0)) {
                const unsigned now = rt() + 2; const int dl = s == 0 ? 99 : (int)(now + TV2 - T2);
                if (dl > TLATE && s > 1) mg = min(mg + MUP, 24u);
                const unsigned n1 = min((dl > 0 ? now + TV2 + (dl > TLATE ? TPEN : 0) : T2) + X + mg, SENT - 1024);
                mv = (lane & 1) ? n1 + Y + (mg >> 1) : n1;
            } else {
                f32x2 a0, a1, a2, a3;
                asm volatile("ds_read_b64 %0, %4\n\tds_read_b64 %1, %4 offset:256\n\tds_read_b64 %2, %4 offset:512\n\tds_read_b64 %3, %4 offset:768\n\ts_waitcnt lgkmcnt(0)"
                             : "=&v"(a0), "=&v"(a1), "=&v"(a2), "=&v"(a3) : "v"(rdr) : "memory");
                const f32x2 t = (a0 + a1) + (a2 + a3);
                mv = pk(t.x, t.y);
            }
            st4(rw, mso, mv, mnx);
            SB();
            const unsigned now = rt(); const int dl = s == 0 ? 99 : (int)(now + TV2 - T2);
            SB();
            {
                const u32x4 z = sent4();
                st16(rw, l16, z, pb);
                st16(rw, l16, z, pb1);
                st4(rw, mso, z.x, ho0 + (HO_MEM + ring * MRS) * 16384 + b * 128);
            }
            if (dl > 0) T2 = now + TV2 + (dl > TLATE ? TM : 0);
        }
        if (__builtin_expect(s == NSEG - 2, 0)) { const u32x2 sv = {s + 1 + tg.t, tg.e}; asm volatile("buffer_store_dwordx2 %0, %1, %2, %3 offen sc1" :: "v"(sv), "v"(tso - 16 * 63), "s"(rw), "s"(kflag) : "memory"); }
        if (w == 1) *(LAS unsigned*)(lds + L_TS + 1024) = qmin;
        ring = rnext;
        }
    };
    for (int s = 0; s < NSEG - 1; ++s) step(s, bool_c<false>{});
    step(NSEG - 1, bool_c<true>{});
    drain(); bar();
    stf(rw, t0o, NSEG, kflag, tg);
}

template <int ROLE>
DI void stream_role(const Params& p, lptr lds, const int j, const int tid) {
    const int w = __builtin_amdgcn_readfirstlane(tid >> 6), lane = tid & 63, n = lane & 15, g = lane >> 4, pp = lane & 3;
    const rsrc_t rw = mkrs(p.ws, WS_END);
    const Tag tg = get_tag(p.ws);
    auto modv = [&](unsigned col) { return __builtin_bit_cast(f32x4, ld16(rw, col * 4, WS_MOD)); };
    unsigned cap = 1u << 17;
    const unsigned l16 = lane * 16, t0o = tid == 64 ? 0u : OOB;
    const int r = ROLE == 0 ? j : ROLE == 1 ? j - 64 : ROLE == 2 ? j - 120 : ROLE == 3 ? j - 152 : ROLE == 5 ? j - 168 : j - 128;
    const int tt = ROLE == 3 ? r >> 3 : ROLE == 1 ? r & 1 : (r >> 3) & 1;
    const int cgx = ROLE == 1 ? r >> 1 : (r & 7) + 8 * (r >> 4);
    constexpr int QD = ROLE == 0 ? 2 : (ROLE == 1 || ROLE == 4) ? 1 : 0;
    const int qm = QD == 2 ? r >> 5 : 2, qh = QD == 2 ? r & 31 : (r & 3) + 4 * (r >> 3), qtt = QD == 2 ? 0 : (r >> 2) & 1;
    const unsigned qline = FL_QKV + (QD == 2 ? r : 64 + r);
    u32x4 pv[32];
    if (QD) {
        unsigned long long pc; unsigned len; int rel;
        asm volatile("s_getpc_b64 %0\n1:\n\ts_mov_b32 %1, _Z5k_endv-_Z6k_main6Params\n\ts_mov_b32 %2, _Z6k_main6Params-1b" : "=s"(pc), "=s"(len), "=s"(rel));
        const unsigned o = (r >> 2) * 8192 + (tid & 63) * 128 + 0 * w;
        if (w == 0 && o + 4 <= len) { const unsigned v = *(const unsigned*)(pc + rel + o); asm volatile("" :: "v"(v)); }
    }
    if (QD) { for (int i = 0; i < 3; ++i) __builtin_amdgcn_s_sleep(16);
        const u16* W = (qm == 0 ? p.Wq : qm == 1 ? p.Wk : p.Wv) + qh * 128; panel_issue(W, W + 64, HD, tid, pv); }
    else {
        const int sg = ROLE == 2 ? r : ROLE == 3 ? 32 + r : 48 + r;
        LAS float* red = (LAS float*)lds;
        if (ROLE == 5) {
            unsigned* ap = (unsigned*)(p.ws + WS_HO + 240 * 16384 + (r & 1) * 8192 + (r >> 1) * 1024 + w * 256);
            unsigned best = 0xffffu;
            for (int rep = 0; rep < 5; ++rep) {
                unsigned long long t0, t1; unsigned q = 0;
                asm volatile("s_memtime %0\n\ts_waitcnt lgkmcnt(0)" : "=s"(t0) :: "memory");
                if (lane == 0) q = __hip_atomic_fetch_add(ap, 1u, __ATOMIC_RELAXED, __HIP_MEMORY_SCOPE_AGENT);
                asm volatile("s_waitcnt vmcnt(0)\n\ts_memtime %0\n\ts_waitcnt lgkmcnt(0)" : "=s"(t1), "+v"(q) :: "memory");
                best = min(best, (unsigned)(t1 - t0));
            }
            ((LAS unsigned*)lds)[w] = best;
            bar();
            const u32x4 b4 = ldsr128(lds);
            stf(rw, t0o, min(min(b4.x, b4.y), min(b4.z, b4.w)), WS_EARLY + (EF_PROBE + r) * 128, tg);
        }
        auto mod_part = [&](auto nw_, int col0, unsigned line, auto&& between) {
            constexpr int NW = decltype(nw_)::value, RP = 64 / NW, NIT = 128 / RP;
            u32x4 wv[NIT]; u16 cv[NIT];
            const int rr = lane / NW, cc = lane % NW;
            const rsrc_t rm = mkrs(p.wmod, 512 * 3072 * 2), rc = mkrs(p.c, 1024);
#pragma unroll
            for (int it = 0; it < NIT; ++it) { wv[it] = ldw(rm, (rr * 3072 + cc * 8) * 2, ((w * 128 + it * RP) * 3072 + col0) * 2); cv[it] = __builtin_amdgcn_raw_buffer_load_b16(rc, rr * 2, (w * 128 + it * RP) * 2, 0); }
            between();
            float acc[8] = {};
#pragma unroll
            for (int it = 0; it < NIT; ++it) {
                const float c_ = bflo(cv[it]), sl = rb(c_ * __builtin_amdgcn_rcpf(1.f + __expf(-c_)));
                acc[0] += sl * bflo(wv[it].x); acc[1] += sl * bfhi(wv[it].x); acc[2] += sl * bflo(wv[it].y); acc[3] += sl * bfhi(wv[it].y);
                acc[4] += sl * bflo(wv[it].z); acc[5] += sl * bfhi(wv[it].z); acc[6] += sl * bflo(wv[it].w); acc[7] += sl * bfhi(wv[it].w);
            }
#pragma unroll
            for (int i = 0; i < 8; ++i) { if (NW == 4) acc[i] += dpp<0x124>(acc[i]); acc[i] += dpp<0x128>(acc[i]); acc[i] = xsum(acc[i]); }
            bar();
            if (lane < NW) {
#pragma unroll
                for (int i = 0; i < 8; ++i) red[w * 64 + cc * 8 + i] = acc[i];
            }
            bar();
            if (w == 1) {
                const unsigned mv_ = __float_as_uint(rb((red[lane] + red[64 + lane]) + (red[128 + lane] + red[192 + lane])));
                if (NW == 4) st8(rw, lane < 32 ? lane * 8 : OOB, (u32x2){mv_, tg.e}, WS_MODG + col0 * 8);
                else { st4(rw, lane * 4, mv_, WS_MOD + col0 * 4); drain(); stf(rw, t0o, 1, WS_EARLY + line * 128, tg); }
            }
        };
        u32x4 xv[8];
        auto x_issue = [&]() {
#pragma unroll
            for (int q = 0; q < 8; ++q) xv[q] = ldw(mkrs(p.x, S * D * 2), l16, (sg * 32 + w * 8 + q) * 1024); };
        if (ROLE != 5 && sg >= 16) mod_part(int_c<4>{}, (sg - 16) * 32, 0, x_issue);
        else x_issue();
        float mean[8], var[8];
#pragma unroll
        for (int q = 0; q < 8; ++q) mean[q] = ((bflo(xv[q].x) + bfhi(xv[q].x)) + (bflo(xv[q].y) + bfhi(xv[q].y))) + ((bflo(xv[q].z) + bfhi(xv[q].z)) + (bflo(xv[q].w) + bfhi(xv[q].w)));
        auto wsum8 = [&](float (&v)[8]) {
#pragma unroll
            for (int q = 0; q < 8; ++q) v[q] += dpp<0xB1>(v[q]);
#pragma unroll
            for (int q = 0; q < 8; ++q) v[q] += dpp<0x4E>(v[q]);
#pragma unroll
            for (int q = 0; q < 8; ++q) v[q] += dpp<0x124>(v[q]);
#pragma unroll
            for (int q = 0; q < 8; ++q) v[q] += dpp<0x128>(v[q]);
#pragma unroll
            for (int q = 0; q < 8; ++q) v[q] = xsum(v[q]) * (1.f / D);
        };
        wsum8(mean);
#pragma unroll
        for (int q = 0; q < 8; ++q) {
            var[q] = 0.f;
#pragma unroll
            for (int i = 0; i < 4; ++i) { const float d0_ = bflo(xv[q][i]) - mean[q], d1_ = bfhi(xv[q][i]) - mean[q]; var[q] += d0_ * d0_ + d1_ * d1_; }
        }
        wsum8(var);
#pragma unroll
        for (int q = 0; q < 8; ++q) {
            const float mb = rb(mean[q]), isd = __builtin_amdgcn_rcpf(rb(__builtin_amdgcn_sqrtf(rb(rb(var[q]) + 1e-5f))));
#pragma unroll
            for (int i = 0; i < 4; ++i) { const unsigned u = vcvt(bflo(xv[q][i]) - mb, bfhi(xv[q][i]) - mb); xv[q][i] = vcvt(bflo(u) * isd, bfhi(u) * isd); }
        }
        float t[8], sh[8];
        for (unsigned spins = 0;; ++spins) {
            asm volatile("" ::: "memory");
            u32x4 gq[8]; unsigned bad = 0;
#pragma unroll
            for (int i = 0; i < 8; ++i) gq[i] = ld16(rw, lane * 64, WS_MODG + (i >> 2) * 4096 + (i & 3) * 16);
#pragma unroll
            for (int i = 0; i < 8; ++i) bad |= (gq[i].y ^ tg.e) | (gq[i].w ^ tg.e);
#pragma unroll
            for (int i = 0; i < 4; ++i) { sh[2 * i] = __uint_as_float(gq[i].x); sh[2 * i + 1] = __uint_as_float(gq[i].z); t[2 * i] = rb(1.f + __uint_as_float(gq[4 + i].x)); t[2 * i + 1] = rb(1.f + __uint_as_float(gq[4 + i].z)); }
            if (__all(bad == 0)) break;
            if (spins > cap) { cap = 0; break; }
            __builtin_amdgcn_s_sleep(4);
        }
#pragma unroll
        for (int q = 0; q < 8; ++q) {
            u32x4 o;
#pragma unroll
            for (int i = 0; i < 4; ++i) { const unsigned u = vcvt(bflo(xv[q][i]) * t[2 * i], bfhi(xv[q][i]) * t[2 * i + 1]); o[i] = pk(bflo(u) + sh[2 * i], bfhi(u) + sh[2 * i + 1]); }
            st16(rw, (lane >> 2) * 1024 + (lane & 3) * 16, o, WS_H1 + (unsigned)(sg * 2 + (w >> 1)) * 16384u + ((w & 1) * 8 + q) * 64);
        }
        drain(); bar();
        stf(rw, t0o, 1, WS_EARLY + (EF_LN + sg) * 128, tg);
        if (ROLE == 2) mod_part(int_c<8>{}, 1024 + r * 64, EF_MOD2 + r, [] {});
    }
    unsigned cfg = 0;
    if (!QD) cfg = get_cfg(rw, tg, lane, cap);
    u32x4 qa[16], qb[16], hv[QD ? 4 * QD : 1];
    const unsigned h1off = WS_H1 + (unsigned)(qtt * 16 + w * 4) * 1024u;
    auto h1_issue = [&](int s) {
#pragma unroll
        for (int i = 0; i < 4 * QD; ++i) hv[i] = ld16(rw, (n * 4 + g) * 16, h1off + (unsigned)s * 32768u + (i >> 2) * 16384 + (i & 3) * 1024); };
    auto qkv_in = [&]() {
#pragma unroll
        for (int i = 0; i < 4 * QD; ++i) ldsw128(lds + L_Q + (i >> 2) * 16384 + (w * 4 + (i & 3)) * 1024 + lane * 16, hv[i]);
    };
    auto qkv_mm = [&](int s) {
#pragma unroll
        for (int t = 0; t < QD; ++t) {
            f32x4 ca, cb;
            tile_mma<QD == 1>(lds + L_Q + t * 16384, lane, qa, qb, ca, cb);
            st16(rw, l16, (u32x4){pk(ca[0], ca[1]), pk(ca[2], ca[3]), pk(cb[0], cb[1]), pk(cb[2], cb[3])}, qkv_off(s * 32 + qh, qm, cfg) + (QD == 2 ? t * 4 + w : w * 2 + qtt) * 1024);
        }
    };
    auto qkv_seg = [&](int s) { qkv_in(); h1_issue(min(s + 1, NSEG - 1)); bar(); qkv_mm(s); };
    auto publish = [&](unsigned v) { drain(); bar(); stf(rw, t0o, v, flg(qline, cfg), tg); };
    u32x4 sa[16], sb[16];
    if (QD) {
        panel_finish(lds, tid, 32 * w + 8 * pp, 32 * w + 8 * pp + 4, pv, qa, qb);
        cfg = get_cfg(rw, tg, lane, cap, EF_LN);
        h1_issue(0);
        for (int s = 0; s < 7; ++s) { qkv_seg(s); publish(s + 1); }
        if (ROLE == 0) { const u16* W = p.Wo + (size_t)(r & 7) * 512 * D + (r >> 4) * 128; panel_issue(W, W + 64, D, tid, pv); }
        if (ROLE == 1) panel_issue(p.wg + cgx * 64, p.wu + cgx * 64, F, tid, pv);
        SB();
        qkv_seg(7); publish(8);
        if (ROLE == 0) panel_finish(lds, tid, 32 * w + 4 * pp, 32 * w + 16 + 4 * pp, pv, sa, sb);
        if (ROLE == 1) panel_finish(lds, tid, 16 * w + 4 * pp, 64 + 16 * w + 4 * pp, pv, sa, sb);
    }
    if (ROLE == 2) {
        const rsrc_t rd = mkrs(p.wd + cgx * 32, (1791 * D + 32) * 2);
#pragma unroll
        for (int half = 0; half < 2; ++half) {
            u32x4 v[14];
#pragma unroll
            for (int i = 0; i < 14; ++i) v[i] = ldw(rd, ((tid >> 2) * D + (tid & 3) * 8) * 2, (half * 896 + i * 64) * D * 2);
            bar();
#pragma unroll
            for (int i = 0; i < 14; ++i) ldsw128(lds + ((tid >> 2) + 64 * i) * 80 + (tid & 3) * 16, v[i]);
            bar();
            if ((w >> 1) == half) {
                lptr pa = lds + ((w & 1) * 448 + 8 * g + (n >> 2)) * 80 + pp * 8;
#pragma unroll
                for (int ks = 0; ks < 14; ++ks) { sa[ks] = tr2(pa + ks * 32 * 80, 4 * 80); sb[ks] = tr2(pa + ks * 32 * 80 + 32, 4 * 80); }
            }
        }
        bar();
    }
    if (QD) { wait_words(rw, tg, WS_EARLY + (EF_LN + lane) * 128, 1, cap); for (int s = 8; s < LEAD; ++s) { qkv_seg(s); publish(s + 1); } }
    unsigned poff; unsigned ahead = 0;
    if (ROLE == 0) poff = FL_A + (r & 7) * 2 + (lane & 1);
    if (ROLE == 1) poff = FL_L + tt * 8 + (lane & 7);
    if (ROLE == 2) poff = FL_C + tt + 2 * (lane < 28 ? lane : 0);
    if (ROLE == 3) poff = FL_B + (lane & 7) + tt * 8 + ((lane >> 3) & 3) * 16;
    if (ROLE == 4) poff = FL_G;
    if (ROLE == 5) {
        const int hh = 2 * r + ((lane >> 1) & 1), k = (lane >> 2) & 3, pr = (hh & 3) + 8 * (hh >> 2) + 4 * (lane & 1);
        poff = k == 0 ? FL_KM + pr : k == 3 ? FL_QKV + 64 + pr : FL_QKV + (k - 1) * 32 + hh;
        ahead = (lane & 12) != 0;
        if (lane == 63) { poff = FL_G; ahead = 0; }
    }
    poff = flg(poff, cfg);
    const unsigned oflag = flg((ROLE == 0 ? FL_B : ROLE == 1 ? FL_C : ROLE == 3 ? FL_L : FL_A) + r, cfg), qflag = flg(qline, cfg);
    unsigned seen = 0;
    auto ready = [&](unsigned nd) {
        if (ROLE != 5) return __all(seen >= nd);
        const unsigned gl = __builtin_amdgcn_readlane(seen, 63);
        return __all(((lane & 12) == 0 ? max(seen, gl) : lane == 63 ? nd : seen) >= nd);
    };
    auto wait_in = [&](unsigned need) {
        if (w == 0) {
            const unsigned nd = min(need + ahead, (unsigned)NSEG);
            for (unsigned spins = 0; !ready(nd); ++spins) {
                if (spins > cap) { cap = 0; break; }
                if (spins) __builtin_amdgcn_s_sleep(1);
                asm volatile("" ::: "memory");
                seen = ldf(rw, poff, tg);
            }
        }
        bar();
    };
    const unsigned pof0 = w == 0 ? poff : OOB;
    const int hh = 2 * r + (w >> 1), att = w & 1;
    u32x4 xq[4], ka[4], kb[4], vb[8];
    const unsigned l16b = att ? l16 : OOB;
    auto a_issue_qk = [&](int s) {
        const unsigned q_ = qkv_off(s * 32 + hh, 0, cfg) + att * 4096, k_ = qkv_off(s * 32 + hh, 1, cfg);
#pragma unroll
        for (int ks = 0; ks < 4; ++ks) { xq[ks] = ld16(rw, l16, q_ + ks * 1024); ka[ks] = ld16(rw, l16, k_ + ks * 1024); kb[ks] = ld16(rw, l16b, k_ + 4096 + ks * 1024); }
    };
    auto a_issue_v = [&](int s) {
        const unsigned v_ = qkv_off(s * 32 + hh, 2, cfg);
#pragma unroll
        for (int i = 0; i < 8; ++i) vb[i] = ld16(rw, (i & 1) ? l16b : l16, v_ + i * 1024);
    };
    if (ROLE == 5) { wait_in(0); a_issue_qk(0); a_issue_v(0); }
    const int d0 = 128 * w + 16 * (lane >> 3) + 4 * (lane & 3);
    f32x4 cga, cgb, cgc;
    if (ROLE == 2 || ROLE == 3) wait_words(rw, tg, WS_EARLY + (EF_MOD2 + (lane & 31)) * 128, 1, cap);
    if (ROLE == 3) { cga = modv(1024 + d0); cgb = modv(1536 + d0); cgc = modv(2048 + d0);
#pragma unroll
        for (int i = 0; i < 4; ++i) cgc[i] = rb(1.f + cgc[i]); }
    if (ROLE == 2) cga = modv(2560 + cgx * 32 + (w & 1) * 16 + 4 * g);
    u32x2 xnext = {0, 0};
    if (ROLE == 3) xnext = *(const u32x2*)(p.x + (size_t)(tt * 16 + 2 * (r & 7) + ((lane >> 2) & 1)) * D + d0);
    if (QD) qkv_in();
    auto iter = [&](const int s, auto doq_) {
        constexpr bool doq = decltype(doq_)::value;
        u32x2 xv = {0, 0};
        wait_in(s + 1);
        u32x4 a[14];
        if (ROLE == 0) {
#pragma unroll
            for (int i = 0; i < 4; ++i) a[i] = ld16(rw, l16, loc(WS_O, (unsigned)((s * 2 + tt) * 128 + (r & 7) * 16 + w * 4) * 1024u, cfg, 1) + i * 1024);
        }
        if (ROLE == 1) {
#pragma unroll
            for (int i = 0; i < 4; ++i) a[i] = ld16(rw, (n * 4 + g) * 16, loc(WS_H2, (unsigned)((s * 2 + tt) * 16 + w * 4) * 1024u, cfg, 0) + i * 1024);
        }
        if (ROLE == 2) {
#pragma unroll
            for (int ks = 0; ks < 14; ++ks) a[ks] = ld16(rw, (g >> 1) * 512 + n * 32 + (g & 1) * 16, loc(WS_G, (unsigned)((s * 2 + tt) * 56 + w * 14 + ks) * 1024u, cfg, 1));
            xv = ld8(rw, (n >> 3) * 16384 + (n & 7) * 1024 + ((w & 1) * 16 + 4 * g) * 2, loc(WS_X1, (s * 2 + tt) * 16384, cfg, 1) + cgx * 64);
        }
        if (ROLE == 3) {
#pragma unroll
            for (int kg = 0; kg < 8; ++kg) a[kg] = ld16(rw, (lane >> 3) * 1024 + (lane & 7) * 16, loc(WS_PB, (unsigned)(((s * 8 + kg) * 2 + tt) * 32 + 8 * w) * 1024u, cfg, 1) + (r & 7) * 128);
            xv = xnext;
            xnext = *(const u32x2*)(p.x + (size_t)(min(s + 1, NSEG - 1) * 32 + tt * 16 + 2 * (r & 7) + ((lane >> 2) & 1)) * D + d0);
        }
        if (ROLE == 5) {
            const unsigned kb_ = kvm_off(WS_HO, cfg, s * 32 + hh);
#pragma unroll
            for (int ks = 0; ks < 4; ++ks) a[ks] = ld16(rw, ((2 * (n >> 2) + (n & 1)) * 4 + g) * 16, kb_ + ks * 512);
            a[4] = ld16(rw, l16, kb_ + 2048); a[5] = ld16(rw, l16, kb_ + 3072);
        }
        const u32x2 sraw = ld8(rw, pof0, 0);
        if (doq) h1_issue(min(s + LEAD + 1, NSEG - 1));
        SB();
        if (doq) qkv_mm(s + LEAD);
        SB();
        if (ROLE == 0 || ROLE == 1) {
#pragma unroll
            for (int i = 0; i < 4; ++i) ldsw128(lds + L_A + (w * 4 + i) * 1024 + lane * 16, a[i]);
            bar();
            f32x4 ca, cb; tile_mma<false>(lds + L_A, lane, sa, sb, ca, cb);
            if (ROLE == 0) {
                const unsigned o = loc(WS_PB, (unsigned)(((s * 8 + (r & 7)) * 2 + tt) * 32 + (r >> 4) * 8 + w * 2) * 1024u, cfg, 1);
                st16(rw, (n * 4 + g) * 16, __builtin_bit_cast(u32x4, ca), o); st16(rw, (n * 4 + g) * 16, __builtin_bit_cast(u32x4, cb), o + 1024);
            } else {
                float q[4];
#pragma unroll
                for (int i = 0; i < 4; ++i) { const float gv = rb(ca[i]); q[i] = rb(gv * __builtin_amdgcn_rcpf(1.f + __expf(-gv))) * rb(cb[i]); }
                st8(rw, n * 32 + g * 8, (u32x2){pk(q[0], q[1]), pk(q[2], q[3])}, loc(WS_G, (unsigned)((s * 2 + tt) * 56 + cgx * 2 + (w >> 1)) * 1024u, cfg, 1) + (w & 1) * 512);
            }
        }
        if (ROLE == 2) {
            f32x4 c0 = {0, 0, 0, 0}, c1 = c0;
            asm volatile("s_nop 1" : "+v"(c0), "+v"(c1));
#pragma unroll
            for (int ks = 0; ks < 14; ++ks) { MFWA(c0, sa[ks], a[ks]); MFWA(c1, sb[ks], a[ks]); }
            asm volatile("s_nop 7" : "+v"(c0), "+v"(c1));
            *(LAS f32x4*)(lds + L_A + (w * 2) * 1024 + lane * 16) = c0; *(LAS f32x4*)(lds + L_A + (w * 2 + 1) * 1024 + lane * 16) = c1;
            bar();
            if (w < 2) {
                lptr q = lds + L_A + w * 1024 + lane * 16;
                const f32x4 y = (*(LAS f32x4*)q + *(LAS f32x4*)(q + 2048)) + (*(LAS f32x4*)(q + 4096) + *(LAS f32x4*)(q + 6144));
                const float o0 = bflo(xv.x) + rb(cga[0] * rb(y[0])), o1 = bfhi(xv.x) + rb(cga[1] * rb(y[1])), o2 = bflo(xv.y) + rb(cga[2] * rb(y[2])), o3 = bfhi(xv.y) + rb(cga[3] * rb(y[3]));
                *(u32x2*)(p.out + (size_t)(s * 32 + tt * 16 + n) * D + cgx * 32 + (w & 1) * 16 + 4 * g) = (u32x2){pk(o0, o1), pk(o2, o3)};
            }
        }
        if (ROLE == 5) {
            f32x4 x0, x1, x2;
            MF0(x1, ka[0], xq[0]); MF0(x2, kb[0], xq[0]);
#pragma unroll
            for (int ks = 1; ks < 4; ++ks) { MF(x1, ka[ks], xq[ks]); MF(x2, kb[ks], xq[ks]); }
            MF0(x0, a[0], xq[0]); MF(x0, a[1], xq[1]); MF(x0, a[2], xq[2]); MF(x0, a[3], xq[3]);
            a_issue_qk(min(s + 1, NSEG - 1));
            asm volatile("s_nop 7" : "+v"(x0), "+v"(x1), "+v"(x2));
            SB();
            const float raw[10] = {x1[0], x1[1], x1[2], x1[3], x2[0], x2[1], x2[2], x2[3], x0[0], x0[1]};
            float sc[10];
            const int qi = 16 * att + n - 4 * g;
#pragma unroll
            for (int i = 0; i < 5; ++i) {
                const unsigned u = vcvt(raw[2 * i], raw[2 * i + 1]);
                const unsigned v = pk(bflo(u) * 0.08838834764831845f, bfhi(u) * 0.08838834764831845f);
                sc[2 * i] = bflo(v); sc[2 * i + 1] = bfhi(v);
            }
#pragma unroll
            for (int i = 0; i < 8; ++i) sc[i] = ((i & 3) + 16 * (i >> 2) <= qi) ? sc[i] : -1e9f;
            float mx = vmax3(vmax3(vmax3(sc[0], sc[1], sc[2]), vmax3(sc[3], sc[4], sc[5]), vmax(sc[6], sc[7])), sc[8], sc[9]);
            mx = xmax(mx) * -1.4426950408889634f;
#pragma unroll
            for (int i = 0; i < 10; ++i) sc[i] = __builtin_amdgcn_exp2f(__builtin_fmaf(sc[i], 1.4426950408889634f, mx));
            const float inv = __builtin_amdgcn_rcpf(xsum((((sc[0] + sc[1]) + (sc[2] + sc[3])) + ((sc[4] + sc[5]) + (sc[6] + sc[7]))) + (sc[8] + sc[9])));
            u32x4 xpx = {pk(sc[0] * inv, sc[1] * inv), pk(sc[2] * inv, sc[3] * inv), pk(sc[4] * inv, sc[5] * inv), pk(sc[6] * inv, sc[7] * inv)};
            u32x4 xpm = {pk(sc[8] * inv, sc[9] * inv), 0u, 0u, 0u};
            f32x4 o[8];
#pragma unroll
            for (int e = 0; e < 4; ++e) {
                u32x4 fa = {vb[2 * e].x, vb[2 * e].y, vb[2 * e + 1].x, vb[2 * e + 1].y}, fb = {vb[2 * e].z, vb[2 * e].w, vb[2 * e + 1].z, vb[2 * e + 1].w};
                u32x4 ma = vb[2 * e], mb = vb[2 * e + 1];
                ma.x = a[4 + (e >> 1)][2 * (e & 1)]; mb.x = a[4 + (e >> 1)][2 * (e & 1) + 1];
                asm volatile("s_nop 1" : "+v"(fa), "+v"(fb), "+v"(ma), "+v"(mb), "+v"(xpx), "+v"(xpm));
                MF0(o[2 * e], fa, xpx); MF0(o[2 * e + 1], fb, xpx); MF(o[2 * e], ma, xpm); MF(o[2 * e + 1], mb, xpm);
            }
            asm volatile("s_nop 7" : "+v"(o[0]), "+v"(o[1]), "+v"(o[2]), "+v"(o[3]), "+v"(o[4]), "+v"(o[5]), "+v"(o[6]), "+v"(o[7]));
#pragma unroll
            for (int e = 0; e < 4; ++e)
                st16(rw, l16, (u32x4){pk(o[2 * e][0], o[2 * e][1]), pk(o[2 * e][2], o[2 * e][3]), pk(o[2 * e + 1][0], o[2 * e + 1][1]), pk(o[2 * e + 1][2], o[2 * e + 1][3])}, loc(WS_O, (unsigned)((s * 2 + att) * 128 + hh * 4) * 1024u, cfg, 1) + e * 1024);
            SB();
            a_issue_v(min(s + 1, NSEG - 1));
        }
        if (ROLE == 3) {
            LAS float* sm = (LAS float*)(lds + L_A);
            const int tk = (lane >> 2) & 1, tok = 2 * (r & 7) + tk;
            f32x4 acc = __builtin_bit_cast(f32x4, a[0]);
#pragma unroll
            for (int kg = 1; kg < 8; ++kg) acc += __builtin_bit_cast(f32x4, a[kg]);
            float x1[4] = {bflo(xv.x), bfhi(xv.x), bflo(xv.y), bfhi(xv.y)};
            float sum = 0.f;
#pragma unroll
            for (int i = 0; i < 4; ++i) { x1[i] = rb(x1[i] + rb(cga[i] * rb(acc[i]))); sum += x1[i]; }
            st8(rw, tk * 1024 + d0 * 2, (u32x2){pk(x1[0], x1[1]), pk(x1[2], x1[3])}, loc(WS_X1, (s * 32 + tt * 16 + 2 * (r & 7)) * 1024, cfg, 1));
            sum += dpp<0xB1>(sum); sum += dpp<0x4E>(sum); sum += dpp<0x128>(sum); sum = xsum(sum);
            const float mw_ = sum * (1.f / 128);
            float var = 0.f;
#pragma unroll
            for (int i = 0; i < 4; ++i) { const float d = x1[i] - mw_; var += d * d; }
            var += dpp<0xB1>(var); var += dpp<0x4E>(var); var += dpp<0x128>(var); var = xsum(var);
            if (lane == 0 || lane == 4) { sm[tk * 8 + w] = mw_; sm[tk * 8 + 4 + w] = var; }
            bar();
            const f32x4 ms = *(LAS f32x4*)(sm + tk * 8), vs = *(LAS f32x4*)(sm + tk * 8 + 4);
            const float mean = ((ms[0] + ms[1]) + (ms[2] + ms[3])) * 0.25f;
            const f32x4 dm = ms - mean;
            var = (((vs[0] + vs[1]) + (vs[2] + vs[3])) + 128.f * ((dm[0] * dm[0] + dm[1] * dm[1]) + (dm[2] * dm[2] + dm[3] * dm[3]))) * (1.f / D);
            const float mb = rb(mean), sd = __builtin_amdgcn_rcpf(rb(__builtin_amdgcn_sqrtf(rb(rb(var) + 1e-5f))));
            float hh2[4];
#pragma unroll
            for (int i = 0; i < 4; ++i) hh2[i] = lnmod(x1[i], mb, sd, cgc[i], cgb[i]);
            st8(rw, ((d0 >> 5) & 3) * 1024 + tok * 64 + (d0 & 31) * 2, (u32x2){pk(hh2[0], hh2[1]), pk(hh2[2], hh2[3])}, loc(WS_H2, (unsigned)((s * 2 + tt) * 16 + 4 * w) * 1024u, cfg, 0));
        }
        if (ROLE == 5) asm volatile("s_waitcnt vmcnt(8)" ::: "memory"); else drain();
        bar();
        if (ROLE != 2 && ROLE != 4) stf(rw, t0o, s + 1, oflag, tg);
        if (doq) stf(rw, t0o, s + LEAD + 1, qflag, tg);
        seen = dec(sraw, tg);
        if (doq) qkv_in();
    };
    int s = 0;
    if (QD) for (; s < NSEG - LEAD; ++s) iter(s, bool_c<QD != 0>{});
    if (ROLE != 4) for (; s < NSEG; ++s) iter(s, bool_c<false>{});
    if (ROLE == 2 && r == 0) st4(rw, t0o, tg.e, WS_EPOCH);
}

__global__ void __launch_bounds__(256, 1) k_main(Params p) {
    extern __shared__ __attribute__((aligned(16))) char smem[];
    const lptr lds = (lptr)smem;
    const int b = blockIdx.x, tid = threadIdx.x;
    if (b < 128 && (b & 7) < 4) chain_role(p, lds, (b >> 3) * 4 + (b & 3), tid);
    else {
        const int u = (b >> 3) * 4 + (b & 3);
        const int j = (b & 4) ? (u < 64 ? u : u + 56) : (u < 120 ? u : u + 64);
        if (j < 64) stream_role<0>(p, lds, j, tid);
        else if (j < 120) stream_role<1>(p, lds, j, tid);
        else if (j < 152) stream_role<2>(p, lds, j, tid);
        else if (j < 168) stream_role<3>(p, lds, j, tid);
        else if (j < 184) stream_role<5>(p, lds, j, tid);
        else stream_role<4>(p, lds, j, tid);
    }
}

__global__ void k_end() {}
extern "C" void kernel_launch(void* const* d_in, const int* in_sizes, int n_in, void* d_out, int out_size, void* d_ws, size_t ws_size, hipStream_t stream) {
    Params p;
    p.x = (const u16*)d_in[0]; p.mem0 = (const u16*)d_in[1]; p.Wq = (const u16*)d_in[2]; p.Wk = (const u16*)d_in[3]; p.Wv = (const u16*)d_in[4]; p.Wo = (const u16*)d_in[5];
    p.wg = (const u16*)d_in[8]; p.wu = (const u16*)d_in[9]; p.wd = (const u16*)d_in[10]; p.c = (const u16*)d_in[11]; p.wmod = (const u16*)d_in[12];
    p.out = (u16*)d_out; p.ws = (char*)d_ws + ((0x200000 - ((uintptr_t)d_ws & 0x1FFFFF)) & 0x1FFFFF);
    constexpr int LDSB = 140 * 1024;
    (void)hipFuncSetAttribute((const void*)k_main, hipFuncAttributeMaxDynamicSharedMemorySize, LDSB);
    k_main<<<256, 256, LDSB, stream>>>(p);
}
```
